```python
import torch

def _softmax(x, axis=-1):
    return torch.softmax(x, dim=axis)
def _gelu(x):
    return torch.nn.functional.gelu(x, approximate="tanh")
def _silu(x):
    return torch.nn.functional.silu(x)
def _sigmoid(x):
    return torch.sigmoid(x)
def _layernorm(x, axis=-1, eps=1e-5):
    m = x.mean(dim=axis, keepdim=True)
    v = x.var(dim=axis, keepdim=True, unbiased=False)
    return (x - m) / torch.sqrt(v + eps)
def _scatter_add(tgt, idx, src, axis=0):
    return tgt.clone().index_add_(axis, idx, src)
def _segment_sum(src, seg, n):
    out = torch.zeros((n,) + src.shape[1:], dtype=src.dtype)
    return out.index_add_(0, seg, src)
def _pad(x, pw):
    flat = tuple(v for pair in reversed(pw) for v in pair)
    return torch.nn.functional.pad(x, flat)

kernel_name = 'parallel_sum__sliding_block__mixer_channel_mlp__prompt'

def setup_inputs():

    _w = (lambda a: (lambda t: t.to(torch.bfloat16) if t.is_floating_point() else t)(torch.as_tensor(a)))
    def _A_setup():
        B, S, D = 1, 2048, 1024
        return {
            "x": _w((lambda *s: torch.randn(*s, dtype=torch.bfloat16) * 0.05)(B, S, D)),
            "wqkv": _w((lambda *s: torch.randn(*s, dtype=torch.bfloat16) * 0.05)(D, 3 * D)),
            "wo": _w((lambda *s: torch.randn(*s, dtype=torch.bfloat16) * 0.05)(D, D)),
        }
    _A_in = _A_setup()

    def _B_setup():
        B, S, D, F = 1, 2048, 1024, 3584
        return {
            "x": _w((lambda *s: torch.randn(*s, dtype=torch.bfloat16) * 0.05)(B, S, D)),
            "w1": _w((lambda *s: torch.randn(*s, dtype=torch.bfloat16) * 3.2)(D, F) * (D ** -0.5)),
            "b1": _w((lambda *s: torch.randn(*s, dtype=torch.bfloat16) * 1.6)(F)),
            "w2": _w((lambda *s: torch.randn(*s, dtype=torch.bfloat16) * 0.4)(F, D) * (F ** -0.5)),
            "b2": _w((lambda *s: torch.randn(*s, dtype=torch.bfloat16) * 0.8)(D)),
        }
    _B_in = _B_setup()

    return {
        "A_x": _A_in["x"],
        "A_wqkv": _A_in["wqkv"],
        "A_wo": _A_in["wo"],
        "B_x": _B_in["x"],
        "B_w1": _B_in["w1"],
        "B_b1": _B_in["b1"],
        "B_w2": _B_in["w2"],
        "B_b2": _B_in["b2"],
    }

def reference(A_x, A_wqkv, A_wo, B_x, B_w1, B_b1, B_w2, B_b2):
    def _A_ref(x, wqkv, wo):
        B, S, D = x.shape
        H = 32; Dh = D // H; W = 256
        qkv = (lambda x, *d: x.permute(*d))((x @ wqkv).reshape(B, S, 3, H, Dh), 2, 0, 3, 1, 4)
        q, k, v = qkv[0], qkv[1], qkv[2]
        scores = torch.einsum("bhqd,bhkd->bhqk", q, k) * (Dh ** -0.5)
        idx = torch.arange(S)
        mask = (idx[:, None] - idx[None, :] >= 0) & (idx[:, None] - idx[None, :] < W)
        scores = torch.where(mask[None, None], scores, -1e9)
        p = _softmax(scores, axis=-1)
        o = torch.einsum("bhqk,bhkd->bhqd", p, v)
        return (lambda x, *d: x.permute(*d))(o, 0, 2, 1, 3).reshape(B, S, D) @ wo

    def _B_ref(x, w1, b1, w2, b2):
        y = _layernorm(x, axis=-1)
        h = _gelu(torch.einsum("bsd,df->bsf", y, w1) + b1)
        return torch.einsum("bsf,fd->bsd", h, w2) + b2

    x = A_x
    xn = _layernorm(x, axis=-1)
    return x + _A_ref(xn, A_wqkv, A_wo) + _B_ref(xn, B_w1, B_b1, B_w2, B_b2)
```

```cpp
#include <hip/hip_runtime.h>
#include <hip/hip_bf16.h>
typedef unsigned short u16;
typedef short bf16x8 __attribute__((ext_vector_type(8)));
typedef float f32x4 __attribute__((ext_vector_type(4)));
#define DI __device__ __forceinline__
DI float bf2f(u16 x){ return __uint_as_float(((unsigned)x)<<16); }
DI u16 f2bf(float f){ unsigned u=__float_as_uint(f); u += 0x7fffu + ((u>>16)&1u); return (u16)(u>>16); }
constexpr int S=2048, D=1024, F=3584, H=32, DH=32, W=256;

__global__ __launch_bounds__(256) void ln_kernel(const u16* x, u16* xn, u16* y){
  int row = blockIdx.x*4 + (threadIdx.x>>6), lane = threadIdx.x&63;
  float v[16];
  const u16* p = x + (size_t)row*D;
  for(int i=0;i<2;i++){ bf16x8 t=*(const bf16x8*)(p+i*512+lane*8); for(int j=0;j<8;j++) v[i*8+j]=bf2f((u16)t[j]); }
  float s=0; for(int i=0;i<16;i++) s+=v[i];
  for(int o=32;o;o>>=1) s+=__shfl_xor(s,o);
  float m=s/D; float q=0; for(int i=0;i<16;i++){ float d=v[i]-m; q+=d*d; }
  for(int o=32;o;o>>=1) q+=__shfl_xor(q,o);
  float r=1.f/sqrtf(q/D+1e-5f);
  for(int i=0;i<16;i++) v[i]=bf2f(f2bf((v[i]-m)*r));
  for(int i=0;i<2;i++){ bf16x8 t; for(int j=0;j<8;j++) t[j]=(short)f2bf(v[i*8+j]); *(bf16x8*)(xn+(size_t)row*D+i*512+lane*8)=t; }
  s=0; for(int i=0;i<16;i++) s+=v[i];
  for(int o=32;o;o>>=1) s+=__shfl_xor(s,o);
  m=s/D; q=0; for(int i=0;i<16;i++){ float d=v[i]-m; q+=d*d; }
  for(int o=32;o;o>>=1) q+=__shfl_xor(q,o);
  r=1.f/sqrtf(q/D+1e-5f);
  for(int i=0;i<2;i++){ bf16x8 t; for(int j=0;j<8;j++) t[j]=(short)f2bf((v[i*8+j]-m)*r); *(bf16x8*)(y+(size_t)row*D+i*512+lane*8)=t; }
}
template<int MODE>
__global__ __launch_bounds__(256) void gemm_kernel(const u16* A, int lda, const u16* B, int N, int K,
    const u16* A2, int lda2, const u16* B2, int K2, const u16* bias, const u16* res, u16* C, int ldc){
  __shared__ u16 As[64][40]; __shared__ u16 Bs[64][40];
  int bm=blockIdx.y*64, bn=blockIdx.x*64;
  int tid=threadIdx.x, lane=tid&63, w=tid>>6, wr=w>>1, wc=w&1, fr=lane&15, fq=lane>>4;
  f32x4 acc[2][2]={};
  for(int part=0; part<(MODE==2?2:1); part++){
    const u16* a = part? A2:A; const u16* b = part? B2:B; int la = part? lda2:lda; int kk = part? K2:K;
    for(int k0=0;k0<kk;k0+=32){
      { int r=tid>>2, c=(tid&3)*8; *(bf16x8*)&As[r][c] = *(const bf16x8*)(a+(size_t)(bm+r)*la+k0+c); }
      { int kr=tid>>3, c=(tid&7)*8; bf16x8 t=*(const bf16x8*)(b+(size_t)(k0+kr)*N+bn+c); for(int j=0;j<8;j++) Bs[c+j][kr]=(u16)t[j]; }
      __syncthreads();
      bf16x8 af[2], bfr[2];
      for(int i=0;i<2;i++){ af[i]=*(bf16x8*)&As[wr*32+i*16+fr][fq*8]; bfr[i]=*(bf16x8*)&Bs[wc*32+i*16+fr][fq*8]; }
      for(int i=0;i<2;i++)for(int j=0;j<2;j++) acc[i][j]=__builtin_amdgcn_mfma_f32_16x16x32_bf16(af[i],bfr[j],acc[i][j],0,0,0);
      __syncthreads();
    }
  }
  for(int i=0;i<2;i++)for(int j=0;j<2;j++)for(int r=0;r<4;r++){
    int row=bm+wr*32+i*16+fq*4+r, col=bn+wc*32+j*16+fr; float v=acc[i][j][r];
    if(MODE==1){ v+=bf2f(bias[col]); float u=0.7978845608f*(v+0.044715f*v*v*v); v = v/(1.f+__expf(-2.f*u)); }
    if(MODE==2){ v+=bf2f(bias[col])+bf2f(res[(size_t)row*ldc+col]); }
    C[(size_t)row*ldc+col]=f2bf(v);
  }
}
__global__ __launch_bounds__(256) void attn_kernel(const u16* qkv, u16* o){
  int gw = blockIdx.x*4 + (threadIdx.x>>6), lane=threadIdx.x&63;
  int q = gw>>5, h = gw&31;
  float qv[32];
  for(int i=0;i<4;i++){ bf16x8 t=*(const bf16x8*)(qkv+(size_t)q*3*D+h*DH+i*8); for(int j=0;j<8;j++) qv[i*8+j]=bf2f((u16)t[j]); }
  float s[4]; float mx=-1e30f;
  for(int i=0;i<4;i++){ int k=q-(i*64+lane); float a=-1e30f;
    if(k>=0){ a=0; for(int c=0;c<4;c++){ bf16x8 t=*(const bf16x8*)(qkv+(size_t)k*3*D+D+h*DH+c*8); for(int j=0;j<8;j++) a+=qv[c*8+j]*bf2f((u16)t[j]); } a*=0.17677669529f; }
    s[i]=a; mx=fmaxf(mx,a);}
  for(int of=32;of;of>>=1) mx=fmaxf(mx,__shfl_xor(mx,of));
  float l=0; float acc[32]; for(int j=0;j<32;j++) acc[j]=0;
  for(int i=0;i<4;i++){ int k=q-(i*64+lane); if(k>=0){ float p=__expf(s[i]-mx); l+=p;
      for(int c=0;c<4;c++){ bf16x8 t=*(const bf16x8*)(qkv+(size_t)k*3*D+2*D+h*DH+c*8); for(int j=0;j<8;j++) acc[c*8+j]+=p*bf2f((u16)t[j]); } } }
  for(int of=32;of;of>>=1) l+=__shfl_xor(l,of);
  float outv=0;
  for(int j=0;j<32;j++){ float a=acc[j]; for(int of=32;of;of>>=1) a+=__shfl_xor(a,of); if(lane==j) outv=a; }
  if(lane<32) o[(size_t)q*D+h*DH+lane]=f2bf(outv/l);
}
extern "C" void kernel_launch(void* const* d_in, const int* in_sizes, int n_in, void* d_out, int out_size, void* d_ws, size_t ws_size, hipStream_t stream){
  const u16* x=(const u16*)d_in[0]; const u16* wqkv=(const u16*)d_in[1]; const u16* wo=(const u16*)d_in[2];
  const u16* w1=(const u16*)d_in[4]; const u16* b1=(const u16*)d_in[5]; const u16* w2=(const u16*)d_in[6]; const u16* b2=(const u16*)d_in[7];
  u16* ws=(u16*)d_ws; u16* xn=ws; u16* y=xn+S*D; u16* qkv=y+S*D; u16* o=qkv+S*3*D; u16* h=o+S*D;
  ln_kernel<<<S/4,256,0,stream>>>(x,xn,y);
  gemm_kernel<0><<<dim3(3*D/64,S/64),256,0,stream>>>(xn,D,wqkv,3*D,D,nullptr,0,nullptr,0,nullptr,nullptr,qkv,3*D);
  gemm_kernel<1><<<dim3(F/64,S/64),256,0,stream>>>(y,D,w1,F,D,nullptr,0,nullptr,0,b1,nullptr,h,F);
  attn_kernel<<<S*H/4,256,0,stream>>>(qkv,o);
  gemm_kernel<2><<<dim3(D/64,S/64),256,0,stream>>>(o,D,wo,D,D,h,F,w2,F,b2,x,(u16*)d_out,D);
}
```

```cpp
#include <hip/hip_runtime.h>
typedef unsigned short u16;
__global__ __launch_bounds__(256,1) __attribute__((aligned(4096))) void fused_kernel(const u16* x_, u16* out_, const u16* b1_, const u16* b2_, const u16* wqkv_, const u16* w1_, const u16* w2_, const u16* wo_, char* ws_){
  auto ka=__builtin_amdgcn_kernarg_segment_ptr(); unsigned b=blockIdx.x, t=threadIdx.x;
  asm volatile(
    "s_load_dwordx16 s[68:83], s[0:1], 0\n"
    "s_load_dwordx2 s[44:45], s[0:1], 64\n"
    "v_readfirstlane_b32 s7, v0\n"
    "v_and_b32 v192, 63, v0\n"
    "s_lshr_b32 s3, s6, 3\n"
    "s_lshr_b32 s7, s7, 6\n"
    "v_min_u32 v27, 4, v192\n"
    "s_lshl2_add_u32 s3, s3, s7\n"
    "s_mul_i32 s3, s3, 640\n"
    "s_getpc_b64 s[0:1]\n"
    ".Lk2_start:\n"
    "v_lshl_add_u32 v27, v27, 7, s3\n"
    "v_min_u32 v27, .Lk2_fin-.Lk2_start-4, v27\n"
    "global_load_dword a220, v27, s[0:1]\n"
    "s_bitcmp1_b32 s6, 6\n"
    "s_cbranch_scc1 .Lk2_w1\n"
    "s_bfe_u32 s57, s6, 0x30003\n"
    "s_and_b32 s0, s6, 7\n"
    "s_lshr_b32 s1, s6, 7\n"
    "s_lshl1_add_u32 s8, s0, s1\n"
    "s_lshl_b32 s9, s8, 6\n"
    "s_and_b32 s23, s7, 1\n"
    "v_lshrrev_b32 v197, 3, v192\n"
    "v_and_b32 v198, 7, v192\n"
    "v_lshlrev_b32 v25, 4, v192\n"
    "v_mul_u32_u24 v199, 0x1800, v197\n"
    "s_mul_i32 s1, s57, 0x18000\n"
    "s_lshl1_add_u32 s1, s9, s1\n"
    "s_lshl_b32 s0, s7, 11\n"
    "s_add_u32 s0, s1, s0\n"
    "s_add_u32 s0, s0, 0x800\n"
    "s_cmp_lt_u32 s7, 3\n"
    "s_cselect_b32 s0, s0, 0x40000000\n"
    "v_add_u32 v200, s0, v199\n"
    "v_lshl_add_u32 v24, v198, 4, v200\n"
    "v_lshrrev_b32 v193, 4, v192\n"
    "v_and_b32 v194, 15, v192\n"
    "s_lshl_b32 s0, s7, 2\n"
    "s_lshl4_add_u32 s0, s8, s0\n"
    "s_lshl_b32 s0, s0, 11\n"
    "v_lshl_add_u32 v200, v193, 11, s0\n"
    "v_lshl_add_u32 v26, v194, 4, v200\n"
    "s_mul_i32 s0, s7, 0xc000\n"
    "s_add_u32 s0, 0x800, s0\n"
    "s_lshl1_add_u32 s0, s9, s0\n"
    "v_add_u32 v200, s0, v199\n"
    "s_lshl_b32 s0, s23, 2\n"
    "v_and_b32 v201, 2, v197\n"
    "v_xor_b32 v201, v198, v201\n"
    "v_xor_b32 v201, s0, v201\n"
    "v_lshl_add_u32 v20, v201, 4, v200\n"
    "v_add_u32 v21, 0x400, v20\n"
    "v_add_u32 v22, 0x800, v20\n"
    "s_mul_i32 s0, s7, 0xc00\n"
    "s_add_u32 s53, s0, 0x12000\n"
    "s_lshl_b32 s36, s57, 19\n"
    "s_lshl_b32 s52, s7, 13\n"
    "s_lshl_b32 s0, s7, 17\n"
    "v_lshl_add_u32 v200, v197, 11, s0\n"
    "v_xor_b32 v201, v198, v193\n"
    "v_lshl_add_u32 v16, v201, 4, v200\n"
    "v_xor_b32 v200, 64, v16\n"
    "v_add_u32 v17, 0x3c00, v200\n"
    "v_add_u32 v18, 0x7800, v16\n"
    "v_add_u32 v19, 0x7800, v17\n"
    "v_and_b32 v196, 3, v192\n"
    "s_mov_b32 s55, 196608\n"
    "s_mov_b32 s42, 6293504\n"
    "s_mov_b32 s43, 0x20000\n"
    "s_mov_b32 s38, 0x80000\n"
    "s_mov_b32 s39, 0x20000\n"
    "s_mov_b32 s86, 0\n"
    "s_mov_b32 m0, s53\n"
    "s_waitcnt lgkmcnt(0)\n"
    "s_sub_u32 s40, s76, 2048\n"
    "s_subb_u32 s41, s77, 0\n"
    "s_and_b32 s41, s41, 0xffff\n"
    "s_add_u32 s36, s68, s36\n"
    "s_addc_u32 s37, s69, 0\n"
    "s_and_b32 s37, s37, 0xffff\n"
    "s_mov_b32 s84, 0\n"
    "buffer_load_dwordx4 v[240:243], v26, s[36:39], 0 offen\n"
    "s_mov_b32 s92, 0\n"
    "buffer_load_dwordx4 a[136:139], v24, s[40:43], s92 offen\n"
    "s_mov_b32 s92, 49152\n"
    "buffer_load_dwordx4 a[140:143], v24, s[40:43], s92 offen\n"
    "buffer_load_dwordx4 v[244:247], v26, s[36:39], 0 offen offset:256\n"
    "s_mov_b32 s92, 786432\n"
    "buffer_load_dwordx4 a[144:147], v24, s[40:43], s92 offen\n"
    "s_mov_b32 s92, 835584\n"
    "buffer_load_dwordx4 a[148:151], v24, s[40:43], s92 offen\n"
    "buffer_load_dwordx4 v20, s[40:43], s86 offen lds\n"
    "buffer_load_dwordx4 v21, s[40:43], s86 offen offset:1024 lds\n"
    "buffer_load_dwordx4 v22, s[40:43], s86 offen offset:2048 lds\n"
    "s_add_u32 s86, s86, s55\n"
    "v_lshlrev_b32 v1, 20, v197\n"
    "buffer_load_dword a220, v1, s[40:43], 0 offen offset:2048\n"
    "s_branch .Lk2_qkv\n"
    ".Lk2_hret:\n"
    "s_setpc_b64 s[0:1]\n"
    ".Lk2_w1:\n"
    "s_bfe_u32 s57, s6, 0x30003\n"
    "s_and_b32 s0, s6, 7\n"
    "s_lshr_b32 s1, s6, 7\n"
    "s_lshl1_add_u32 s8, s0, s1\n"
    "s_mul_i32 s1, s8, 0xe0\n"
    "s_and_b32 s0, s8, 1\n"
    "s_lshl_b32 s0, s0, 5\n"
    "s_add_u32 s9, s1, s0\n"
    "s_mul_i32 s0, s8, 0xe0\n"
    "s_and_b32 s1, s8, 1\n"
    "s_xor_b32 s1, s1, 1\n"
    "s_mul_i32 s1, s1, 0xc0\n"
    "s_add_u32 s16, s0, s1\n"
    "s_and_b32 s23, s7, 1\n"
    "v_lshrrev_b32 v197, 3, v192\n"
    "v_and_b32 v198, 7, v192\n"
    "v_lshlrev_b32 v25, 4, v192\n"
    "v_mul_u32_u24 v199, 0x1c00, v197\n"
    "s_mul_i32 s1, s57, 0x1c000\n"
    "s_add_u32 s1, 0x800, s1\n"
    "s_lshl_b32 s0, s7, 7\n"
    "s_lshl1_add_u32 s0, s9, s0\n"
    "s_lshl_b32 s2, s16, 1\n"
    "s_cmp_lt_u32 s7, 3\n"
    "s_cselect_b32 s2, s0, s2\n"
    "s_add_u32 s2, s1, s2\n"
    "v_add_u32 v200, s2, v199\n"
    "v_lshl_add_u32 v24, v198, 4, v200\n"
    "s_cmp_lt_u32 s7, 3\n"
    "s_cselect_b32 s2, 8, 4\n"
    "v_cmp_gt_u32 vcc, s2, v198\n"
    "v_mov_b32 v200, 0x40000000\n"
    "s_nop 0\n"
    "v_cndmask_b32 v24, v200, v24, vcc\n"
    "v_lshrrev_b32 v193, 4, v192\n"
    "v_and_b32 v194, 15, v192\n"
    "s_mul_i32 s2, s7, 0xe000\n"
    "s_add_u32 s2, 0x800, s2\n"
    "s_lshl1_add_u32 s2, s9, s2\n"
    "v_add_u32 v200, s2, v199\n"
    "s_lshl_b32 s2, s23, 2\n"
    "v_and_b32 v201, 2, v197\n"
    "v_xor_b32 v201, v198, v201\n"
    "v_xor_b32 v201, s2, v201\n"
    "v_lshl_add_u32 v20, v201, 4, v200\n"
    "v_add_u32 v21, 0xfffffc80, v20\n"
    "v_add_u32 v22, 0xfffff900, v20\n"
    "s_mul_i32 s2, s7, 0xc00\n"
    "s_add_u32 s53, s2, 0x12000\n"
    "s_lshl_b32 s36, s57, 19\n"
    "s_lshl_b32 s52, s7, 13\n"
    "s_lshl_b32 s2, s7, 17\n"
    "v_lshl_add_u32 v200, v197, 11, s2\n"
    "v_xor_b32 v201, v198, v193\n"
    "v_lshl_add_u32 v16, v201, 4, v200\n"
    "v_xor_b32 v200, 64, v16\n"
    "v_add_u32 v17, 0x3c00, v200\n"
    "v_add_u32 v18, 0x7800, v16\n"
    "v_add_u32 v19, 0x7800, v17\n"
    "v_and_b32 v196, 3, v192\n"
    "s_lshl_b32 s2, s7, 10\n"
    "s_add_u32 s54, s2, 0x8000\n"
    "s_mul_i32 s2, s7, 0xe000\n"
    "s_add_u32 s2, 0x800, s2\n"
    "s_lshl1_add_u32 s2, s16, s2\n"
    "v_bfe_u32 v200, v192, 2, 3\n"
    "v_and_b32 v201, 32, v192\n"
    "v_add_u32 v200, v200, v201\n"
    "v_mul_u32_u24 v200, 0x1c00, v200\n"
    "v_add_u32 v200, s2, v200\n"
    "s_lshl_b32 s2, s23, 1\n"
    "v_xor_b32 v201, s2, v196\n"
    "v_lshl_add_u32 v23, v201, 4, v200\n"
    "s_mov_b32 s55, 229376\n"
    "s_mov_b32 s42, 7342080\n"
    "s_mov_b32 s43, 0x20000\n"
    "s_mov_b32 s38, 0x80000\n"
    "s_mov_b32 s39, 0x20000\n"
    "s_mov_b32 s86, 0\n"
    "s_mov_b32 m0, s53\n"
    "s_waitcnt lgkmcnt(0)\n"
    "s_sub_u32 s40, s78, 2048\n"
    "s_subb_u32 s41, s79, 0\n"
    "s_and_b32 s41, s41, 0xffff\n"
    "s_add_u32 s36, s68, s36\n"
    "s_addc_u32 s37, s69, 0\n"
    "s_and_b32 s37, s37, 0xffff\n"
    "s_mov_b32 s84, 0\n"
    "buffer_load_dword a220, v16, s[36:39], 0 offen\n"
    "s_mov_b32 s92, 0\n"
    "buffer_load_dwordx4 a[136:139], v24, s[40:43], s92 offen\n"
    "s_mov_b32 s92, 57344\n"
    "buffer_load_dwordx4 a[140:143], v24, s[40:43], s92 offen\n"
    "s_mov_b32 s92, 917504\n"
    "buffer_load_dwordx4 a[144:147], v24, s[40:43], s92 offen\n"
    "s_mov_b32 s92, 974848\n"
    "buffer_load_dwordx4 a[148:151], v24, s[40:43], s92 offen\n"
    "buffer_load_dwordx4 v20, s[40:43], s86 offen lds\n"
    "buffer_load_dwordx4 v21, s[40:43], s86 offen offset:1024 lds\n"
    "buffer_load_dwordx4 v22, s[40:43], s86 offen offset:2048 lds\n"
    "s_add_u32 s86, s86, s55\n"
    "v_lshlrev_b32 v1, 20, v197\n"
    "buffer_load_dword a220, v1, s[40:43], 0 offen offset:2048\n"
    "v_accvgpr_write_b32 a222, v27\n"
    "s_lshr_b32 s22, s7, 1\n"
    "v_lshrrev_b32 v195, 2, v194\n"
    "s_lshl_b32 s2, s22, 11\n"
    "s_lshl_b32 s1, s23, 14\n"
    "s_add_u32 s1, s2, s1\n"
    "v_lshl_add_u32 v200, v194, 7, s1\n"
    "v_lshrrev_b32 v201, 1, v194\n"
    "v_xor_b32 v201, v193, v201\n"
    "v_lshl_add_u32 v8, v201, 4, v200\n"
    "v_xor_b32 v9, 0x4000, v8\n"
    "v_xor_b32 v10, 64, v8\n"
    "v_xor_b32 v11, 0x4040, v8\n"
    "v_and_b32 v208, 1, v193\n"
    "v_and_b32 v209, 1, v196\n"
    "v_lshrrev_b32 v210, 1, v196\n"
    "v_mul_u32_u24 v200, 0xc00, v193\n"
    "v_add_u32 v200, 0x12000, v200\n"
    "v_lshl_add_u32 v200, v195, 7, v200\n"
    "s_lshl_b32 s1, s23, 2\n"
    "v_and_b32 v201, 2, v195\n"
    "v_xor_b32 v201, v210, v201\n"
    "v_lshlrev_b32 v202, 2, v208\n"
    "v_xor_b32 v202, v201, v202\n"
    "v_xor_b32 v202, s1, v202\n"
    "v_lshl_add_u32 v200, v202, 4, v200\n"
    "v_lshl_add_u32 v14, v209, 3, v200\n"
    "v_xor_b32 v15, 32, v14\n"
    "s_mov_b32 s1, 0x8000\n"
    "v_lshl_add_u32 v200, v193, 10, s1\n"
    "v_lshl_add_u32 v200, v195, 6, v200\n"
    "v_lshlrev_b32 v202, 1, v208\n"
    "v_xor_b32 v202, v210, v202\n"
    "v_lshl_add_u32 v200, v202, 4, v200\n"
    "v_lshl_add_u32 v12, v209, 3, v200\n"
    "v_xor_b32 v13, 32, v12\n"
    "v_mov_b32 v200, 0x3f80\n"
    "v_lshlrev_b32 v202, 4, v209\n"
    "v_lshlrev_b32 v211, v202, v200\n"
    "v_lshrrev_b32 v212, 1, v198\n"
    "v_cmp_eq_u32 vcc, 0, v212\n"
    "s_nop 1\n"
    "v_cndmask_b32 v202, 0, v211, vcc\n"
    "v_accvgpr_write_b32 a132, v202\n"
    "v_cmp_eq_u32 vcc, 1, v212\n"
    "s_nop 1\n"
    "v_cndmask_b32 v202, 0, v211, vcc\n"
    "v_accvgpr_write_b32 a133, v202\n"
    "v_cmp_eq_u32 vcc, 2, v212\n"
    "s_nop 1\n"
    "v_cndmask_b32 v202, 0, v211, vcc\n"
    "v_accvgpr_write_b32 a134, v202\n"
    "v_cmp_eq_u32 vcc, 3, v212\n"
    "s_nop 1\n"
    "v_cndmask_b32 v202, 0, v211, vcc\n"
    "v_accvgpr_write_b32 a135, v202\n"
    "s_and_b32 s17, s6, 1\n"
    "s_and_b32 s1, s6, 2\n"
    "s_lshl2_add_u32 s18, s1, s57\n"
    "s_and_b32 s1, s6, 4\n"
    "s_lshr_b32 s2, s6, 6\n"
    "s_add_u32 s19, s1, s2\n"
    "s_lshr_b32 s2, s6, 3\n"
    "s_and_b32 s1, s6, 2\n"
    "s_lshl_b32 s1, s1, 6\n"
    "s_lshl2_add_u32 s1, s2, s1\n"
    "s_add_u32 s99, s1, s7\n"
    "s_mul_i32 s1, s99, 0x3800\n"
    "s_add_u32 s1, 0x400, s1\n"
    "s_and_b32 s2, s6, 4\n"
    "s_lshl_b32 s2, s2, 8\n"
    "s_add_u32 s2, s1, s2\n"
    "v_lshrrev_b32 v202, 5, v192\n"
    "v_lshl_add_u32 v202, v202, 11, s2\n"
    "v_and_b32 v200, 31, v192\n"
    "v_lshl_add_u32 v28, v200, 5, v202\n"
    "s_lshl2_add_u32 s35, s6, s7\n"
    "s_and_b32 s45, s45, 0xffff\n"
    "s_mov_b32 s46, 41943040\n"
    "s_mov_b32 s47, 0x20000\n"
    "s_mul_i32 s0, s17, 3670016\n"
    "s_add_u32 s48, s80, s0\n"
    "s_addc_u32 s49, s81, 0\n"
    "s_sub_u32 s48, s48, 1024\n"
    "s_subb_u32 s49, s49, 0\n"
    "s_and_b32 s49, s49, 0xffff\n"
    "s_mov_b32 s50, 3671040\n"
    "s_mov_b32 s51, 0x20000\n"
    "s_sub_u32 s58, s82, 1024\n"
    "s_subb_u32 s59, s83, 0\n"
    "s_and_b32 s59, s59, 0xffff\n"
    "s_cmp_eq_u32 s23, 0\n"
    "s_cselect_b64 s[96:97], s[80:81], s[82:83]\n"
    "s_cselect_b64 s[28:29], s[70:71], s[68:69]\n"
    "s_cselect_b64 s[0:1], s[72:73], s[74:75]\n"
    "s_cmp_lt_u32 s7, 2\n"
    "s_cselect_b64 s[96:97], s[96:97], s[44:45]\n"
    "s_cselect_b64 s[28:29], s[28:29], s[0:1]\n"
    "s_lshl1_add_u32 s2, s57, s23\n"
    "s_mul_i32 s2, s2, 0x120000\n"
    "s_lshl_b32 s1, s22, 12\n"
    "s_add_u32 s60, s2, s1\n"
    "s_xor_b32 s1, s23, 1\n"
    "s_lshl1_add_u32 s1, s57, s1\n"
    "s_mul_i32 s1, s1, 0x120000\n"
    "s_lshl_b32 s2, s22, 12\n"
    "s_add_u32 s61, s1, s2\n"
    "s_lshr_b32 s2, s8, 3\n"
    "s_lshl_b32 s65, s2, 17\n"
    "s_lshr_b32 s2, s9, 5\n"
    "s_add_u32 s2, s2, s23\n"
    "s_lshl_b32 s2, s2, 13\n"
    "s_add_u32 s62, s2, s65\n"
    "s_add_u32 s63, s62, 0x4000\n"
    "s_add_u32 s64, s62, 0x8000\n"
    "s_lshr_b32 s2, s16, 5\n"
    "s_lshl_b32 s2, s2, 13\n"
    "s_add_u32 s65, s2, s65\n"
    "s_lshl_b32 s2, s57, 12\n"
    "s_lshl_b32 s1, s8, 7\n"
    "s_add_u32 s1, s2, s1\n"
    "s_lshl2_add_u32 s1, s7, s1\n"
    "s_add_u32 s66, s1, 0x20a0000\n"
    "s_xor_b32 s83, s66, 0x100\n"
    "s_mul_i32 s1, s18, 0x120000\n"
    "s_lshl_b32 s2, s23, 11\n"
    "s_add_u32 s67, s1, s2\n"
    "s_lshl_b32 s2, s8, 14\n"
    "s_lshl_b32 s1, s7, 9\n"
    "s_add_u32 s1, s2, s1\n"
    "s_add_u32 s77, s1, 0x2060000\n"
    "s_lshl_b32 s1, s57, 11\n"
    "s_add_u32 s76, s77, s1\n"
    "s_lshl_b32 s1, s57, 12\n"
    "s_lshl_b32 s2, s7, 10\n"
    "s_add_u32 s2, s1, s2\n"
    "s_add_u32 s79, s2, 0x2010000\n"
    "s_lshl_b32 s2, s35, 12\n"
    "s_add_u32 s81, s2, 0x1c00000\n"
    "s_xor_b32 s80, s81, 0x4000\n"
    "s_lshr_b32 s2, s18, 1\n"
    "s_lshl_b32 s2, s2, 12\n"
    "s_lshl_b32 s1, s17, 10\n"
    "s_add_u32 s1, s2, s1\n"
    "s_add_u32 s82, s1, 0x20a0000\n"
    "s_cmp_eq_u32 s7, 0\n"
    "s_cselect_b32 s1, 0x380000, 0\n"
    "s_mul_i32 s1, s17, s1\n"
    "s_mul_i32 s2, s57, 0x240000\n"
    "s_cmp_eq_u32 s7, 2\n"
    "s_cselect_b32 s2, s2, s67\n"
    "s_cmp_lt_u32 s7, 2\n"
    "s_cselect_b32 s93, s1, s2\n"
    "s_mov_b32 s2, 0x37fffc\n"
    "s_cmp_eq_u32 s7, 0\n"
    "s_cselect_b32 s2, s2, 0x1ffffc\n"
    "s_mov_b32 s1, 0x23fffc\n"
    "s_cmp_eq_u32 s7, 2\n"
    "s_cselect_b32 s1, s1, 0x11f7fc\n"
    "s_cmp_lt_u32 s7, 2\n"
    "s_cselect_b32 s94, s2, s1\n"
    "v_lshlrev_b32 v202, 19, v197\n"
    "v_min_u32 v202, s94, v202\n"
    "v_add_u32 v29, s93, v202\n"
    "s_cmp_eq_u32 s7, 3\n"
    "s_cselect_b32 s1, 4, 8\n"
    "v_cmp_eq_u32 vcc, s1, v197\n"
    "v_mov_b32 v202, s66\n"
    "s_nop 0\n"
    "v_cndmask_b32 v29, v29, v202, vcc\n"
    "s_lshl_b32 s1, s18, 18\n"
    "s_cmp_lt_u32 s7, 2\n"
    "s_cselect_b32 s1, s1, 0\n"
    "s_mov_b32 s2, 0x1bfc\n"
    "s_cmp_eq_u32 s7, 2\n"
    "s_cselect_b32 s2, s2, 0x7fc\n"
    "s_cmp_lt_u32 s7, 2\n"
    "s_cselect_b32 s2, 0x3fffc, s2\n"
    "v_lshrrev_b32 v202, 5, v192\n"
    "v_mul_u32_u24 v202, s2, v202\n"
    "v_add_u32 v31, s1, v202\n"
    "s_mov_b32 s85, 0x10000\n"
    "s_mov_b32 s87, 0\n"
    "global_load_dword a220, v29, s[96:97] sc1\n"
    "global_load_dword a220, v31, s[28:29] sc1\n"
    "s_getpc_b64 s[0:1]\n"
    ".Lk2_wi0:\n"
    "s_add_u32 s0, s0, .Lk2_wi1-.Lk2_wi0\n"
    "s_addc_u32 s1, s1, 0\n"
    "s_mov_b64 exec, 0\n"
    "s_cmp_eq_u32 s7, 0\n"
    "s_cbranch_scc1 .Lk2_hw0\n"
    "s_cmp_eq_u32 s7, 1\n"
    "s_cbranch_scc1 .Lk2_hw1\n"
    "s_cmp_eq_u32 s7, 2\n"
    "s_cbranch_scc1 .Lk2_hw2\n"
    "s_branch .Lk2_hw3\n"
    ".Lk2_wi1:\n"
    "s_mov_b64 exec, -1\n"
    "s_add_u32 m0, s52, 0\n"
    "s_nop 0\n"
    "buffer_load_dwordx4 v16, s[36:39], s84 offen lds\n"
    "v_lshl_add_u32 v212, v208, 2, v195\n"
    "v_lshrrev_b32 v213, 1, v193\n"
    "s_lshl_b32 s1, s23, 11\n"
    "v_lshl_add_u32 v202, v213, 13, s1\n"
    "v_lshl_add_u32 v202, v212, 7, v202\n"
    "v_and_b32 v200, 6, v212\n"
    "v_xor_b32 v200, v210, v200\n"
    "v_lshl_add_u32 v202, v200, 4, v202\n"
    "v_lshl_add_u32 v214, v209, 3, v202\n"
    "v_accvgpr_write_b32 a160, v214\n"
    "v_xor_b32 v202, 32, v214\n"
    "v_accvgpr_write_b32 a161, v202\n"
    "v_xor_b32 v202, 64, v214\n"
    "v_accvgpr_write_b32 a162, v202\n"
    "v_xor_b32 v202, 0x60, v214\n"
    "v_accvgpr_write_b32 a163, v202\n"
    "buffer_load_dwordx4 v17, s[36:39], s84 offen offset:1024 lds\n"
    "s_lshl_b32 s1, s7, 14\n"
    "s_lshl_b32 s2, s19, 8\n"
    "s_add_u32 s2, s1, s2\n"
    "v_lshl_add_u32 v202, v197, 11, s2\n"
    "v_lshlrev_b32 v200, 1, v193\n"
    "v_xor_b32 v200, v198, v200\n"
    "v_lshl_add_u32 v214, v200, 4, v202\n"
    "s_lshl_b32 s2, s17, 7\n"
    "s_add_u32 s2, 0x400, s2\n"
    "v_add_u32 v202, s2, v214\n"
    "v_accvgpr_write_b32 a164, v202\n"
    "s_xor_b32 s2, s17, 1\n"
    "s_lshl_b32 s2, s2, 7\n"
    "v_add_u32 v202, s2, v214\n"
    "v_accvgpr_write_b32 a165, v202\n"
    "buffer_load_dwordx4 v18, s[36:39], s84 offen offset:2048 lds\n"
    "s_lshl_b32 s2, s22, 12\n"
    "s_lshl_b32 s1, s23, 13\n"
    "s_add_u32 s1, s2, s1\n"
    "v_add_u32 v202, s1, v25\n"
    "v_accvgpr_write_b32 a166, v202\n"
    "s_lshr_b32 s1, s19, 1\n"
    "s_add_u32 s1, s18, s1\n"
    "s_and_b32 s1, s1, 1\n"
    "s_lshl_b32 s1, s1, 1\n"
    "v_xor_b32 v214, s1, v192\n"
    "v_cmp_gt_u32 vcc, 24, v192\n"
    "v_bfe_u32 v202, v214, 1, 2\n"
    "v_mul_u32_u24 v202, 7, v202\n"
    "v_lshrrev_b32 v200, 3, v214\n"
    "v_add_u32 v202, v202, v200\n"
    "v_and_b32 v200, 1, v214\n"
    "v_lshl_add_u32 v202, v200, 2, v202\n"
    "v_mul_u32_u24 v200, 7, v214\n"
    "v_add_u32 v200, 0xffffff5b, v200\n"
    "v_cndmask_b32 v215, v200, v202, vcc\n"
    "buffer_load_dwordx4 v19, s[36:39], s84 offen offset:3072 lds\n"
    "s_mul_i32 s1, s17, 9\n"
    "s_add_u32 s1, 0xffffffe4, s1\n"
    "v_cmp_gt_u32 vcc, 32, v192\n"
    "s_nop 1\n"
    "v_cndmask_b32 v200, v192, v214, vcc\n"
    "v_add_u32 v214, s1, v200\n"
    "v_cmp_gt_u32 vcc, 28, v192\n"
    "s_nop 1\n"
    "v_cndmask_b32 v200, v214, v215, vcc\n"
    "v_lshlrev_b32 v200, 17, v200\n"
    "v_accvgpr_write_b32 a198, v200\n"
    "v_cmp_gt_u32 vcc, 28, v192\n"
    "s_mul_i32 s1, s17, 36\n"
    "v_add_u32 v200, s1, v215\n"
    "v_lshrrev_b32 v202, 3, v214\n"
    "v_mul_u32_u24 v202, 28, v202\n"
    "v_add3_u32 v202, v202, v214, 28\n"
    "v_cndmask_b32 v202, v202, v200, vcc\n"
    "v_lshlrev_b32 v202, 14, v202\n"
    "v_accvgpr_write_b32 a167, v202\n"
    "s_add_u32 s84, s84, 128\n"
    "s_add_u32 m0, s52, 4096\n"
    "s_nop 0\n"
    "buffer_load_dwordx4 v16, s[36:39], s85 offen lds\n"
    "s_lshl_b32 s1, s23, 2\n"
    "s_lshl3_add_u32 s1, s18, s1\n"
    "s_add_u32 s1, s1, s22\n"
    "s_lshl_b32 s1, s1, 15\n"
    "s_lshl_b32 s2, s19, 8\n"
    "s_add_u32 s93, s1, s2\n"
    "s_lshl_b32 s2, s17, 7\n"
    "s_add_u32 s2, s93, s2\n"
    "v_lshl_add_u32 v202, v197, 11, s2\n"
    "v_xor_b32 v200, v198, v193\n"
    "v_lshl_add_u32 v202, v200, 4, v202\n"
    "v_accvgpr_write_b32 a168, v202\n"
    "buffer_load_dwordx4 v17, s[36:39], s85 offen offset:1024 lds\n"
    "s_lshl_b32 s2, s19, 8\n"
    "s_lshl_b32 s1, s17, 7\n"
    "s_add_u32 s1, s2, s1\n"
    "v_and_b32 v202, 31, v192\n"
    "v_lshl_add_u32 v202, v202, 2, s1\n"
    "v_accvgpr_write_b32 a169, v202\n"
    "s_lshl_b32 s1, s17, 7\n"
    "s_add_u32 s1, s93, s1\n"
    "v_lshl_add_u32 v202, v197, 11, s1\n"
    "v_lshl_add_u32 v202, v198, 4, v202\n"
    "v_accvgpr_write_b32 a170, v202\n"
    "s_lshl_b32 s1, s22, 7\n"
    "s_add_u32 s1, 0x20000, s1\n"
    "s_lshl_b32 s2, s23, 10\n"
    "s_add_u32 s2, s1, s2\n"
    "v_lshl_add_u32 v214, v194, 3, s2\n"
    "buffer_load_dwordx4 v18, s[36:39], s85 offen offset:2048 lds\n"
    "v_accvgpr_write_b32 a171, v214\n"
    "v_xor_b32 v202, 0x400, v214\n"
    "v_accvgpr_write_b32 a172, v202\n"
    "s_lshl_b32 s2, s23, 7\n"
    "s_add_u32 s2, 0x20800, s2\n"
    "v_lshl_add_u32 v214, v193, 4, s2\n"
    "v_accvgpr_write_b32 a173, v214\n"
    "s_mov_b32 s2, 0x20b00\n"
    "v_lshl_add_u32 v202, v193, 4, s2\n"
    "v_accvgpr_write_b32 a174, v202\n"
    "s_lshl_b32 s2, s7, 6\n"
    "s_add_u32 s2, s9, s2\n"
    "s_cmp_lt_u32 s7, 3\n"
    "s_cselect_b32 s2, s2, s16\n"
    "s_lshl_b32 s2, s2, 1\n"
    "s_cmp_lt_u32 s7, 3\n"
    "s_cselect_b32 s1, 63, 31\n"
    "v_and_b32 v202, s1, v192\n"
    "v_lshl_add_u32 v202, v202, 1, s2\n"
    "v_accvgpr_write_b32 a176, v202\n"
    "buffer_load_dwordx4 v19, s[36:39], s85 offen offset:3072 lds\n"
    "s_lshl_b32 s2, s99, 12\n"
    "s_add_u32 s2, 0x400, s2\n"
    "s_and_b32 s1, s6, 4\n"
    "s_lshl_b32 s1, s1, 8\n"
    "s_add_u32 s1, s2, s1\n"
    "s_lshl_b32 s2, s17, 20\n"
    "s_add_u32 s2, s1, s2\n"
    "v_lshrrev_b32 v202, 5, v192\n"
    "v_lshl_add_u32 v202, v202, 11, s2\n"
    "v_and_b32 v200, 31, v192\n"
    "v_lshl_add_u32 v202, v200, 5, v202\n"
    "v_accvgpr_write_b32 a178, v202\n"
    "s_add_u32 s85, s85, 128\n"
    "s_add_u32 m0, s54, 0\n"
    "s_nop 0\n"
    "buffer_load_dwordx4 v23, s[40:43], s87 offen lds\n"
    "v_lshlrev_b32 v202, 6, v198\n"
    "v_lshl_add_u32 v202, v208, 5, v202\n"
    "v_lshl_add_u32 v202, v213, 4, v202\n"
    "v_and_b32 v200, 1, v197\n"
    "v_lshl_add_u32 v202, v200, 3, v202\n"
    "v_accvgpr_write_b32 a192, v202\n"
    "s_lshl_b32 s2, s7, 12\n"
    "s_add_u32 s2, 0x21800, s2\n"
    "v_lshl_add_u32 v202, v194, 7, s2\n"
    "v_lshrrev_b32 v200, 1, v194\n"
    "v_xor_b32 v200, v213, v200\n"
    "v_lshl_add_u32 v202, v200, 4, v202\n"
    "v_lshl_add_u32 v202, v208, 3, v202\n"
    "v_accvgpr_write_b32 a194, v202\n"
    "s_add_u32 s87, s87, s55\n"
    "s_add_u32 s87, s87, s55\n"
    "s_add_u32 m0, s53, 12288\n"
    "s_nop 0\n"
    "buffer_load_dwordx4 v20, s[40:43], s86 offen lds\n"
    "s_lshl_b32 s2, s7, 8\n"
    "s_add_u32 s2, 0x21400, s2\n"
    "v_lshl_add_u32 v202, v193, 3, s2\n"
    "v_accvgpr_write_b32 a195, v202\n"
    "s_xor_b32 s2, s7, 1\n"
    "s_lshl_b32 s2, s2, 13\n"
    "s_add_u32 s25, s2, 0x20000\n"
    "v_mul_u32_u24 v202, 0x90, v194\n"
    "v_add_u32 v202, s25, v202\n"
    "v_lshl_add_u32 v202, v193, 3, v202\n"
    "v_accvgpr_write_b32 a196, v202\n"
    "v_mul_u32_u24 v202, 0x90, v197\n"
    "v_add_u32 v202, s25, v202\n"
    "v_lshl_add_u32 v202, v198, 4, v202\n"
    "v_accvgpr_write_b32 a197, v202\n"
    "buffer_load_dwordx4 v21, s[40:43], s86 offen offset:1024 lds\n"
    "v_cmp_eq_u32 vcc, v193, v195\n"
    "s_nop 1\n"
    "v_cndmask_b32 v214, 0, v211, vcc\n"
    "v_cmp_gt_u32 vcc, 2, v196\n"
    "s_nop 1\n"
    "v_cndmask_b32 v215, 0, v214, vcc\n"
    "v_xor_b32 v214, v214, v215\n"
    "v_accvgpr_write_b32 a200, v215\n"
    "v_accvgpr_write_b32 a201, v214\n"
    "v_accvgpr_write_b32 a202, 0\n"
    "v_accvgpr_write_b32 a203, 0\n"
    "v_accvgpr_write_b32 a204, 0\n"
    "v_accvgpr_write_b32 a205, 0\n"
    "v_accvgpr_write_b32 a206, v215\n"
    "v_accvgpr_write_b32 a207, v214\n"
    "buffer_load_dwordx4 v22, s[40:43], s86 offen offset:2048 lds\n"
    "v_accvgpr_write_b32 a208, v215\n"
    "v_accvgpr_write_b32 a209, v214\n"
    "v_accvgpr_write_b32 a210, v215\n"
    "v_accvgpr_write_b32 a211, v214\n"
    "s_mov_b32 s98, 0\n"
    "s_mov_b32 s88, 0\n"
    "s_lshl_b32 s89, s55, 3\n"
    "s_lshr_b32 s27, s55, 2\n"
    "s_mov_b32 s90, 1073741824\n"
    "s_mov_b32 s91, 512\n"
    "s_mov_b32 s34, 1073741824\n"
    "v_accvgpr_write_b32 a216, -1\n"
    "v_accvgpr_write_b32 a217, -1\n"
    "v_accvgpr_write_b32 a218, -1\n"
    "v_accvgpr_write_b32 a219, -1\n"
    "s_add_u32 s86, s86, s55\n"
    "s_add_u32 m0, s53, 24576\n"
    "s_nop 0\n"
    "buffer_load_dwordx4 v20, s[40:43], s86 offen lds\n"
    "buffer_load_dwordx4 v21, s[40:43], s86 offen offset:1024 lds\n"
    "buffer_load_dwordx4 v22, s[40:43], s86 offen offset:2048 lds\n"
    "s_add_u32 s86, s86, s55\n"
    "s_mov_b32 s94, 0x2000000\n"
    "s_mov_b64 exec, 1\n"
    "buffer_load_dword v27, off, s[44:47], s94 sc1\n"
    "s_mov_b64 exec, -1\n"
    "s_waitcnt vmcnt(18)\n"
    "v_mfma_f32_16x16x32_bf16 a[128:131], a[132:135], a[136:139], 0\n"
    "v_mfma_f32_16x16x32_bf16 a[128:131], a[132:135], a[140:143], a[128:131]\n"
    "v_mfma_f32_16x16x32_bf16 a[128:131], a[132:135], a[144:147], a[128:131]\n"
    "v_mfma_f32_16x16x32_bf16 a[128:131], a[132:135], a[148:151], a[128:131]\n"
    "s_waitcnt vmcnt(4)\n"
    "s_barrier\n"
    "ds_read_b128 a[64:67], v8 offset:0\n"
    "ds_read_b128 a[68:71], v10 offset:0\n"
    "ds_read_b128 a[72:75], v8 offset:4096\n"
    "ds_read_b128 a[76:79], v10 offset:4096\n"
    "ds_read_b64_tr_b16 a[0:1], v14 offset:0\n"
    "ds_read_b64_tr_b16 a[2:3], v14 offset:512\n"
    "ds_read_b64_tr_b16 a[4:5], v14 offset:12288\n"
    "ds_read_b64_tr_b16 a[6:7], v14 offset:12800\n"
    "ds_read_b64_tr_b16 a[8:9], v15 offset:0\n"
    "ds_read_b64_tr_b16 a[10:11], v15 offset:512\n"
    "ds_read_b64_tr_b16 a[12:13], v15 offset:12288\n"
    "ds_read_b64_tr_b16 a[14:15], v15 offset:12800\n"
    "ds_read_b64_tr_b16 a[16:17], v14 offset:1024\n"
    "ds_read_b64_tr_b16 a[18:19], v14 offset:1536\n"
    "ds_read_b64_tr_b16 a[20:21], v14 offset:13312\n"
    "ds_read_b64_tr_b16 a[22:23], v14 offset:13824\n"
    "ds_read_b64_tr_b16 a[24:25], v15 offset:1024\n"
    "ds_read_b64_tr_b16 a[26:27], v15 offset:1536\n"
    "ds_read_b64_tr_b16 a[28:29], v15 offset:13312\n"
    "ds_read_b64_tr_b16 a[30:31], v15 offset:13824\n"
    "ds_read_b64_tr_b16 a[32:33], v14 offset:2048\n"
    "ds_read_b64_tr_b16 a[34:35], v14 offset:2560\n"
    "ds_read_b64_tr_b16 a[36:37], v14 offset:14336\n"
    "ds_read_b64_tr_b16 a[38:39], v14 offset:14848\n"
    "s_waitcnt lgkmcnt(6)\n"
    "v_mfma_f32_16x16x32_bf16 v[32:35], a[0:3], a[64:67], 0\n"
    "s_add_u32 m0, s53, 36864\n"
    "ds_read_b64_tr_b16 a[40:41], v15 offset:2048\n"
    "v_mfma_f32_16x16x32_bf16 v[32:35], a[4:7], a[68:71], v[32:35]\n"
    "buffer_load_dwordx4 v20, s[40:43], s86 offen lds\n"
    "v_mfma_f32_16x16x32_bf16 v[56:59], a[4:7], a[76:79], 0\n"
    "ds_read_b64_tr_b16 a[42:43], v15 offset:2560\n"
    "v_mfma_f32_16x16x32_bf16 v[56:59], a[0:3], a[72:75], v[56:59]\n"
    "ds_read_b64_tr_b16 a[44:45], v15 offset:14336\n"
    "v_mfma_f32_16x16x32_bf16 v[60:63], a[8:11], a[72:75], 0\n"
    "buffer_load_dwordx4 v21, s[40:43], s86 offen offset:1024 lds\n"
    "v_mfma_f32_16x16x32_bf16 v[60:63], a[12:15], a[76:79], v[60:63]\n"
    "ds_read_b64_tr_b16 a[46:47], v15 offset:14848\n"
    "v_mfma_f32_16x16x32_bf16 v[36:39], a[12:15], a[68:71], 0\n"
    "ds_read_b64_tr_b16 a[48:49], v12 offset:0\n"
    "v_mfma_f32_16x16x32_bf16 v[36:39], a[8:11], a[64:67], v[36:39]\n"
    "ds_read_b64_tr_b16 a[50:51], v12 offset:256\n"
    "v_mfma_f32_16x16x32_bf16 v[40:43], a[16:19], a[64:67], 0\n"
    "buffer_load_dwordx4 v22, s[40:43], s86 offen offset:2048 lds\n"
    "v_mfma_f32_16x16x32_bf16 v[40:43], a[20:23], a[68:71], v[40:43]\n"
    "ds_read_b64_tr_b16 a[52:53], v12 offset:512\n"
    "v_mfma_f32_16x16x32_bf16 v[64:67], a[20:23], a[76:79], 0\n"
    "s_add_u32 s86, s86, s55\n"
    "s_add_u32 m0, s52, 36864\n"
    "v_mfma_f32_16x16x32_bf16 v[64:67], a[16:19], a[72:75], v[64:67]\n"
    "buffer_load_dwordx4 v16, s[36:39], s84 offen lds\n"
    "v_mfma_f32_16x16x32_bf16 v[68:71], a[24:27], a[72:75], 0\n"
    "ds_read_b64_tr_b16 a[54:55], v12 offset:768\n"
    "s_waitcnt lgkmcnt(6)\n"
    "v_mfma_f32_16x16x32_bf16 v[68:71], a[28:31], a[76:79], v[68:71]\n"
    "ds_read_b64_tr_b16 a[56:57], v13 offset:0\n"
    "v_mfma_f32_16x16x32_bf16 v[44:47], a[28:31], a[68:71], 0\n"
    "ds_read_b64_tr_b16 a[58:59], v13 offset:256\n"
    "v_mfma_f32_16x16x32_bf16 v[44:47], a[24:27], a[64:67], v[44:47]\n"
    "buffer_load_dwordx4 v17, s[36:39], s84 offen offset:1024 lds\n"
    "v_mfma_f32_16x16x32_bf16 v[48:51], a[32:35], a[64:67], 0\n"
    "ds_read_b64_tr_b16 a[60:61], v13 offset:512\n"
    "v_mfma_f32_16x16x32_bf16 v[48:51], a[36:39], a[68:71], v[48:51]\n"
    "ds_read_b64_tr_b16 a[62:63], v13 offset:768\n"
    "v_mfma_f32_16x16x32_bf16 v[72:75], a[36:39], a[76:79], 0\n"
    "buffer_load_dwordx4 v18, s[36:39], s84 offen offset:2048 lds\n"
    "v_mfma_f32_16x16x32_bf16 v[72:75], a[32:35], a[72:75], v[72:75]\n"
    "ds_read_b128 a[80:83], v8 offset:8192\n"
    "v_mfma_f32_16x16x32_bf16 v[76:79], a[40:43], a[72:75], 0\n"
    ".Lk2_m702:\n"
    ".Lk2_hw0:\n"
    "s_cbranch_execz .Lk2_m3044\n"
    "ds_read_b128 a[84:87], v10 offset:8192\n"
    "s_waitcnt lgkmcnt(6)\n"
    "v_mfma_f32_16x16x32_bf16 v[76:79], a[44:47], a[76:79], v[76:79]\n"
    "ds_read_b128 a[88:91], v8 offset:12288\n"
    "v_mfma_f32_16x16x32_bf16 v[52:55], a[44:47], a[68:71], 0\n"
    "buffer_load_dwordx4 v19, s[36:39], s84 offen offset:3072 lds\n"
    "v_mfma_f32_16x16x32_bf16 v[52:55], a[40:43], a[64:67], v[52:55]\n"
    "ds_read_b128 a[92:95], v10 offset:12288\n"
    "v_mfma_f32_16x16x32_bf16 v[224:227], a[48:51], a[64:67], 0\n"
    "s_add_u32 s84, s84, 128\n"
    "s_add_u32 m0, s52, 40960\n"
    "v_mfma_f32_16x16x32_bf16 v[224:227], a[52:55], a[68:71], v[224:227]\n"
    "buffer_load_dwordx4 v16, s[36:39], s85 offen lds\n"
    "v_mfma_f32_16x16x32_bf16 v[232:235], a[52:55], a[76:79], 0\n"
    "ds_read_b128 a[96:99], v9 offset:0\n"
    "v_mfma_f32_16x16x32_bf16 v[232:235], a[48:51], a[72:75], v[232:235]\n"
    "ds_read_b128 a[100:103], v11 offset:0\n"
    "s_waitcnt lgkmcnt(6)\n"
    "v_mfma_f32_16x16x32_bf16 v[236:239], a[56:59], a[72:75], 0\n"
    "ds_read_b128 a[104:107], v9 offset:4096\n"
    "v_mfma_f32_16x16x32_bf16 v[236:239], a[60:63], a[76:79], v[236:239]\n"
    "buffer_load_dwordx4 v17, s[36:39], s85 offen offset:1024 lds\n"
    "v_mfma_f32_16x16x32_bf16 v[228:231], a[60:63], a[68:71], 0\n"
    "ds_read_b128 a[108:111], v11 offset:4096\n"
    "v_mfma_f32_16x16x32_bf16 v[228:231], a[56:59], a[64:67], v[228:231]\n"
    "s_waitcnt lgkmcnt(6)\n"
    "v_mfma_f32_16x16x32_bf16 v[244:247], a[56:59], a[80:83], 0\n"
    "buffer_load_dwordx4 v18, s[36:39], s85 offen offset:2048 lds\n"
    "v_mfma_f32_16x16x32_bf16 v[244:247], a[60:63], a[84:87], v[244:247]\n"
    "ds_read_b128 a[112:115], v9 offset:8192\n"
    "s_waitcnt lgkmcnt(5)\n"
    "v_mfma_f32_16x16x32_bf16 v[252:255], a[60:63], a[92:95], 0\n"
    "v_mfma_f32_16x16x32_bf16 v[252:255], a[56:59], a[88:91], v[252:255]\n"
    "ds_read_b128 a[116:119], v11 offset:8192\n"
    "v_mfma_f32_16x16x32_bf16 v[248:251], a[48:51], a[88:91], 0\n"
    "buffer_load_dwordx4 v19, s[36:39], s85 offen offset:3072 lds\n"
    "v_mfma_f32_16x16x32_bf16 v[248:251], a[52:55], a[92:95], v[248:251]\n"
    "ds_read_b128 a[120:123], v9 offset:12288\n"
    "v_mfma_f32_16x16x32_bf16 v[240:243], a[52:55], a[84:87], 0\n"
    "s_add_u32 s85, s85, 128\n"
    "s_add_u32 m0, s54, 36864\n"
    "v_mfma_f32_16x16x32_bf16 v[240:243], a[48:51], a[80:83], v[240:243]\n"
    "buffer_load_dwordx4 v23, s[40:43], s87 offen lds\n"
    "v_mfma_f32_16x16x32_bf16 v[100:103], a[40:43], a[80:83], 0\n"
    "s_add_u32 s87, s87, s55\n"
    "s_add_u32 s87, s87, s55\n"
    "v_mfma_f32_16x16x32_bf16 v[100:103], a[44:47], a[84:87], v[100:103]\n"
    "ds_read_b128 a[124:127], v11 offset:12288\n"
    "v_mfma_f32_16x16x32_bf16 v[124:127], a[44:47], a[92:95], 0\n"
    "v_mfma_f32_16x16x32_bf16 v[124:127], a[40:43], a[88:91], v[124:127]\n"
    "v_mfma_f32_16x16x32_bf16 v[120:123], a[32:35], a[88:91], 0\n"
    "v_mfma_f32_16x16x32_bf16 v[120:123], a[36:39], a[92:95], v[120:123]\n"
    "v_mfma_f32_16x16x32_bf16 v[96:99], a[36:39], a[84:87], 0\n"
    "v_mfma_f32_16x16x32_bf16 v[96:99], a[32:35], a[80:83], v[96:99]\n"
    "s_waitcnt lgkmcnt(0)\n"
    "s_barrier\n"
    "v_mfma_f32_16x16x32_bf16 v[92:95], a[24:27], a[80:83], 0\n"
    "s_add_u32 m0, s53, 0\n"
    "v_mfma_f32_16x16x32_bf16 v[92:95], a[28:31], a[84:87], v[92:95]\n"
    "buffer_load_dwordx4 v20, s[40:43], s86 offen lds\n"
    "v_mfma_f32_16x16x32_bf16 v[116:119], a[28:31], a[92:95], 0\n"
    "v_mfma_f32_16x16x32_bf16 v[116:119], a[24:27], a[88:91], v[116:119]\n"
    "v_mfma_f32_16x16x32_bf16 v[112:115], a[16:19], a[88:91], 0\n"
    "v_mfma_f32_16x16x32_bf16 v[112:115], a[20:23], a[92:95], v[112:115]\n"
    "buffer_load_dwordx4 v21, s[40:43], s86 offen offset:1024 lds\n"
    "v_mfma_f32_16x16x32_bf16 v[88:91], a[20:23], a[84:87], 0\n"
    "v_mfma_f32_16x16x32_bf16 v[88:91], a[16:19], a[80:83], v[88:91]\n"
    "v_mfma_f32_16x16x32_bf16 v[84:87], a[8:11], a[80:83], 0\n"
    "v_mfma_f32_16x16x32_bf16 v[84:87], a[12:15], a[84:87], v[84:87]\n"
    "buffer_load_dwordx4 v22, s[40:43], s86 offen offset:2048 lds\n"
    "v_mfma_f32_16x16x32_bf16 v[108:111], a[12:15], a[92:95], 0\n"
    "v_mfma_f32_16x16x32_bf16 v[108:111], a[8:11], a[88:91], v[108:111]\n"
    "v_mfma_f32_16x16x32_bf16 v[104:107], a[0:3], a[88:91], 0\n"
    "s_add_u32 s86, s86, s55\n"
    "s_add_u32 m0, s53, 12288\n"
    "v_mfma_f32_16x16x32_bf16 v[104:107], a[4:7], a[92:95], v[104:107]\n"
    "buffer_load_dwordx4 v20, s[40:43], s86 offen lds\n"
    "v_mfma_f32_16x16x32_bf16 v[80:83], a[4:7], a[84:87], 0\n"
    "v_mfma_f32_16x16x32_bf16 v[80:83], a[0:3], a[80:83], v[80:83]\n"
    "v_mfma_f32_16x16x32_bf16 v[148:151], a[40:43], a[96:99], 0\n"
    "v_mfma_f32_16x16x32_bf16 v[148:151], a[44:47], a[100:103], v[148:151]\n"
    "buffer_load_dwordx4 v21, s[40:43], s86 offen offset:1024 lds\n"
    "v_mfma_f32_16x16x32_bf16 v[172:175], a[44:47], a[108:111], 0\n"
    "v_mfma_f32_16x16x32_bf16 v[172:175], a[40:43], a[104:107], v[172:175]\n"
    "v_mfma_f32_16x16x32_bf16 v[168:171], a[32:35], a[104:107], 0\n"
    "v_mfma_f32_16x16x32_bf16 v[168:171], a[36:39], a[108:111], v[168:171]\n"
    "buffer_load_dwordx4 v22, s[40:43], s86 offen offset:2048 lds\n"
    "v_mfma_f32_16x16x32_bf16 v[144:147], a[36:39], a[100:103], 0\n"
    "s_add_u32 s86, s86, s55\n"
    "v_mfma_f32_16x16x32_bf16 v[144:147], a[32:35], a[96:99], v[144:147]\n"
    "v_mfma_f32_16x16x32_bf16 v[140:143], a[24:27], a[96:99], 0\n"
    "buffer_load_dwordx4 a[136:139], v24, s[40:43], s89 offen\n"
    "v_mfma_f32_16x16x32_bf16 v[140:143], a[28:31], a[100:103], v[140:143]\n"
    "v_mfma_f32_16x16x32_bf16 v[164:167], a[28:31], a[108:111], 0\n"
    "v_mfma_f32_16x16x32_bf16 v[164:167], a[24:27], a[104:107], v[164:167]\n"
    "s_add_u32 s94, s89, s27\n"
    "v_mfma_f32_16x16x32_bf16 v[160:163], a[16:19], a[104:107], 0\n"
    "buffer_load_dwordx4 a[140:143], v24, s[40:43], s94 offen\n"
    "v_mfma_f32_16x16x32_bf16 v[160:163], a[20:23], a[108:111], v[160:163]\n"
    "v_mfma_f32_16x16x32_bf16 v[136:139], a[20:23], a[100:103], 0\n"
    "v_mfma_f32_16x16x32_bf16 v[136:139], a[16:19], a[96:99], v[136:139]\n"
    "s_lshl_b32 s94, s55, 2\n"
    "s_add_u32 s89, s89, s94\n"
    "v_mfma_f32_16x16x32_bf16 v[132:135], a[8:11], a[96:99], 0\n"
    "buffer_store_dwordx4 a[216:219], v25, s[44:47], s90 offen sc1\n"
    "v_mfma_f32_16x16x32_bf16 v[132:135], a[12:15], a[100:103], v[132:135]\n"
    "s_add_u32 s90, s90, 1024\n"
    "v_mfma_f32_16x16x32_bf16 v[156:159], a[12:15], a[108:111], 0\n"
    "v_mfma_f32_16x16x32_bf16 v[156:159], a[8:11], a[104:107], v[156:159]\n"
    "v_mfma_f32_16x16x32_bf16 v[152:155], a[0:3], a[104:107], 0\n"
    "v_mfma_f32_16x16x32_bf16 v[152:155], a[4:7], a[108:111], v[152:155]\n"
    "v_mfma_f32_16x16x32_bf16 v[128:131], a[4:7], a[100:103], 0\n"
    "v_mfma_f32_16x16x32_bf16 v[128:131], a[0:3], a[96:99], v[128:131]\n"
    "s_waitcnt vmcnt(9)\n"
    "s_barrier\n"
    "v_mfma_f32_16x16x32_bf16 v[176:179], a[0:3], a[112:115], 0\n"
    "ds_read_b128 a[64:67], v8 offset:36864\n"
    "v_mfma_f32_16x16x32_bf16 v[176:179], a[4:7], a[116:119], v[176:179]\n"
    "ds_read_b128 a[68:71], v10 offset:36864\n"
    "v_mfma_f32_16x16x32_bf16 v[200:203], a[4:7], a[124:127], 0\n"
    "ds_read_b128 a[72:75], v8 offset:40960\n"
    "v_mfma_f32_16x16x32_bf16 v[200:203], a[0:3], a[120:123], v[200:203]\n"
    "ds_read_b128 a[76:79], v10 offset:40960\n"
    "v_mfma_f32_16x16x32_bf16 v[204:207], a[8:11], a[120:123], 0\n"
    "ds_read_b64_tr_b16 a[0:1], v14 offset:24576\n"
    "v_mfma_f32_16x16x32_bf16 v[204:207], a[12:15], a[124:127], v[204:207]\n"
    "ds_read_b64_tr_b16 a[2:3], v14 offset:25088\n"
    "v_mfma_f32_16x16x32_bf16 v[180:183], a[12:15], a[116:119], 0\n"
    "ds_read_b64_tr_b16 a[4:5], v14 offset:36864\n"
    "v_mfma_f32_16x16x32_bf16 v[180:183], a[8:11], a[112:115], v[180:183]\n"
    "ds_read_b64_tr_b16 a[6:7], v14 offset:37376\n"
    "v_mfma_f32_16x16x32_bf16 v[184:187], a[16:19], a[112:115], 0\n"
    "ds_read_b64_tr_b16 a[8:9], v15 offset:24576\n"
    "v_mfma_f32_16x16x32_bf16 v[184:187], a[20:23], a[116:119], v[184:187]\n"
    "ds_read_b64_tr_b16 a[10:11], v15 offset:25088\n"
    "v_mfma_f32_16x16x32_bf16 v[208:211], a[20:23], a[124:127], 0\n"
    "ds_read_b64_tr_b16 a[12:13], v15 offset:36864\n"
    "v_mfma_f32_16x16x32_bf16 v[208:211], a[16:19], a[120:123], v[208:211]\n"
    "ds_read_b64_tr_b16 a[14:15], v15 offset:37376\n"
    "v_mfma_f32_16x16x32_bf16 v[212:215], a[24:27], a[120:123], 0\n"
    "ds_read_b64_tr_b16 a[16:17], v14 offset:25600\n"
    "v_mfma_f32_16x16x32_bf16 v[212:215], a[28:31], a[124:127], v[212:215]\n"
    "ds_read_b64_tr_b16 a[18:19], v14 offset:26112\n"
    "v_mfma_f32_16x16x32_bf16 v[188:191], a[28:31], a[116:119], 0\n"
    "ds_read_b64_tr_b16 a[20:21], v14 offset:37888\n"
    "v_mfma_f32_16x16x32_bf16 v[188:191], a[24:27], a[112:115], v[188:191]\n"
    "ds_read_b64_tr_b16 a[22:23], v14 offset:38400\n"
    "v_mfma_f32_16x16x32_bf16 v[192:195], a[32:35], a[112:115], 0\n"
    "ds_read_b64_tr_b16 a[24:25], v15 offset:25600\n"
    "v_mfma_f32_16x16x32_bf16 v[192:195], a[36:39], a[116:119], v[192:195]\n"
    "ds_read_b64_tr_b16 a[26:27], v15 offset:26112\n"
    "v_mfma_f32_16x16x32_bf16 v[216:219], a[36:39], a[124:127], 0\n"
    "ds_read_b64_tr_b16 a[28:29], v15 offset:37888\n"
    "v_mfma_f32_16x16x32_bf16 v[216:219], a[32:35], a[120:123], v[216:219]\n"
    "ds_read_b64_tr_b16 a[30:31], v15 offset:38400\n"
    "v_mfma_f32_16x16x32_bf16 v[220:223], a[40:43], a[120:123], 0\n"
    "ds_read_b64_tr_b16 a[32:33], v14 offset:26624\n"
    "v_mfma_f32_16x16x32_bf16 v[220:223], a[44:47], a[124:127], v[220:223]\n"
    "ds_read_b64_tr_b16 a[34:35], v14 offset:27136\n"
    "v_mfma_f32_16x16x32_bf16 v[196:199], a[44:47], a[116:119], 0\n"
    "ds_read_b64_tr_b16 a[36:37], v14 offset:38912\n"
    "v_mfma_f32_16x16x32_bf16 v[196:199], a[40:43], a[112:115], v[196:199]\n"
    "ds_read_b64_tr_b16 a[38:39], v14 offset:39424\n"
    "s_branch .Lk2_wu1\n"
    ".Lk2_wloop:\n"
    "s_waitcnt lgkmcnt(6)\n"
    "v_mfma_f32_16x16x32_bf16 v[32:35], a[0:3], a[64:67], v[32:35]\n"
    "s_add_u32 m0, s52, 36864\n"
    "ds_read_b64_tr_b16 a[40:41], v15 offset:2048\n"
    "v_mfma_f32_16x16x32_bf16 v[32:35], a[4:7], a[68:71], v[32:35]\n"
    "buffer_load_dwordx4 v16, s[36:39], s84 offen lds\n"
    "v_mfma_f32_16x16x32_bf16 v[56:59], a[4:7], a[76:79], v[56:59]\n"
    "ds_read_b64_tr_b16 a[42:43], v15 offset:2560\n"
    "v_mfma_f32_16x16x32_bf16 v[56:59], a[0:3], a[72:75], v[56:59]\n"
    "ds_read_b64_tr_b16 a[44:45], v15 offset:14336\n"
    "v_mfma_f32_16x16x32_bf16 v[60:63], a[8:11], a[72:75], v[60:63]\n"
    "ds_read_b64_tr_b16 a[46:47], v15 offset:14848\n"
    "v_mfma_f32_16x16x32_bf16 v[60:63], a[12:15], a[76:79], v[60:63]\n"
    "buffer_load_dwordx4 v17, s[36:39], s84 offen offset:1024 lds\n"
    "v_mfma_f32_16x16x32_bf16 v[36:39], a[12:15], a[68:71], v[36:39]\n"
    "ds_read_b64_tr_b16 a[48:49], v12 offset:0\n"
    "v_mfma_f32_16x16x32_bf16 v[36:39], a[8:11], a[64:67], v[36:39]\n"
    "ds_read_b64_tr_b16 a[50:51], v12 offset:256\n"
    "v_mfma_f32_16x16x32_bf16 v[40:43], a[16:19], a[64:67], v[40:43]\n"
    "ds_read_b64_tr_b16 a[52:53], v12 offset:512\n"
    "v_mfma_f32_16x16x32_bf16 v[40:43], a[20:23], a[68:71], v[40:43]\n"
    "v_mfma_f32_16x16x32_bf16 v[64:67], a[20:23], a[76:79], v[64:67]\n"
    "buffer_load_dwordx4 v18, s[36:39], s84 offen offset:2048 lds\n"
    "v_mfma_f32_16x16x32_bf16 v[64:67], a[16:19], a[72:75], v[64:67]\n"
    "ds_read_b64_tr_b16 a[54:55], v12 offset:768\n"
    "v_mfma_f32_16x16x32_bf16 v[68:71], a[24:27], a[72:75], v[68:71]\n"
    "ds_read_b64_tr_b16 a[56:57], v13 offset:0\n"
    "s_waitcnt lgkmcnt(6)\n"
    "v_mfma_f32_16x16x32_bf16 v[68:71], a[28:31], a[76:79], v[68:71]\n"
    "ds_read_b64_tr_b16 a[58:59], v13 offset:256\n"
    "v_mfma_f32_16x16x32_bf16 v[44:47], a[28:31], a[68:71], v[44:47]\n"
    "ds_read_b64_tr_b16 a[60:61], v13 offset:512\n"
    "v_mfma_f32_16x16x32_bf16 v[44:47], a[24:27], a[64:67], v[44:47]\n"
    "buffer_load_dwordx4 v19, s[36:39], s84 offen offset:3072 lds\n"
    "v_mfma_f32_16x16x32_bf16 v[48:51], a[32:35], a[64:67], v[48:51]\n"
    "ds_read_b64_tr_b16 a[62:63], v13 offset:768\n"
    "v_mfma_f32_16x16x32_bf16 v[48:51], a[36:39], a[68:71], v[48:51]\n"
    "ds_read_b128 a[80:83], v8 offset:8192\n"
    "v_mfma_f32_16x16x32_bf16 v[72:75], a[36:39], a[76:79], v[72:75]\n"
    "s_add_u32 s84, s84, 128\n"
    "s_add_u32 m0, s52, 40960\n"
    "v_mfma_f32_16x16x32_bf16 v[72:75], a[32:35], a[72:75], v[72:75]\n"
    "buffer_load_dwordx4 v16, s[36:39], s85 offen lds\n"
    "v_mfma_f32_16x16x32_bf16 v[76:79], a[40:43], a[72:75], v[76:79]\n"
    "ds_read_b128 a[84:87], v10 offset:8192\n"
    "s_waitcnt lgkmcnt(6)\n"
    "v_mfma_f32_16x16x32_bf16 v[76:79], a[44:47], a[76:79], v[76:79]\n"
    "ds_read_b128 a[88:91], v8 offset:12288\n"
    "v_mfma_f32_16x16x32_bf16 v[52:55], a[44:47], a[68:71], v[52:55]\n"
    "ds_read_b128 a[92:95], v10 offset:12288\n"
    "v_mfma_f32_16x16x32_bf16 v[52:55], a[40:43], a[64:67], v[52:55]\n"
    "ds_read_b128 a[96:99], v9 offset:0\n"
    "v_mfma_f32_16x16x32_bf16 v[224:227], a[48:51], a[64:67], v[224:227]\n"
    "buffer_load_dwordx4 v17, s[36:39], s85 offen offset:1024 lds\n"
    "v_mfma_f32_16x16x32_bf16 v[224:227], a[52:55], a[68:71], v[224:227]\n"
    "ds_read_b128 a[100:103], v11 offset:0\n"
    "v_mfma_f32_16x16x32_bf16 v[232:235], a[52:55], a[76:79], v[232:235]\n"
    "v_mfma_f32_16x16x32_bf16 v[232:235], a[48:51], a[72:75], v[232:235]\n"
    "ds_read_b128 a[104:107], v9 offset:4096\n"
    "s_waitcnt lgkmcnt(6)\n"
    "v_mfma_f32_16x16x32_bf16 v[236:239], a[56:59], a[72:75], v[236:239]\n"
    "v_mfma_f32_16x16x32_bf16 v[236:239], a[60:63], a[76:79], v[236:239]\n"
    "buffer_load_dwordx4 v18, s[36:39], s85 offen offset:2048 lds\n"
    "v_mfma_f32_16x16x32_bf16 v[228:231], a[60:63], a[68:71], v[228:231]\n"
    "ds_read_b128 a[108:111], v11 offset:4096\n"
    "v_mfma_f32_16x16x32_bf16 v[228:231], a[56:59], a[64:67], v[228:231]\n"
    "v_mfma_f32_16x16x32_bf16 v[244:247], a[56:59], a[80:83], v[244:247]\n"
    "ds_read_b128 a[112:115], v9 offset:8192\n"
    "s_waitcnt lgkmcnt(6)\n"
    "v_mfma_f32_16x16x32_bf16 v[244:247], a[60:63], a[84:87], v[244:247]\n"
    "buffer_load_dwordx4 v19, s[36:39], s85 offen offset:3072 lds\n"
    "s_waitcnt lgkmcnt(5)\n"
    "v_mfma_f32_16x16x32_bf16 v[252:255], a[60:63], a[92:95], v[252:255]\n"
    "ds_read_b128 a[116:119], v11 offset:8192\n"
    "v_mfma_f32_16x16x32_bf16 v[252:255], a[56:59], a[88:91], v[252:255]\n"
    "v_mfma_f32_16x16x32_bf16 v[248:251], a[48:51], a[88:91], v[248:251]\n"
    "v_mfma_f32_16x16x32_bf16 v[248:251], a[52:55], a[92:95], v[248:251]\n"
    "s_add_u32 s85, s85, 128\n"
    "s_add_u32 m0, s54, 36864\n"
    "v_mfma_f32_16x16x32_bf16 v[240:243], a[52:55], a[84:87], v[240:243]\n"
    "buffer_load_dwordx4 v23, s[40:43], s87 offen lds\n"
    "v_mfma_f32_16x16x32_bf16 v[240:243], a[48:51], a[80:83], v[240:243]\n"
    "s_add_u32 s87, s87, s55\n"
    "s_add_u32 s87, s87, s55\n"
    "v_mfma_f32_16x16x32_bf16 v[100:103], a[40:43], a[80:83], v[100:103]\n"
    "ds_read_b128 a[120:123], v9 offset:12288\n"
    "v_mfma_f32_16x16x32_bf16 v[100:103], a[44:47], a[84:87], v[100:103]\n"
    "ds_read_b128 a[124:127], v11 offset:12288\n"
    "v_mfma_f32_16x16x32_bf16 v[124:127], a[44:47], a[92:95], v[124:127]\n"
    "v_mfma_f32_16x16x32_bf16 v[124:127], a[40:43], a[88:91], v[124:127]\n"
    "v_mfma_f32_16x16x32_bf16 v[120:123], a[32:35], a[88:91], v[120:123]\n"
    "v_mfma_f32_16x16x32_bf16 v[120:123], a[36:39], a[92:95], v[120:123]\n"
    "v_mfma_f32_16x16x32_bf16 v[96:99], a[36:39], a[84:87], v[96:99]\n"
    "v_mfma_f32_16x16x32_bf16 v[96:99], a[32:35], a[80:83], v[96:99]\n"
    "s_waitcnt lgkmcnt(0)\n"
    "s_barrier\n"
    "v_mfma_f32_16x16x32_bf16 v[92:95], a[24:27], a[80:83], v[92:95]\n"
    "s_add_u32 m0, s53, 0\n"
    "v_mfma_f32_16x16x32_bf16 v[92:95], a[28:31], a[84:87], v[92:95]\n"
    "buffer_load_dwordx4 v20, s[40:43], s86 offen lds\n"
    "v_mfma_f32_16x16x32_bf16 v[116:119], a[28:31], a[92:95], v[116:119]\n"
    "v_mfma_f32_16x16x32_bf16 v[116:119], a[24:27], a[88:91], v[116:119]\n"
    "v_mfma_f32_16x16x32_bf16 v[112:115], a[16:19], a[88:91], v[112:115]\n"
    "v_mfma_f32_16x16x32_bf16 v[112:115], a[20:23], a[92:95], v[112:115]\n"
    "buffer_load_dwordx4 v21, s[40:43], s86 offen offset:1024 lds\n"
    "v_mfma_f32_16x16x32_bf16 v[88:91], a[20:23], a[84:87], v[88:91]\n"
    "v_mfma_f32_16x16x32_bf16 v[88:91], a[16:19], a[80:83], v[88:91]\n"
    "v_mfma_f32_16x16x32_bf16 v[84:87], a[8:11], a[80:83], v[84:87]\n"
    "v_mfma_f32_16x16x32_bf16 v[84:87], a[12:15], a[84:87], v[84:87]\n"
    "buffer_load_dwordx4 v22, s[40:43], s86 offen offset:2048 lds\n"
    "v_mfma_f32_16x16x32_bf16 v[108:111], a[12:15], a[92:95], v[108:111]\n"
    "v_mfma_f32_16x16x32_bf16 v[108:111], a[8:11], a[88:91], v[108:111]\n"
    "v_mfma_f32_16x16x32_bf16 v[104:107], a[0:3], a[88:91], v[104:107]\n"
    "s_add_u32 s86, s86, s55\n"
    "s_add_u32 m0, s53, 12288\n"
    "v_mfma_f32_16x16x32_bf16 v[104:107], a[4:7], a[92:95], v[104:107]\n"
    "buffer_load_dwordx4 v20, s[40:43], s86 offen lds\n"
    "v_mfma_f32_16x16x32_bf16 v[80:83], a[4:7], a[84:87], v[80:83]\n"
    "v_mfma_f32_16x16x32_bf16 v[80:83], a[0:3], a[80:83], v[80:83]\n"
    ".Lk2_wback:\n"
    "v_mfma_f32_16x16x32_bf16 v[148:151], a[40:43], a[96:99], v[148:151]\n"
    "v_mfma_f32_16x16x32_bf16 v[148:151], a[44:47], a[100:103], v[148:151]\n"
    "buffer_load_dwordx4 v21, s[40:43], s86 offen offset:1024 lds\n"
    "v_mfma_f32_16x16x32_bf16 v[172:175], a[44:47], a[108:111], v[172:175]\n"
    "v_mfma_f32_16x16x32_bf16 v[172:175], a[40:43], a[104:107], v[172:175]\n"
    "v_mfma_f32_16x16x32_bf16 v[168:171], a[32:35], a[104:107], v[168:171]\n"
    "v_mfma_f32_16x16x32_bf16 v[168:171], a[36:39], a[108:111], v[168:171]\n"
    "buffer_load_dwordx4 v22, s[40:43], s86 offen offset:2048 lds\n"
    "v_mfma_f32_16x16x32_bf16 v[144:147], a[36:39], a[100:103], v[144:147]\n"
    "s_add_u32 s86, s86, s55\n"
    "v_mfma_f32_16x16x32_bf16 v[144:147], a[32:35], a[96:99], v[144:147]\n"
    "v_mfma_f32_16x16x32_bf16 v[140:143], a[24:27], a[96:99], v[140:143]\n"
    "buffer_load_dwordx4 a[136:139], v24, s[40:43], s89 offen\n"
    "v_mfma_f32_16x16x32_bf16 v[140:143], a[28:31], a[100:103], v[140:143]\n"
    "v_mfma_f32_16x16x32_bf16 v[164:167], a[28:31], a[108:111], v[164:167]\n"
    "v_mfma_f32_16x16x32_bf16 v[164:167], a[24:27], a[104:107], v[164:167]\n"
    "s_add_u32 s94, s89, s27\n"
    "v_mfma_f32_16x16x32_bf16 v[160:163], a[16:19], a[104:107], v[160:163]\n"
    "buffer_load_dwordx4 a[140:143], v24, s[40:43], s94 offen\n"
    "v_mfma_f32_16x16x32_bf16 v[160:163], a[20:23], a[108:111], v[160:163]\n"
    "v_mfma_f32_16x16x32_bf16 v[136:139], a[20:23], a[100:103], v[136:139]\n"
    "v_mfma_f32_16x16x32_bf16 v[136:139], a[16:19], a[96:99], v[136:139]\n"
    "s_lshl_b32 s94, s55, 2\n"
    "s_add_u32 s89, s89, s94\n"
    "v_mfma_f32_16x16x32_bf16 v[132:135], a[8:11], a[96:99], v[132:135]\n"
    "buffer_store_dwordx4 a[216:219], v25, s[44:47], s90 offen sc1\n"
    "v_mfma_f32_16x16x32_bf16 v[132:135], a[12:15], a[100:103], v[132:135]\n"
    "s_add_u32 s90, s90, 1024\n"
    "v_mfma_f32_16x16x32_bf16 v[156:159], a[12:15], a[108:111], v[156:159]\n"
    "v_mfma_f32_16x16x32_bf16 v[156:159], a[8:11], a[104:107], v[156:159]\n"
    "v_mfma_f32_16x16x32_bf16 v[152:155], a[0:3], a[104:107], v[152:155]\n"
    "v_mfma_f32_16x16x32_bf16 v[152:155], a[4:7], a[108:111], v[152:155]\n"
    "v_mfma_f32_16x16x32_bf16 v[128:131], a[4:7], a[100:103], v[128:131]\n"
    "v_mfma_f32_16x16x32_bf16 v[128:131], a[0:3], a[96:99], v[128:131]\n"
    "s_waitcnt vmcnt(9)\n"
    "s_barrier\n"
    "v_mfma_f32_16x16x32_bf16 v[176:179], a[0:3], a[112:115], v[176:179]\n"
    "ds_read_b128 a[64:67], v8 offset:36864\n"
    "v_mfma_f32_16x16x32_bf16 v[176:179], a[4:7], a[116:119], v[176:179]\n"
    "ds_read_b128 a[68:71], v10 offset:36864\n"
    "v_mfma_f32_16x16x32_bf16 v[200:203], a[4:7], a[124:127], v[200:203]\n"
    "ds_read_b128 a[72:75], v8 offset:40960\n"
    "v_mfma_f32_16x16x32_bf16 v[200:203], a[0:3], a[120:123], v[200:203]\n"
    "ds_read_b128 a[76:79], v10 offset:40960\n"
    "v_mfma_f32_16x16x32_bf16 v[204:207], a[8:11], a[120:123], v[204:207]\n"
    "ds_read_b64_tr_b16 a[0:1], v14 offset:24576\n"
    "v_mfma_f32_16x16x32_bf16 v[204:207], a[12:15], a[124:127], v[204:207]\n"
    "ds_read_b64_tr_b16 a[2:3], v14 offset:25088\n"
    "v_mfma_f32_16x16x32_bf16 v[180:183], a[12:15], a[116:119], v[180:183]\n"
    "ds_read_b64_tr_b16 a[4:5], v14 offset:36864\n"
    "v_mfma_f32_16x16x32_bf16 v[180:183], a[8:11], a[112:115], v[180:183]\n"
    "ds_read_b64_tr_b16 a[6:7], v14 offset:37376\n"
    "v_mfma_f32_16x16x32_bf16 v[184:187], a[16:19], a[112:115], v[184:187]\n"
    "ds_read_b64_tr_b16 a[8:9], v15 offset:24576\n"
    "v_mfma_f32_16x16x32_bf16 v[184:187], a[20:23], a[116:119], v[184:187]\n"
    "ds_read_b64_tr_b16 a[10:11], v15 offset:25088\n"
    "v_mfma_f32_16x16x32_bf16 v[208:211], a[20:23], a[124:127], v[208:211]\n"
    "ds_read_b64_tr_b16 a[12:13], v15 offset:36864\n"
    "v_mfma_f32_16x16x32_bf16 v[208:211], a[16:19], a[120:123], v[208:211]\n"
    "ds_read_b64_tr_b16 a[14:15], v15 offset:37376\n"
    "v_mfma_f32_16x16x32_bf16 v[212:215], a[24:27], a[120:123], v[212:215]\n"
    "ds_read_b64_tr_b16 a[16:17], v14 offset:25600\n"
    "v_mfma_f32_16x16x32_bf16 v[212:215], a[28:31], a[124:127], v[212:215]\n"
    "ds_read_b64_tr_b16 a[18:19], v14 offset:26112\n"
    "v_mfma_f32_16x16x32_bf16 v[188:191], a[28:31], a[116:119], v[188:191]\n"
    "ds_read_b64_tr_b16 a[20:21], v14 offset:37888\n"
    "v_mfma_f32_16x16x32_bf16 v[188:191], a[24:27], a[112:115], v[188:191]\n"
    "ds_read_b64_tr_b16 a[22:23], v14 offset:38400\n"
    "v_mfma_f32_16x16x32_bf16 v[192:195], a[32:35], a[112:115], v[192:195]\n"
    "ds_read_b64_tr_b16 a[24:25], v15 offset:25600\n"
    "v_mfma_f32_16x16x32_bf16 v[192:195], a[36:39], a[116:119], v[192:195]\n"
    "ds_read_b64_tr_b16 a[26:27], v15 offset:26112\n"
    "v_mfma_f32_16x16x32_bf16 v[216:219], a[36:39], a[124:127], v[216:219]\n"
    "ds_read_b64_tr_b16 a[28:29], v15 offset:37888\n"
    "v_mfma_f32_16x16x32_bf16 v[216:219], a[32:35], a[120:123], v[216:219]\n"
    "ds_read_b64_tr_b16 a[30:31], v15 offset:38400\n"
    "v_mfma_f32_16x16x32_bf16 v[220:223], a[40:43], a[120:123], v[220:223]\n"
    "ds_read_b64_tr_b16 a[32:33], v14 offset:26624\n"
    "v_mfma_f32_16x16x32_bf16 v[220:223], a[44:47], a[124:127], v[220:223]\n"
    "ds_read_b64_tr_b16 a[34:35], v14 offset:27136\n"
    "v_mfma_f32_16x16x32_bf16 v[196:199], a[44:47], a[116:119], v[196:199]\n"
    "ds_read_b64_tr_b16 a[36:37], v14 offset:38912\n"
    "v_mfma_f32_16x16x32_bf16 v[196:199], a[40:43], a[112:115], v[196:199]\n"
    "ds_read_b64_tr_b16 a[38:39], v14 offset:39424\n"
    ".Lk2_wu1:\n"
    "s_waitcnt lgkmcnt(6)\n"
    "v_mfma_f32_16x16x32_bf16 v[32:35], a[0:3], a[64:67], v[32:35]\n"
    "s_add_u32 m0, s52, 0\n"
    "ds_read_b64_tr_b16 a[40:41], v15 offset:26624\n"
    "v_mfma_f32_16x16x32_bf16 v[32:35], a[4:7], a[68:71], v[32:35]\n"
    "buffer_load_dwordx4 v16, s[36:39], s84 offen lds\n"
    "v_mfma_f32_16x16x32_bf16 v[56:59], a[4:7], a[76:79], v[56:59]\n"
    "ds_read_b64_tr_b16 a[42:43], v15 offset:27136\n"
    "v_mfma_f32_16x16x32_bf16 v[56:59], a[0:3], a[72:75], v[56:59]\n"
    "ds_read_b64_tr_b16 a[44:45], v15 offset:38912\n"
    "v_mfma_f32_16x16x32_bf16 v[60:63], a[8:11], a[72:75], v[60:63]\n"
    "ds_read_b64_tr_b16 a[46:47], v15 offset:39424\n"
    "v_mfma_f32_16x16x32_bf16 v[60:63], a[12:15], a[76:79], v[60:63]\n"
    "buffer_load_dwordx4 v17, s[36:39], s84 offen offset:1024 lds\n"
    "v_mfma_f32_16x16x32_bf16 v[36:39], a[12:15], a[68:71], v[36:39]\n"
    "ds_read_b64_tr_b16 a[48:49], v12 offset:36864\n"
    "v_mfma_f32_16x16x32_bf16 v[36:39], a[8:11], a[64:67], v[36:39]\n"
    "ds_read_b64_tr_b16 a[50:51], v12 offset:37120\n"
    "v_mfma_f32_16x16x32_bf16 v[40:43], a[16:19], a[64:67], v[40:43]\n"
    "ds_read_b64_tr_b16 a[52:53], v12 offset:37376\n"
    "v_mfma_f32_16x16x32_bf16 v[40:43], a[20:23], a[68:71], v[40:43]\n"
    "s_cmp_eq_u32 s88, 2\n"
    "s_cselect_b32 s34, 0, s34\n"
    "v_mfma_f32_16x16x32_bf16 v[64:67], a[20:23], a[76:79], v[64:67]\n"
    "buffer_load_dwordx4 v18, s[36:39], s84 offen offset:2048 lds\n"
    "v_mfma_f32_16x16x32_bf16 v[64:67], a[16:19], a[72:75], v[64:67]\n"
    "ds_read_b64_tr_b16 a[54:55], v12 offset:37632\n"
    "v_mfma_f32_16x16x32_bf16 v[68:71], a[24:27], a[72:75], v[68:71]\n"
    "ds_read_b64_tr_b16 a[56:57], v13 offset:36864\n"
    "s_waitcnt lgkmcnt(6)\n"
    "v_mfma_f32_16x16x32_bf16 v[68:71], a[28:31], a[76:79], v[68:71]\n"
    "ds_read_b64_tr_b16 a[58:59], v13 offset:37120\n"
    "s_cmp_eq_u32 s88, 1\n"
    "s_cselect_b32 s90, s81, s90\n"
    "v_mfma_f32_16x16x32_bf16 v[44:47], a[28:31], a[68:71], v[44:47]\n"
    "ds_read_b64_tr_b16 a[60:61], v13 offset:37376\n"
    "v_mfma_f32_16x16x32_bf16 v[44:47], a[24:27], a[64:67], v[44:47]\n"
    "buffer_load_dwordx4 v19, s[36:39], s84 offen offset:3072 lds\n"
    "v_mfma_f32_16x16x32_bf16 v[48:51], a[32:35], a[64:67], v[48:51]\n"
    "ds_read_b64_tr_b16 a[62:63], v13 offset:37632\n"
    "v_mfma_f32_16x16x32_bf16 v[48:51], a[36:39], a[68:71], v[48:51]\n"
    "ds_read_b128 a[80:83], v8 offset:45056\n"
    "v_mfma_f32_16x16x32_bf16 v[72:75], a[36:39], a[76:79], v[72:75]\n"
    "s_add_u32 s84, s84, 128\n"
    "s_add_u32 m0, s52, 4096\n"
    "v_mfma_f32_16x16x32_bf16 v[72:75], a[32:35], a[72:75], v[72:75]\n"
    "buffer_load_dwordx4 v16, s[36:39], s85 offen lds\n"
    "v_mfma_f32_16x16x32_bf16 v[76:79], a[40:43], a[72:75], v[76:79]\n"
    "ds_read_b128 a[84:87], v10 offset:45056\n"
    "s_waitcnt lgkmcnt(6)\n"
    "v_mfma_f32_16x16x32_bf16 v[76:79], a[44:47], a[76:79], v[76:79]\n"
    "ds_read_b128 a[88:91], v8 offset:49152\n"
    "v_mfma_f32_16x16x32_bf16 v[52:55], a[44:47], a[68:71], v[52:55]\n"
    "ds_read_b128 a[92:95], v10 offset:49152\n"
    "v_mfma_f32_16x16x32_bf16 v[52:55], a[40:43], a[64:67], v[52:55]\n"
    "ds_read_b128 a[96:99], v9 offset:36864\n"
    "v_mfma_f32_16x16x32_bf16 v[224:227], a[48:51], a[64:67], v[224:227]\n"
    "buffer_load_dwordx4 v17, s[36:39], s85 offen offset:1024 lds\n"
    "v_mfma_f32_16x16x32_bf16 v[224:227], a[52:55], a[68:71], v[224:227]\n"
    "ds_read_b128 a[100:103], v11 offset:36864\n"
    "v_mfma_f32_16x16x32_bf16 v[232:235], a[52:55], a[76:79], v[232:235]\n"
    "s_cmp_eq_u32 s88, 5\n"
    "s_cselect_b32 s90, 1073741824, s90\n"
    "v_mfma_f32_16x16x32_bf16 v[232:235], a[48:51], a[72:75], v[232:235]\n"
    "ds_read_b128 a[104:107], v9 offset:40960\n"
    "s_waitcnt lgkmcnt(6)\n"
    "v_mfma_f32_16x16x32_bf16 v[236:239], a[56:59], a[72:75], v[236:239]\n"
    "v_mfma_f32_16x16x32_bf16 v[236:239], a[60:63], a[76:79], v[236:239]\n"
    "buffer_load_dwordx4 v18, s[36:39], s85 offen offset:2048 lds\n"
    "v_mfma_f32_16x16x32_bf16 v[228:231], a[60:63], a[68:71], v[228:231]\n"
    "ds_read_b128 a[108:111], v11 offset:40960\n"
    "v_mfma_f32_16x16x32_bf16 v[228:231], a[56:59], a[64:67], v[228:231]\n"
    "v_mfma_f32_16x16x32_bf16 v[244:247], a[56:59], a[80:83], v[244:247]\n"
    "ds_read_b128 a[112:115], v9 offset:45056\n"
    "s_waitcnt lgkmcnt(6)\n"
    "v_mfma_f32_16x16x32_bf16 v[244:247], a[60:63], a[84:87], v[244:247]\n"
    "buffer_load_dwordx4 v19, s[36:39], s85 offen offset:3072 lds\n"
    "s_waitcnt lgkmcnt(5)\n"
    "v_mfma_f32_16x16x32_bf16 v[252:255], a[60:63], a[92:95], v[252:255]\n"
    "ds_read_b128 a[116:119], v11 offset:45056\n"
    "v_mfma_f32_16x16x32_bf16 v[252:255], a[56:59], a[88:91], v[252:255]\n"
    "v_mfma_f32_16x16x32_bf16 v[248:251], a[48:51], a[88:91], v[248:251]\n"
    "v_mfma_f32_16x16x32_bf16 v[248:251], a[52:55], a[92:95], v[248:251]\n"
    "s_add_u32 s85, s85, 128\n"
    "s_add_u32 m0, s54, 0\n"
    "v_mfma_f32_16x16x32_bf16 v[240:243], a[52:55], a[84:87], v[240:243]\n"
    "buffer_load_dwordx4 v23, s[40:43], s87 offen lds\n"
    "v_mfma_f32_16x16x32_bf16 v[240:243], a[48:51], a[80:83], v[240:243]\n"
    "s_add_u32 s87, s87, s55\n"
    "s_add_u32 s87, s87, s55\n"
    "v_mfma_f32_16x16x32_bf16 v[100:103], a[40:43], a[80:83], v[100:103]\n"
    "ds_read_b128 a[120:123], v9 offset:49152\n"
    "v_mfma_f32_16x16x32_bf16 v[100:103], a[44:47], a[84:87], v[100:103]\n"
    "ds_read_b128 a[124:127], v11 offset:49152\n"
    "v_mfma_f32_16x16x32_bf16 v[124:127], a[44:47], a[92:95], v[124:127]\n"
    "v_mfma_f32_16x16x32_bf16 v[124:127], a[40:43], a[88:91], v[124:127]\n"
    "v_mfma_f32_16x16x32_bf16 v[120:123], a[32:35], a[88:91], v[120:123]\n"
    "v_mfma_f32_16x16x32_bf16 v[120:123], a[36:39], a[92:95], v[120:123]\n"
    "v_mfma_f32_16x16x32_bf16 v[96:99], a[36:39], a[84:87], v[96:99]\n"
    "v_mfma_f32_16x16x32_bf16 v[96:99], a[32:35], a[80:83], v[96:99]\n"
    "s_waitcnt vmcnt(9) lgkmcnt(0)\n"
    "s_barrier\n"
    "v_mfma_f32_16x16x32_bf16 v[92:95], a[24:27], a[80:83], v[92:95]\n"
    "s_add_u32 m0, s53, 24576\n"
    "v_mfma_f32_16x16x32_bf16 v[92:95], a[28:31], a[84:87], v[92:95]\n"
    "buffer_load_dwordx4 v20, s[40:43], s86 offen lds\n"
    "v_mfma_f32_16x16x32_bf16 v[116:119], a[28:31], a[92:95], v[116:119]\n"
    "v_mfma_f32_16x16x32_bf16 v[116:119], a[24:27], a[88:91], v[116:119]\n"
    "v_mfma_f32_16x16x32_bf16 a[128:131], a[132:135], a[136:139], a[128:131]\n"
    "v_mfma_f32_16x16x32_bf16 v[112:115], a[16:19], a[88:91], v[112:115]\n"
    "v_mfma_f32_16x16x32_bf16 v[112:115], a[20:23], a[92:95], v[112:115]\n"
    "buffer_load_dwordx4 v21, s[40:43], s86 offen offset:1024 lds\n"
    "v_mfma_f32_16x16x32_bf16 v[88:91], a[20:23], a[84:87], v[88:91]\n"
    "v_mfma_f32_16x16x32_bf16 v[88:91], a[16:19], a[80:83], v[88:91]\n"
    "v_mfma_f32_16x16x32_bf16 v[84:87], a[8:11], a[80:83], v[84:87]\n"
    "v_mfma_f32_16x16x32_bf16 v[84:87], a[12:15], a[84:87], v[84:87]\n"
    "buffer_load_dwordx4 v22, s[40:43], s86 offen offset:2048 lds\n"
    "v_mfma_f32_16x16x32_bf16 v[108:111], a[12:15], a[92:95], v[108:111]\n"
    "v_mfma_f32_16x16x32_bf16 v[108:111], a[8:11], a[88:91], v[108:111]\n"
    "v_mfma_f32_16x16x32_bf16 a[128:131], a[132:135], a[140:143], a[128:131]\n"
    "v_mfma_f32_16x16x32_bf16 v[104:107], a[0:3], a[88:91], v[104:107]\n"
    "s_add_u32 s86, s86, s55\n"
    "s_add_u32 m0, s53, 36864\n"
    "v_mfma_f32_16x16x32_bf16 v[104:107], a[4:7], a[92:95], v[104:107]\n"
    "buffer_load_dwordx4 v20, s[40:43], s86 offen lds\n"
    "v_mfma_f32_16x16x32_bf16 v[80:83], a[4:7], a[84:87], v[80:83]\n"
    "v_mfma_f32_16x16x32_bf16 v[80:83], a[0:3], a[80:83], v[80:83]\n"
    "v_mfma_f32_16x16x32_bf16 v[148:151], a[40:43], a[96:99], v[148:151]\n"
    "v_mfma_f32_16x16x32_bf16 v[148:151], a[44:47], a[100:103], v[148:151]\n"
    "buffer_load_dwordx4 v21, s[40:43], s86 offen offset:1024 lds\n"
    "v_mfma_f32_16x16x32_bf16 v[172:175], a[44:47], a[108:111], v[172:175]\n"
    "v_mfma_f32_16x16x32_bf16 v[172:175], a[40:43], a[104:107], v[172:175]\n"
    "v_mfma_f32_16x16x32_bf16 v[168:171], a[32:35], a[104:107], v[168:171]\n"
    "v_mfma_f32_16x16x32_bf16 v[168:171], a[36:39], a[108:111], v[168:171]\n"
    "buffer_load_dwordx4 v22, s[40:43], s86 offen offset:2048 lds\n"
    "v_mfma_f32_16x16x32_bf16 v[144:147], a[36:39], a[100:103], v[144:147]\n"
    "s_add_u32 s86, s86, s55\n"
    "v_mfma_f32_16x16x32_bf16 v[144:147], a[32:35], a[96:99], v[144:147]\n"
    "v_mfma_f32_16x16x32_bf16 v[140:143], a[24:27], a[96:99], v[140:143]\n"
    "v_mfma_f32_16x16x32_bf16 v[140:143], a[28:31], a[100:103], v[140:143]\n"
    "buffer_load_dword a220, v28, s[48:51], s34 offen\n"
    "v_mfma_f32_16x16x32_bf16 v[164:167], a[28:31], a[108:111], v[164:167]\n"
    "s_add_u32 s34, s34, 4096\n"
    "v_mfma_f32_16x16x32_bf16 v[164:167], a[24:27], a[104:107], v[164:167]\n"
    "v_mfma_f32_16x16x32_bf16 v[160:163], a[16:19], a[104:107], v[160:163]\n"
    "v_mfma_f32_16x16x32_bf16 v[160:163], a[20:23], a[108:111], v[160:163]\n"
    "v_mfma_f32_16x16x32_bf16 v[136:139], a[20:23], a[100:103], v[136:139]\n"
    "v_mfma_f32_16x16x32_bf16 v[136:139], a[16:19], a[96:99], v[136:139]\n"
    "v_mfma_f32_16x16x32_bf16 v[132:135], a[8:11], a[96:99], v[132:135]\n"
    "v_mfma_f32_16x16x32_bf16 v[132:135], a[12:15], a[100:103], v[132:135]\n"
    "v_mfma_f32_16x16x32_bf16 v[156:159], a[12:15], a[108:111], v[156:159]\n"
    "v_mfma_f32_16x16x32_bf16 v[156:159], a[8:11], a[104:107], v[156:159]\n"
    "v_mfma_f32_16x16x32_bf16 v[152:155], a[0:3], a[104:107], v[152:155]\n"
    "v_mfma_f32_16x16x32_bf16 v[152:155], a[4:7], a[108:111], v[152:155]\n"
    "v_mfma_f32_16x16x32_bf16 v[128:131], a[4:7], a[100:103], v[128:131]\n"
    "v_mfma_f32_16x16x32_bf16 v[128:131], a[0:3], a[96:99], v[128:131]\n"
    "s_waitcnt vmcnt(7)\n"
    "s_barrier\n"
    "v_mfma_f32_16x16x32_bf16 v[176:179], a[0:3], a[112:115], v[176:179]\n"
    "ds_read_b128 a[64:67], v8 offset:0\n"
    "v_mfma_f32_16x16x32_bf16 v[176:179], a[4:7], a[116:119], v[176:179]\n"
    "ds_read_b128 a[68:71], v10 offset:0\n"
    "v_mfma_f32_16x16x32_bf16 v[200:203], a[4:7], a[124:127], v[200:203]\n"
    "ds_read_b128 a[72:75], v8 offset:4096\n"
    "v_mfma_f32_16x16x32_bf16 v[200:203], a[0:3], a[120:123], v[200:203]\n"
    "ds_read_b128 a[76:79], v10 offset:4096\n"
    "v_mfma_f32_16x16x32_bf16 v[204:207], a[8:11], a[120:123], v[204:207]\n"
    "ds_read_b64_tr_b16 a[0:1], v14 offset:0\n"
    "v_mfma_f32_16x16x32_bf16 v[204:207], a[12:15], a[124:127], v[204:207]\n"
    "ds_read_b64_tr_b16 a[2:3], v14 offset:512\n"
    "v_mfma_f32_16x16x32_bf16 v[180:183], a[12:15], a[116:119], v[180:183]\n"
    "ds_read_b64_tr_b16 a[4:5], v14 offset:12288\n"
    "v_mfma_f32_16x16x32_bf16 v[180:183], a[8:11], a[112:115], v[180:183]\n"
    ".Lk2_m1246:\n"
    ".Lk2_hw1:\n"
    "s_cbranch_execz .Lk2_m8695\n"
    "ds_read_b64_tr_b16 a[6:7], v14 offset:12800\n"
    "v_mfma_f32_16x16x32_bf16 v[184:187], a[16:19], a[112:115], v[184:187]\n"
    "ds_read_b64_tr_b16 a[8:9], v15 offset:0\n"
    "v_mfma_f32_16x16x32_bf16 v[184:187], a[20:23], a[116:119], v[184:187]\n"
    "ds_read_b64_tr_b16 a[10:11], v15 offset:512\n"
    "v_mfma_f32_16x16x32_bf16 v[208:211], a[20:23], a[124:127], v[208:211]\n"
    "ds_read_b64_tr_b16 a[12:13], v15 offset:12288\n"
    "v_mfma_f32_16x16x32_bf16 v[208:211], a[16:19], a[120:123], v[208:211]\n"
    "ds_read_b64_tr_b16 a[14:15], v15 offset:12800\n"
    "v_mfma_f32_16x16x32_bf16 v[212:215], a[24:27], a[120:123], v[212:215]\n"
    "ds_read_b64_tr_b16 a[16:17], v14 offset:1024\n"
    "v_mfma_f32_16x16x32_bf16 v[212:215], a[28:31], a[124:127], v[212:215]\n"
    "ds_read_b64_tr_b16 a[18:19], v14 offset:1536\n"
    "v_mfma_f32_16x16x32_bf16 v[188:191], a[28:31], a[116:119], v[188:191]\n"
    "ds_read_b64_tr_b16 a[20:21], v14 offset:13312\n"
    "v_mfma_f32_16x16x32_bf16 v[188:191], a[24:27], a[112:115], v[188:191]\n"
    "ds_read_b64_tr_b16 a[22:23], v14 offset:13824\n"
    "v_mfma_f32_16x16x32_bf16 v[192:195], a[32:35], a[112:115], v[192:195]\n"
    "ds_read_b64_tr_b16 a[24:25], v15 offset:1024\n"
    "v_mfma_f32_16x16x32_bf16 v[192:195], a[36:39], a[116:119], v[192:195]\n"
    "ds_read_b64_tr_b16 a[26:27], v15 offset:1536\n"
    "v_mfma_f32_16x16x32_bf16 v[216:219], a[36:39], a[124:127], v[216:219]\n"
    "ds_read_b64_tr_b16 a[28:29], v15 offset:13312\n"
    "v_mfma_f32_16x16x32_bf16 v[216:219], a[32:35], a[120:123], v[216:219]\n"
    "ds_read_b64_tr_b16 a[30:31], v15 offset:13824\n"
    "v_mfma_f32_16x16x32_bf16 v[220:223], a[40:43], a[120:123], v[220:223]\n"
    "ds_read_b64_tr_b16 a[32:33], v14 offset:2048\n"
    "v_mfma_f32_16x16x32_bf16 v[220:223], a[44:47], a[124:127], v[220:223]\n"
    "ds_read_b64_tr_b16 a[34:35], v14 offset:2560\n"
    "s_add_u32 s88, s88, 1\n"
    "v_mfma_f32_16x16x32_bf16 v[196:199], a[44:47], a[116:119], v[196:199]\n"
    "ds_read_b64_tr_b16 a[36:37], v14 offset:14336\n"
    "s_cmp_lt_u32 s88, 6\n"
    "v_mfma_f32_16x16x32_bf16 v[196:199], a[40:43], a[112:115], v[196:199]\n"
    "ds_read_b64_tr_b16 a[38:39], v14 offset:14848\n"
    "s_cbranch_scc1 .Lk2_wloop\n"
    "s_cmp_eq_u32 s88, 6\n"
    "s_cbranch_scc1 .Lk2_wpub\n"
    "s_waitcnt lgkmcnt(6)\n"
    "v_mfma_f32_16x16x32_bf16 v[32:35], a[0:3], a[64:67], v[32:35]\n"
    "s_add_u32 m0, s52, 36864\n"
    "ds_read_b64_tr_b16 a[40:41], v15 offset:2048\n"
    "v_mfma_f32_16x16x32_bf16 v[32:35], a[4:7], a[68:71], v[32:35]\n"
    "buffer_load_dwordx4 v16, s[36:39], s84 offen lds\n"
    "v_mfma_f32_16x16x32_bf16 v[56:59], a[4:7], a[76:79], v[56:59]\n"
    "ds_read_b64_tr_b16 a[42:43], v15 offset:2560\n"
    "v_mfma_f32_16x16x32_bf16 v[56:59], a[0:3], a[72:75], v[56:59]\n"
    "ds_read_b64_tr_b16 a[44:45], v15 offset:14336\n"
    "v_mfma_f32_16x16x32_bf16 v[60:63], a[8:11], a[72:75], v[60:63]\n"
    "ds_read_b64_tr_b16 a[46:47], v15 offset:14848\n"
    "v_mfma_f32_16x16x32_bf16 v[60:63], a[12:15], a[76:79], v[60:63]\n"
    "buffer_load_dwordx4 v17, s[36:39], s84 offen offset:1024 lds\n"
    "v_mfma_f32_16x16x32_bf16 v[36:39], a[12:15], a[68:71], v[36:39]\n"
    "ds_read_b64_tr_b16 a[48:49], v12 offset:0\n"
    "v_mfma_f32_16x16x32_bf16 v[36:39], a[8:11], a[64:67], v[36:39]\n"
    "ds_read_b64_tr_b16 a[50:51], v12 offset:256\n"
    "v_mfma_f32_16x16x32_bf16 v[40:43], a[16:19], a[64:67], v[40:43]\n"
    "ds_read_b64_tr_b16 a[52:53], v12 offset:512\n"
    "v_mfma_f32_16x16x32_bf16 v[40:43], a[20:23], a[68:71], v[40:43]\n"
    "buffer_load_dwordx4 v18, s[36:39], s84 offen offset:2048 lds\n"
    "v_mfma_f32_16x16x32_bf16 v[64:67], a[20:23], a[76:79], v[64:67]\n"
    "ds_read_b64_tr_b16 a[54:55], v12 offset:768\n"
    "v_mfma_f32_16x16x32_bf16 v[64:67], a[16:19], a[72:75], v[64:67]\n"
    "ds_read_b64_tr_b16 a[56:57], v13 offset:0\n"
    "v_mfma_f32_16x16x32_bf16 v[68:71], a[24:27], a[72:75], v[68:71]\n"
    "ds_read_b64_tr_b16 a[58:59], v13 offset:256\n"
    "s_waitcnt lgkmcnt(6)\n"
    "v_mfma_f32_16x16x32_bf16 v[68:71], a[28:31], a[76:79], v[68:71]\n"
    "buffer_load_dwordx4 v19, s[36:39], s84 offen offset:3072 lds\n"
    "v_mfma_f32_16x16x32_bf16 v[44:47], a[28:31], a[68:71], v[44:47]\n"
    "ds_read_b64_tr_b16 a[60:61], v13 offset:512\n"
    "v_mfma_f32_16x16x32_bf16 v[44:47], a[24:27], a[64:67], v[44:47]\n"
    "ds_read_b64_tr_b16 a[62:63], v13 offset:768\n"
    "v_mfma_f32_16x16x32_bf16 v[48:51], a[32:35], a[64:67], v[48:51]\n"
    "s_add_u32 s84, s84, 128\n"
    "s_add_u32 m0, s52, 40960\n"
    "v_mfma_f32_16x16x32_bf16 v[48:51], a[36:39], a[68:71], v[48:51]\n"
    "buffer_load_dwordx4 v16, s[36:39], s85 offen lds\n"
    "v_mfma_f32_16x16x32_bf16 v[72:75], a[36:39], a[76:79], v[72:75]\n"
    "ds_read_b128 a[80:83], v8 offset:8192\n"
    "v_mfma_f32_16x16x32_bf16 v[72:75], a[32:35], a[72:75], v[72:75]\n"
    "ds_read_b128 a[84:87], v10 offset:8192\n"
    "v_mfma_f32_16x16x32_bf16 v[76:79], a[40:43], a[72:75], v[76:79]\n"
    "ds_read_b128 a[88:91], v8 offset:12288\n"
    "v_mfma_f32_16x16x32_bf16 v[76:79], a[44:47], a[76:79], v[76:79]\n"
    "buffer_load_dwordx4 v17, s[36:39], s85 offen offset:1024 lds\n"
    "v_mfma_f32_16x16x32_bf16 v[52:55], a[44:47], a[68:71], v[52:55]\n"
    "ds_read_b128 a[92:95], v10 offset:12288\n"
    "v_mfma_f32_16x16x32_bf16 v[52:55], a[40:43], a[64:67], v[52:55]\n"
    "ds_read_b128 a[96:99], v9 offset:0\n"
    "s_waitcnt lgkmcnt(6)\n"
    "v_mfma_f32_16x16x32_bf16 v[224:227], a[48:51], a[64:67], v[224:227]\n"
    "v_mfma_f32_16x16x32_bf16 v[224:227], a[52:55], a[68:71], v[224:227]\n"
    "buffer_load_dwordx4 v18, s[36:39], s85 offen offset:2048 lds\n"
    "v_mfma_f32_16x16x32_bf16 v[232:235], a[52:55], a[76:79], v[232:235]\n"
    "ds_read_b128 a[100:103], v11 offset:0\n"
    "v_mfma_f32_16x16x32_bf16 v[232:235], a[48:51], a[72:75], v[232:235]\n"
    "ds_read_b128 a[104:107], v9 offset:4096\n"
    "v_mfma_f32_16x16x32_bf16 v[236:239], a[56:59], a[72:75], v[236:239]\n"
    "s_waitcnt lgkmcnt(6)\n"
    "v_mfma_f32_16x16x32_bf16 v[236:239], a[60:63], a[76:79], v[236:239]\n"
    "buffer_load_dwordx4 v19, s[36:39], s85 offen offset:3072 lds\n"
    "v_mfma_f32_16x16x32_bf16 v[228:231], a[60:63], a[68:71], v[228:231]\n"
    "ds_read_b128 a[108:111], v11 offset:4096\n"
    "v_mfma_f32_16x16x32_bf16 v[228:231], a[56:59], a[64:67], v[228:231]\n"
    "v_mfma_f32_16x16x32_bf16 v[244:247], a[56:59], a[80:83], v[244:247]\n"
    "s_add_u32 s85, s85, 128\n"
    "s_add_u32 m0, s54, 36864\n"
    "s_waitcnt lgkmcnt(6)\n"
    "v_mfma_f32_16x16x32_bf16 v[244:247], a[60:63], a[84:87], v[244:247]\n"
    "buffer_load_dwordx4 v23, s[40:43], s87 offen lds\n"
    "s_waitcnt lgkmcnt(4)\n"
    "v_mfma_f32_16x16x32_bf16 v[252:255], a[60:63], a[92:95], v[252:255]\n"
    "s_add_u32 s87, s87, s55\n"
    "s_add_u32 s87, s87, s55\n"
    "v_mfma_f32_16x16x32_bf16 v[252:255], a[56:59], a[88:91], v[252:255]\n"
    "v_lshrrev_b32 v26, 1, v25\n"
    "s_add_u32 s93, s77, 4096\n"
    "v_mfma_f32_16x16x32_bf16 v[248:251], a[48:51], a[88:91], v[248:251]\n"
    "buffer_load_dwordx2 v[0:1], v26, s[44:47], s77 offen sc1\n"
    "v_mfma_f32_16x16x32_bf16 v[248:251], a[52:55], a[92:95], v[248:251]\n"
    "ds_read_b128 a[112:115], v9 offset:8192\n"
    "v_mfma_f32_16x16x32_bf16 v[240:243], a[52:55], a[84:87], v[240:243]\n"
    "buffer_load_dwordx2 v[2:3], v26, s[44:47], s77 offen offset:2048 sc1\n"
    "v_mfma_f32_16x16x32_bf16 v[240:243], a[48:51], a[80:83], v[240:243]\n"
    "s_add_u32 s94, s77, 8192\n"
    "ds_read_b128 a[116:119], v11 offset:8192\n"
    "v_mfma_f32_16x16x32_bf16 v[100:103], a[40:43], a[80:83], v[100:103]\n"
    "buffer_load_dwordx2 v[4:5], v26, s[44:47], s93 offen sc1\n"
    "v_mfma_f32_16x16x32_bf16 v[100:103], a[44:47], a[84:87], v[100:103]\n"
    "ds_read_b128 a[120:123], v9 offset:12288\n"
    "v_mfma_f32_16x16x32_bf16 v[124:127], a[44:47], a[92:95], v[124:127]\n"
    "buffer_load_dwordx2 v[6:7], v26, s[44:47], s93 offen offset:2048 sc1\n"
    "v_mfma_f32_16x16x32_bf16 v[124:127], a[40:43], a[88:91], v[124:127]\n"
    "s_add_u32 s93, s77, 12288\n"
    "ds_read_b128 a[124:127], v11 offset:12288\n"
    "v_mfma_f32_16x16x32_bf16 v[120:123], a[32:35], a[88:91], v[120:123]\n"
    "buffer_load_dwordx2 v[16:17], v26, s[44:47], s94 offen sc1\n"
    "v_mfma_f32_16x16x32_bf16 v[120:123], a[36:39], a[92:95], v[120:123]\n"
    "v_mfma_f32_16x16x32_bf16 v[96:99], a[36:39], a[84:87], v[96:99]\n"
    "buffer_load_dwordx2 v[18:19], v26, s[44:47], s94 offen offset:2048 sc1\n"
    "v_mfma_f32_16x16x32_bf16 v[96:99], a[32:35], a[80:83], v[96:99]\n"
    "s_waitcnt lgkmcnt(0)\n"
    "s_barrier\n"
    "v_mfma_f32_16x16x32_bf16 v[92:95], a[24:27], a[80:83], v[92:95]\n"
    "v_mfma_f32_16x16x32_bf16 v[92:95], a[28:31], a[84:87], v[92:95]\n"
    "buffer_load_dwordx2 v[28:29], v26, s[44:47], s93 offen sc1\n"
    "v_mfma_f32_16x16x32_bf16 v[116:119], a[28:31], a[92:95], v[116:119]\n"
    "v_mfma_f32_16x16x32_bf16 v[116:119], a[24:27], a[88:91], v[116:119]\n"
    "buffer_load_dwordx2 v[30:31], v26, s[44:47], s93 offen offset:2048 sc1\n"
    "v_mfma_f32_16x16x32_bf16 v[112:115], a[16:19], a[88:91], v[112:115]\n"
    "v_mfma_f32_16x16x32_bf16 v[112:115], a[20:23], a[92:95], v[112:115]\n"
    "buffer_load_dwordx4 v[20:23], v25, s[44:47], s79 offen sc1\n"
    "v_mfma_f32_16x16x32_bf16 v[88:91], a[20:23], a[84:87], v[88:91]\n"
    "v_accvgpr_read_b32 v24, a176\n"
    "v_mfma_f32_16x16x32_bf16 v[88:91], a[16:19], a[80:83], v[88:91]\n"
    "global_load_ushort v24, v24, s[72:73]\n"
    "v_mfma_f32_16x16x32_bf16 v[84:87], a[8:11], a[80:83], v[84:87]\n"
    "s_lshr_b32 s94, s52, 5\n"
    "v_accvgpr_read_b32 v27, a169\n"
    "s_add_u32 m0, s94, 136192\n"
    "v_mfma_f32_16x16x32_bf16 v[84:87], a[12:15], a[84:87], v[84:87]\n"
    "global_load_lds_dword v27, s[74:75]\n"
    "v_mfma_f32_16x16x32_bf16 v[108:111], a[12:15], a[92:95], v[108:111]\n"
    "s_mov_b64 s[48:49], s[28:29]\n"
    "v_accvgpr_read_b32 v27, a198\n"
    "v_mfma_f32_16x16x32_bf16 v[108:111], a[8:11], a[88:91], v[108:111]\n"
    "s_and_b32 s91, s35, 4\n"
    "v_mfma_f32_16x16x32_bf16 v[104:107], a[0:3], a[88:91], v[104:107]\n"
    "v_readlane_b32 s84, v27, 0\n"
    "v_readlane_b32 s85, v27, 1\n"
    "v_mfma_f32_16x16x32_bf16 v[104:107], a[4:7], a[92:95], v[104:107]\n"
    "v_readlane_b32 s87, v27, 2\n"
    "v_mfma_f32_16x16x32_bf16 v[80:83], a[4:7], a[84:87], v[80:83]\n"
    "v_readlane_b32 s89, v27, 3\n"
    "s_lshl_b32 s91, s91, 6\n"
    "v_mfma_f32_16x16x32_bf16 v[80:83], a[0:3], a[80:83], v[80:83]\n"
    "s_add_u32 s91, s91, 896\n"
    "v_mfma_f32_16x16x32_bf16 v[148:151], a[40:43], a[96:99], v[148:151]\n"
    "v_accvgpr_read_b32 v27, a165\n"
    "v_mfma_f32_16x16x32_bf16 v[148:151], a[44:47], a[100:103], v[148:151]\n"
    "v_mfma_f32_16x16x32_bf16 v[172:175], a[44:47], a[108:111], v[172:175]\n"
    "s_add_u32 s25, s84, s91\n"
    "s_add_u32 m0, s52, 0\n"
    "v_mfma_f32_16x16x32_bf16 v[172:175], a[40:43], a[104:107], v[172:175]\n"
    "buffer_load_dwordx4 v27, s[48:51], s25 offen lds\n"
    "v_mfma_f32_16x16x32_bf16 v[168:171], a[32:35], a[104:107], v[168:171]\n"
    "v_mfma_f32_16x16x32_bf16 v[168:171], a[36:39], a[108:111], v[168:171]\n"
    "v_mfma_f32_16x16x32_bf16 v[144:147], a[36:39], a[100:103], v[144:147]\n"
    "v_mfma_f32_16x16x32_bf16 v[144:147], a[32:35], a[96:99], v[144:147]\n"
    "buffer_load_dwordx4 v27, s[48:51], s84 offen offset:1024 lds\n"
    "v_mfma_f32_16x16x32_bf16 v[140:143], a[24:27], a[96:99], v[140:143]\n"
    "v_mfma_f32_16x16x32_bf16 v[140:143], a[28:31], a[100:103], v[140:143]\n"
    "v_mfma_f32_16x16x32_bf16 v[164:167], a[28:31], a[108:111], v[164:167]\n"
    "v_mfma_f32_16x16x32_bf16 v[164:167], a[24:27], a[104:107], v[164:167]\n"
    "s_xor_b32 s84, s84, 0x10000\n"
    "s_add_u32 s25, s84, s91\n"
    "s_add_u32 m0, s52, 2048\n"
    "v_mfma_f32_16x16x32_bf16 v[160:163], a[16:19], a[104:107], v[160:163]\n"
    "buffer_load_dwordx4 v27, s[48:51], s25 offen lds\n"
    "v_mfma_f32_16x16x32_bf16 v[160:163], a[20:23], a[108:111], v[160:163]\n"
    "v_mfma_f32_16x16x32_bf16 v[136:139], a[20:23], a[100:103], v[136:139]\n"
    "v_mfma_f32_16x16x32_bf16 v[136:139], a[16:19], a[96:99], v[136:139]\n"
    "v_mfma_f32_16x16x32_bf16 v[132:135], a[8:11], a[96:99], v[132:135]\n"
    "buffer_load_dwordx4 v27, s[48:51], s84 offen offset:1024 lds\n"
    "v_mfma_f32_16x16x32_bf16 v[132:135], a[12:15], a[100:103], v[132:135]\n"
    "s_xor_b32 s84, s84, 0x10000\n"
    "v_mfma_f32_16x16x32_bf16 v[156:159], a[12:15], a[108:111], v[156:159]\n"
    "v_mfma_f32_16x16x32_bf16 v[156:159], a[8:11], a[104:107], v[156:159]\n"
    "v_mfma_f32_16x16x32_bf16 v[152:155], a[0:3], a[104:107], v[152:155]\n"
    "v_mfma_f32_16x16x32_bf16 v[152:155], a[4:7], a[108:111], v[152:155]\n"
    "v_mfma_f32_16x16x32_bf16 v[128:131], a[4:7], a[100:103], v[128:131]\n"
    "v_mfma_f32_16x16x32_bf16 v[128:131], a[0:3], a[96:99], v[128:131]\n"
    "s_waitcnt vmcnt(15)\n"
    "s_barrier\n"
    "v_mfma_f32_16x16x32_bf16 v[176:179], a[0:3], a[112:115], v[176:179]\n"
    "ds_read_b128 a[64:67], v8 offset:36864\n"
    "v_mfma_f32_16x16x32_bf16 v[176:179], a[4:7], a[116:119], v[176:179]\n"
    "ds_read_b128 a[68:71], v10 offset:36864\n"
    "v_mfma_f32_16x16x32_bf16 v[200:203], a[4:7], a[124:127], v[200:203]\n"
    "ds_read_b128 a[72:75], v8 offset:40960\n"
    "v_mfma_f32_16x16x32_bf16 v[200:203], a[0:3], a[120:123], v[200:203]\n"
    "ds_read_b128 a[76:79], v10 offset:40960\n"
    "v_mfma_f32_16x16x32_bf16 v[204:207], a[8:11], a[120:123], v[204:207]\n"
    "ds_read_b64_tr_b16 a[0:1], v14 offset:24576\n"
    "v_mfma_f32_16x16x32_bf16 v[204:207], a[12:15], a[124:127], v[204:207]\n"
    "ds_read_b64_tr_b16 a[2:3], v14 offset:25088\n"
    "v_mfma_f32_16x16x32_bf16 v[180:183], a[12:15], a[116:119], v[180:183]\n"
    "ds_read_b64_tr_b16 a[4:5], v14 offset:36864\n"
    "v_mfma_f32_16x16x32_bf16 v[180:183], a[8:11], a[112:115], v[180:183]\n"
    "ds_read_b64_tr_b16 a[6:7], v14 offset:37376\n"
    "v_mfma_f32_16x16x32_bf16 v[184:187], a[16:19], a[112:115], v[184:187]\n"
    "ds_read_b64_tr_b16 a[8:9], v15 offset:24576\n"
    "v_mfma_f32_16x16x32_bf16 v[184:187], a[20:23], a[116:119], v[184:187]\n"
    "ds_read_b64_tr_b16 a[10:11], v15 offset:25088\n"
    "v_mfma_f32_16x16x32_bf16 v[208:211], a[20:23], a[124:127], v[208:211]\n"
    "ds_read_b64_tr_b16 a[12:13], v15 offset:36864\n"
    "v_mfma_f32_16x16x32_bf16 v[208:211], a[16:19], a[120:123], v[208:211]\n"
    "ds_read_b64_tr_b16 a[14:15], v15 offset:37376\n"
    "v_mfma_f32_16x16x32_bf16 v[212:215], a[24:27], a[120:123], v[212:215]\n"
    "ds_read_b64_tr_b16 a[16:17], v14 offset:25600\n"
    "v_mfma_f32_16x16x32_bf16 v[212:215], a[28:31], a[124:127], v[212:215]\n"
    "ds_read_b64_tr_b16 a[18:19], v14 offset:26112\n"
    "v_mfma_f32_16x16x32_bf16 v[188:191], a[28:31], a[116:119], v[188:191]\n"
    "ds_read_b64_tr_b16 a[20:21], v14 offset:37888\n"
    "v_mfma_f32_16x16x32_bf16 v[188:191], a[24:27], a[112:115], v[188:191]\n"
    "ds_read_b64_tr_b16 a[22:23], v14 offset:38400\n"
    "v_mfma_f32_16x16x32_bf16 v[192:195], a[32:35], a[112:115], v[192:195]\n"
    "ds_read_b64_tr_b16 a[24:25], v15 offset:25600\n"
    "v_mfma_f32_16x16x32_bf16 v[192:195], a[36:39], a[116:119], v[192:195]\n"
    "ds_read_b64_tr_b16 a[26:27], v15 offset:26112\n"
    "v_mfma_f32_16x16x32_bf16 v[216:219], a[36:39], a[124:127], v[216:219]\n"
    "ds_read_b64_tr_b16 a[28:29], v15 offset:37888\n"
    "v_mfma_f32_16x16x32_bf16 v[216:219], a[32:35], a[120:123], v[216:219]\n"
    "ds_read_b64_tr_b16 a[30:31], v15 offset:38400\n"
    "v_mfma_f32_16x16x32_bf16 v[220:223], a[40:43], a[120:123], v[220:223]\n"
    "ds_read_b64_tr_b16 a[32:33], v14 offset:26624\n"
    "v_mfma_f32_16x16x32_bf16 v[220:223], a[44:47], a[124:127], v[220:223]\n"
    "ds_read_b64_tr_b16 a[34:35], v14 offset:27136\n"
    "v_mfma_f32_16x16x32_bf16 v[196:199], a[44:47], a[116:119], v[196:199]\n"
    "ds_read_b64_tr_b16 a[36:37], v14 offset:38912\n"
    "v_mfma_f32_16x16x32_bf16 v[196:199], a[40:43], a[112:115], v[196:199]\n"
    "ds_read_b64_tr_b16 a[38:39], v14 offset:39424\n"
    "s_waitcnt lgkmcnt(6)\n"
    "v_mfma_f32_16x16x32_bf16 v[32:35], a[0:3], a[64:67], v[32:35]\n"
    "ds_read_b64_tr_b16 a[40:41], v15 offset:26624\n"
    "v_mfma_f32_16x16x32_bf16 v[32:35], a[4:7], a[68:71], v[32:35]\n"
    "s_add_u32 s25, s85, s91\n"
    "s_add_u32 m0, s52, 4096\n"
    "s_waitcnt vmcnt(5)\n"
    "v_mfma_f32_16x16x32_bf16 v[56:59], a[4:7], a[76:79], v[56:59]\n"
    "buffer_load_dwordx4 v27, s[48:51], s25 offen lds\n"
    "v_mfma_f32_16x16x32_bf16 v[56:59], a[0:3], a[72:75], v[56:59]\n"
    "ds_read_b64_tr_b16 a[42:43], v15 offset:27136\n"
    "s_add_u32 s93, s92, 1\n"
    "v_mfma_f32_16x16x32_bf16 v[60:63], a[8:11], a[72:75], v[60:63]\n"
    "ds_read_b64_tr_b16 a[44:45], v15 offset:38912\n"
    "v_xor_b32 v21, s93, v21\n"
    "v_mfma_f32_16x16x32_bf16 v[60:63], a[12:15], a[76:79], v[60:63]\n"
    "ds_read_b64_tr_b16 a[46:47], v15 offset:39424\n"
    "v_xor_b32 v23, s93, v23\n"
    "v_mfma_f32_16x16x32_bf16 v[36:39], a[12:15], a[68:71], v[36:39]\n"
    "ds_read_b64_tr_b16 a[48:49], v12 offset:36864\n"
    "v_xor_b32 v1, s93, v1\n"
    "v_mfma_f32_16x16x32_bf16 v[36:39], a[8:11], a[64:67], v[36:39]\n"
    "ds_read_b64_tr_b16 a[50:51], v12 offset:37120\n"
    "v_xor_b32 v3, s93, v3\n"
    "v_mfma_f32_16x16x32_bf16 v[40:43], a[16:19], a[64:67], v[40:43]\n"
    "ds_read_b64_tr_b16 a[52:53], v12 offset:37376\n"
    "v_xor_b32 v5, s93, v5\n"
    "v_mfma_f32_16x16x32_bf16 v[40:43], a[20:23], a[68:71], v[40:43]\n"
    "v_xor_b32 v7, s93, v7\n"
    "v_xor_b32 v17, s93, v17\n"
    "v_mfma_f32_16x16x32_bf16 v[64:67], a[20:23], a[76:79], v[64:67]\n"
    "ds_read_b64_tr_b16 a[54:55], v12 offset:37632\n"
    "v_xor_b32 v19, s93, v19\n"
    "v_mfma_f32_16x16x32_bf16 v[64:67], a[16:19], a[72:75], v[64:67]\n"
    "ds_read_b64_tr_b16 a[56:57], v13 offset:36864\n"
    "v_xor_b32 v29, s93, v29\n"
    "v_mfma_f32_16x16x32_bf16 v[68:71], a[24:27], a[72:75], v[68:71]\n"
    "buffer_load_dwordx4 v27, s[48:51], s85 offen offset:1024 lds\n"
    "s_waitcnt lgkmcnt(6)\n"
    "v_mfma_f32_16x16x32_bf16 v[68:71], a[28:31], a[76:79], v[68:71]\n"
    "ds_read_b64_tr_b16 a[58:59], v13 offset:37120\n"
    "v_xor_b32 v31, s93, v31\n"
    "v_mfma_f32_16x16x32_bf16 v[44:47], a[28:31], a[68:71], v[44:47]\n"
    "ds_read_b64_tr_b16 a[60:61], v13 offset:37376\n"
    "v_or3_b32 v21, v21, v23, v1\n"
    "v_mfma_f32_16x16x32_bf16 v[44:47], a[24:27], a[64:67], v[44:47]\n"
    "ds_read_b64_tr_b16 a[62:63], v13 offset:37632\n"
    "v_or3_b32 v3, v3, v5, v7\n"
    "v_mfma_f32_16x16x32_bf16 v[48:51], a[32:35], a[64:67], v[48:51]\n"
    "v_or3_b32 v17, v17, v19, v29\n"
    "v_or3_b32 v21, v21, v3, v17\n"
    "v_mfma_f32_16x16x32_bf16 v[48:51], a[36:39], a[68:71], v[48:51]\n"
    "ds_read_b128 a[80:83], v8 offset:45056\n"
    "v_or_b32 v21, v21, v31\n"
    "v_mfma_f32_16x16x32_bf16 v[72:75], a[36:39], a[76:79], v[72:75]\n"
    "ds_read_b128 a[84:87], v10 offset:45056\n"
    "v_cmp_ne_u32 vcc, 0, v21\n"
    "v_mfma_f32_16x16x32_bf16 v[72:75], a[32:35], a[72:75], v[72:75]\n"
    "s_cmp_eq_u64 vcc, 0\n"
    "s_cbranch_scc0 .Lk2_wslow\n"
    ".Lk2_wok:\n"
    "v_add_f32 v0, v0, v2\n"
    "v_mfma_f32_16x16x32_bf16 v[76:79], a[40:43], a[72:75], v[76:79]\n"
    "ds_read_b128 a[88:91], v8 offset:49152\n"
    "v_add_f32 v4, v4, v6\n"
    "s_waitcnt lgkmcnt(6)\n"
    "v_mfma_f32_16x16x32_bf16 v[76:79], a[44:47], a[76:79], v[76:79]\n"
    "s_xor_b32 s85, s85, 0x10000\n"
    "s_add_u32 s25, s85, s91\n"
    "s_add_u32 m0, s52, 6144\n"
    "v_mfma_f32_16x16x32_bf16 v[52:55], a[44:47], a[68:71], v[52:55]\n"
    "buffer_load_dwordx4 v27, s[48:51], s25 offen lds\n"
    "v_mfma_f32_16x16x32_bf16 v[52:55], a[40:43], a[64:67], v[52:55]\n"
    "ds_read_b128 a[92:95], v10 offset:49152\n"
    "v_add_f32 v16, v16, v18\n"
    "v_mfma_f32_16x16x32_bf16 v[224:227], a[48:51], a[64:67], v[224:227]\n"
    "ds_read_b128 a[96:99], v9 offset:36864\n"
    "v_add_f32 v28, v28, v30\n"
    "v_mfma_f32_16x16x32_bf16 v[224:227], a[52:55], a[68:71], v[224:227]\n"
    "v_add_f32 v0, v0, v4\n"
    "v_add_f32 v16, v16, v28\n"
    "v_mfma_f32_16x16x32_bf16 v[232:235], a[52:55], a[76:79], v[232:235]\n"
    "ds_read_b128 a[100:103], v11 offset:36864\n"
    "v_add_f32 v0, v0, v16\n"
    "v_mfma_f32_16x16x32_bf16 v[232:235], a[48:51], a[72:75], v[232:235]\n"
    "v_add_f32 v23, 0x3727c5ac, v22\n"
    "v_rsq_f32 v2, v23\n"
    "s_waitcnt lgkmcnt(6)\n"
    "v_mfma_f32_16x16x32_bf16 v[236:239], a[56:59], a[72:75], v[236:239]\n"
    "ds_read_b128 a[104:107], v9 offset:40960\n"
    "v_lshlrev_b32 v24, 16, v24\n"
    "v_mfma_f32_16x16x32_bf16 v[236:239], a[60:63], a[76:79], v[236:239]\n"
    "v_mul_f32 v3, v2, v2\n"
    "v_mov_b32 v5, 0x3727c5ac\n"
    "v_mfma_f32_16x16x32_bf16 v[228:231], a[60:63], a[68:71], v[228:231]\n"
    "ds_read_b128 a[108:111], v11 offset:40960\n"
    "v_fma_f32 v3, v3, v22, v5\n"
    "v_mfma_f32_16x16x32_bf16 v[228:231], a[56:59], a[64:67], v[228:231]\n"
    "v_rsq_f32 v3, v3\n"
    "s_nop 0\n"
    "v_mul_f32 v2, v2, v3\n"
    "s_waitcnt lgkmcnt(6)\n"
    "v_mfma_f32_16x16x32_bf16 v[244:247], a[56:59], a[80:83], v[244:247]\n"
    "buffer_load_dwordx4 v27, s[48:51], s85 offen offset:1024 lds\n"
    "v_mfma_f32_16x16x32_bf16 v[244:247], a[60:63], a[84:87], v[244:247]\n"
    "s_xor_b32 s85, s85, 0x10000\n"
    "ds_read_b128 a[112:115], v9 offset:45056\n"
    "s_waitcnt lgkmcnt(5)\n"
    "v_mfma_f32_16x16x32_bf16 v[252:255], a[60:63], a[92:95], v[252:255]\n"
    "s_lshr_b32 s93, s52, 4\n"
    "v_mul_f32 v3, v2, v20\n"
    "v_add_u32 v26, s93, v26\n"
    "v_mfma_f32_16x16x32_bf16 v[252:255], a[56:59], a[88:91], v[252:255]\n"
    "ds_read_b128 a[116:119], v11 offset:45056\n"
    "v_xor_b32 v3, 0x80000000, v3\n"
    "v_mfma_f32_16x16x32_bf16 v[248:251], a[48:51], a[88:91], v[248:251]\n"
    "v_lshrrev_b32 v4, 1, v26\n"
    "v_add_u32 v26, 131072, v26\n"
    "v_mfma_f32_16x16x32_bf16 v[248:251], a[52:55], a[92:95], v[248:251]\n"
    "ds_read_b128 a[120:123], v9 offset:49152\n"
    "v_add_u32 v4, 133120, v4\n"
    "v_mfma_f32_16x16x32_bf16 v[240:243], a[52:55], a[84:87], v[240:243]\n"
    "ds_write_b64 v26, v[2:3]\n"
    "v_mfma_f32_16x16x32_bf16 v[240:243], a[48:51], a[80:83], v[240:243]\n"
    "ds_read_b128 a[124:127], v11 offset:49152\n"
    "v_mfma_f32_16x16x32_bf16 v[100:103], a[40:43], a[80:83], v[100:103]\n"
    "ds_write_b32 v4, v0\n"
    "v_mfma_f32_16x16x32_bf16 v[100:103], a[44:47], a[84:87], v[100:103]\n"
    "ds_write_b32 v4, v24 offset:1024\n"
    "v_mfma_f32_16x16x32_bf16 v[124:127], a[44:47], a[92:95], v[124:127]\n"
    "v_mfma_f32_16x16x32_bf16 v[124:127], a[40:43], a[88:91], v[124:127]\n"
    "v_mfma_f32_16x16x32_bf16 v[120:123], a[32:35], a[88:91], v[120:123]\n"
    "v_mfma_f32_16x16x32_bf16 v[120:123], a[36:39], a[92:95], v[120:123]\n"
    "v_mfma_f32_16x16x32_bf16 v[96:99], a[36:39], a[84:87], v[96:99]\n"
    "v_mfma_f32_16x16x32_bf16 v[96:99], a[32:35], a[80:83], v[96:99]\n"
    "s_waitcnt lgkmcnt(0)\n"
    "s_barrier\n"
    "v_mfma_f32_16x16x32_bf16 v[92:95], a[24:27], a[80:83], v[92:95]\n"
    "v_accvgpr_read_b32 v20, a171\n"
    "v_mfma_f32_16x16x32_bf16 v[92:95], a[28:31], a[84:87], v[92:95]\n"
    "s_add_u32 s25, s87, s91\n"
    "s_add_u32 m0, s52, 32768\n"
    "v_mfma_f32_16x16x32_bf16 v[116:119], a[28:31], a[92:95], v[116:119]\n"
    "buffer_load_dwordx4 v27, s[48:51], s25 offen lds\n"
    "v_mfma_f32_16x16x32_bf16 v[116:119], a[24:27], a[88:91], v[116:119]\n"
    "v_accvgpr_read_b32 v21, a172\n"
    "v_mfma_f32_16x16x32_bf16 v[112:115], a[16:19], a[88:91], v[112:115]\n"
    "v_mfma_f32_16x16x32_bf16 v[112:115], a[20:23], a[92:95], v[112:115]\n"
    "v_mfma_f32_16x16x32_bf16 v[88:91], a[20:23], a[84:87], v[88:91]\n"
    "v_mfma_f32_16x16x32_bf16 v[88:91], a[16:19], a[80:83], v[88:91]\n"
    "v_accvgpr_read_b32 v22, a173\n"
    "v_mfma_f32_16x16x32_bf16 v[84:87], a[8:11], a[80:83], v[84:87]\n"
    "v_mfma_f32_16x16x32_bf16 v[84:87], a[12:15], a[84:87], v[84:87]\n"
    "buffer_load_dwordx4 v27, s[48:51], s87 offen offset:1024 lds\n"
    "v_mfma_f32_16x16x32_bf16 v[108:111], a[12:15], a[92:95], v[108:111]\n"
    "v_mov_b32 v23, v25\n"
    "v_mfma_f32_16x16x32_bf16 v[108:111], a[8:11], a[88:91], v[108:111]\n"
    "v_mfma_f32_16x16x32_bf16 v[104:107], a[0:3], a[88:91], v[104:107]\n"
    "v_mfma_f32_16x16x32_bf16 v[104:107], a[4:7], a[92:95], v[104:107]\n"
    "v_mfma_f32_16x16x32_bf16 v[80:83], a[4:7], a[84:87], v[80:83]\n"
    "ds_read_b128 v[0:3], v22\n"
    "v_mfma_f32_16x16x32_bf16 v[80:83], a[0:3], a[80:83], v[80:83]\n"
    "v_mfma_f32_16x16x32_bf16 v[148:151], a[40:43], a[96:99], v[148:151]\n"
    "s_xor_b32 s87, s87, 0x10000\n"
    "s_add_u32 s25, s87, s91\n"
    "s_add_u32 m0, s52, 34816\n"
    "v_mfma_f32_16x16x32_bf16 v[148:151], a[44:47], a[100:103], v[148:151]\n"
    "buffer_load_dwordx4 v27, s[48:51], s25 offen lds\n"
    "v_mfma_f32_16x16x32_bf16 v[172:175], a[44:47], a[108:111], v[172:175]\n"
    "ds_read_b128 v[4:7], v22 offset:64\n"
    "v_mfma_f32_16x16x32_bf16 v[172:175], a[40:43], a[104:107], v[172:175]\n"
    "v_mfma_f32_16x16x32_bf16 v[168:171], a[32:35], a[104:107], v[168:171]\n"
    "v_mfma_f32_16x16x32_bf16 v[168:171], a[36:39], a[108:111], v[168:171]\n"
    "ds_read_b128 v[8:11], v22 offset:1024\n"
    "v_mfma_f32_16x16x32_bf16 v[144:147], a[36:39], a[100:103], v[144:147]\n"
    "v_mfma_f32_16x16x32_bf16 v[144:147], a[32:35], a[96:99], v[144:147]\n"
    "v_mfma_f32_16x16x32_bf16 v[140:143], a[24:27], a[96:99], v[140:143]\n"
    "buffer_load_dwordx4 v27, s[48:51], s87 offen offset:1024 lds\n"
    "v_mfma_f32_16x16x32_bf16 v[140:143], a[28:31], a[100:103], v[140:143]\n"
    "ds_read_b128 v[12:15], v22 offset:1088\n"
    "v_mfma_f32_16x16x32_bf16 v[164:167], a[28:31], a[108:111], v[164:167]\n"
    "v_mfma_f32_16x16x32_bf16 v[164:167], a[24:27], a[104:107], v[164:167]\n"
    "v_mfma_f32_16x16x32_bf16 v[160:163], a[16:19], a[104:107], v[160:163]\n"
    "v_mfma_f32_16x16x32_bf16 v[160:163], a[20:23], a[108:111], v[160:163]\n"
    "ds_read2_b64 v[16:19], v20 offset1:32\n"
    "v_mfma_f32_16x16x32_bf16 v[136:139], a[20:23], a[100:103], v[136:139]\n"
    "v_mfma_f32_16x16x32_bf16 v[136:139], a[16:19], a[96:99], v[136:139]\n"
    "s_xor_b32 s87, s87, 0x10000\n"
    "s_add_u32 s25, s89, s91\n"
    "s_add_u32 m0, s52, 36864\n"
    "v_mfma_f32_16x16x32_bf16 v[132:135], a[8:11], a[96:99], v[132:135]\n"
    "buffer_load_dwordx4 v27, s[48:51], s25 offen lds\n"
    "v_mfma_f32_16x16x32_bf16 v[132:135], a[12:15], a[100:103], v[132:135]\n"
    "s_mov_b32 s96, 0xc0135761\n"
    "v_mfma_f32_16x16x32_bf16 v[156:159], a[12:15], a[108:111], v[156:159]\n"
    "v_mfma_f32_16x16x32_bf16 v[156:159], a[8:11], a[104:107], v[156:159]\n"
    "v_mfma_f32_16x16x32_bf16 v[152:155], a[0:3], a[104:107], v[152:155]\n"
    "s_mov_b32 s97, 0xbdd2d3e8\n"
    "v_mfma_f32_16x16x32_bf16 v[152:155], a[4:7], a[108:111], v[152:155]\n"
    "v_mfma_f32_16x16x32_bf16 v[128:131], a[4:7], a[100:103], v[128:131]\n"
    "v_mfma_f32_16x16x32_bf16 v[128:131], a[0:3], a[96:99], v[128:131]\n"
    "buffer_load_dwordx4 v27, s[48:51], s89 offen offset:1024 lds\n"
    "v_mfma_f32_16x16x32_bf16 v[176:179], a[0:3], a[112:115], v[176:179]\n"
    "s_add_u32 s99, s60, s62\n"
    "v_mfma_f32_16x16x32_bf16 v[176:179], a[4:7], a[116:119], v[176:179]\n"
    "v_mfma_f32_16x16x32_bf16 v[200:203], a[4:7], a[124:127], v[200:203]\n"
    "v_mfma_f32_16x16x32_bf16 v[200:203], a[0:3], a[120:123], v[200:203]\n"
    "s_add_u32 s28, s60, s64\n"
    "v_mfma_f32_16x16x32_bf16 v[204:207], a[8:11], a[120:123], v[204:207]\n"
    "v_mfma_f32_16x16x32_bf16 v[204:207], a[12:15], a[124:127], v[204:207]\n"
    "v_mfma_f32_16x16x32_bf16 v[180:183], a[12:15], a[116:119], v[180:183]\n"
    "s_xor_b32 s89, s89, 0x10000\n"
    "s_add_u32 s25, s89, s91\n"
    "s_add_u32 m0, s52, 38912\n"
    "v_mfma_f32_16x16x32_bf16 v[180:183], a[8:11], a[112:115], v[180:183]\n"
    "buffer_load_dwordx4 v27, s[48:51], s25 offen lds\n"
    "v_mfma_f32_16x16x32_bf16 v[184:187], a[16:19], a[112:115], v[184:187]\n"
    "s_add_u32 s29, s61, s64\n"
    "v_mfma_f32_16x16x32_bf16 v[184:187], a[20:23], a[116:119], v[184:187]\n"
    "v_mfma_f32_16x16x32_bf16 v[208:211], a[20:23], a[124:127], v[208:211]\n"
    "v_mfma_f32_16x16x32_bf16 v[208:211], a[16:19], a[120:123], v[208:211]\n"
    "s_add_u32 s30, s60, s65\n"
    "v_mfma_f32_16x16x32_bf16 v[212:215], a[24:27], a[120:123], v[212:215]\n"
    "v_mfma_f32_16x16x32_bf16 v[212:215], a[28:31], a[124:127], v[212:215]\n"
    "v_mfma_f32_16x16x32_bf16 v[188:191], a[28:31], a[116:119], v[188:191]\n"
    "buffer_load_dwordx4 v27, s[48:51], s89 offen offset:1024 lds\n"
    "v_mfma_f32_16x16x32_bf16 v[188:191], a[24:27], a[112:115], v[188:191]\n"
    "s_xor_b32 s89, s89, 0x10000\n"
    "s_add_u32 s34, s60, s63\n"
    "v_mfma_f32_16x16x32_bf16 v[192:195], a[32:35], a[112:115], v[192:195]\n"
    "v_mfma_f32_16x16x32_bf16 v[192:195], a[36:39], a[116:119], v[192:195]\n"
    "v_mfma_f32_16x16x32_bf16 v[216:219], a[36:39], a[124:127], v[216:219]\n"
    "s_add_u32 s86, s61, s63\n"
    "v_mfma_f32_16x16x32_bf16 v[216:219], a[32:35], a[120:123], v[216:219]\n"
    "v_mfma_f32_16x16x32_bf16 v[220:223], a[40:43], a[120:123], v[220:223]\n"
    "v_mfma_f32_16x16x32_bf16 v[220:223], a[44:47], a[124:127], v[220:223]\n"
    "v_mfma_f32_16x16x32_bf16 v[196:199], a[44:47], a[116:119], v[196:199]\n"
    "v_mfma_f32_16x16x32_bf16 v[196:199], a[40:43], a[112:115], v[196:199]\n"
    "s_waitcnt lgkmcnt(0)\n"
    "v_pk_fma_f32 v[24:25], v[0:1], v[16:17], v[8:9] op_sel:[0,1,0] op_sel_hi:[1,1,1]\n"
    "v_pk_fma_f32 v[26:27], v[2:3], v[16:17], v[10:11] op_sel:[0,1,0] op_sel_hi:[1,1,1]\n"
    "v_pk_fma_f32 v[28:29], v[4:5], v[16:17], v[12:13] op_sel:[0,1,0] op_sel_hi:[1,1,1]\n"
    "v_pk_fma_f32 v[30:31], v[6:7], v[16:17], v[14:15] op_sel:[0,1,0] op_sel_hi:[1,1,1]\n"
    "v_pk_fma_f32 v[32:33], v[32:33], v[16:17], v[24:25] op_sel_hi:[1,0,1]\n"
    "v_pk_fma_f32 v[34:35], v[34:35], v[16:17], v[26:27] op_sel_hi:[1,0,1]\n"
    "v_pk_fma_f32 v[36:37], v[36:37], v[16:17], v[28:29] op_sel_hi:[1,0,1]\n"
    "v_pk_fma_f32 v[38:39], v[38:39], v[16:17], v[30:31] op_sel_hi:[1,0,1]\n"
    "v_pk_mul_f32 v[24:25], v[32:33], v[32:33]\n"
    "v_pk_mul_f32 v[26:27], v[34:35], v[34:35]\n"
    "v_pk_mul_f32 v[28:29], v[36:37], v[36:37]\n"
    "v_pk_mul_f32 v[30:31], v[38:39], v[38:39]\n"
    "v_pk_fma_f32 v[24:25], v[24:25], s[96:97], s[96:97] op_sel:[0,1,0] op_sel_hi:[1,1,0]\n"
    "v_pk_fma_f32 v[26:27], v[26:27], s[96:97], s[96:97] op_sel:[0,1,0] op_sel_hi:[1,1,0]\n"
    "v_pk_fma_f32 v[28:29], v[28:29], s[96:97], s[96:97] op_sel:[0,1,0] op_sel_hi:[1,1,0]\n"
    "v_pk_fma_f32 v[30:31], v[30:31], s[96:97], s[96:97] op_sel:[0,1,0] op_sel_hi:[1,1,0]\n"
    "v_pk_mul_f32 v[24:25], v[24:25], v[32:33]\n"
    "v_pk_mul_f32 v[26:27], v[26:27], v[34:35]\n"
    "v_pk_mul_f32 v[28:29], v[28:29], v[36:37]\n"
    "v_pk_mul_f32 v[30:31], v[30:31], v[38:39]\n"
    "v_exp_f32 v24, v24\n"
    "v_exp_f32 v25, v25\n"
    "v_exp_f32 v26, v26\n"
    "v_exp_f32 v27, v27\n"
    "v_exp_f32 v28, v28\n"
    "v_exp_f32 v29, v29\n"
    "v_exp_f32 v30, v30\n"
    "v_exp_f32 v31, v31\n"
    "v_pk_add_f32 v[24:25], v[24:25], 1.0 op_sel_hi:[1,0]\n"
    "v_pk_add_f32 v[26:27], v[26:27], 1.0 op_sel_hi:[1,0]\n"
    "v_pk_add_f32 v[28:29], v[28:29], 1.0 op_sel_hi:[1,0]\n"
    "v_pk_add_f32 v[30:31], v[30:31], 1.0 op_sel_hi:[1,0]\n"
    "v_rcp_f32 v24, v24\n"
    "v_rcp_f32 v25, v25\n"
    "v_rcp_f32 v26, v26\n"
    "v_rcp_f32 v27, v27\n"
    "v_rcp_f32 v28, v28\n"
    "v_rcp_f32 v29, v29\n"
    "v_rcp_f32 v30, v30\n"
    "v_rcp_f32 v31, v31\n"
    "v_pk_mul_f32 v[32:33], v[32:33], v[24:25]\n"
    "v_pk_mul_f32 v[34:35], v[34:35], v[26:27]\n"
    "v_pk_mul_f32 v[36:37], v[36:37], v[28:29]\n"
    "v_pk_mul_f32 v[38:39], v[38:39], v[30:31]\n"
    "v_cvt_pk_bf16_f32 v32, v32, v33\n"
    "v_cvt_pk_bf16_f32 v33, v34, v35\n"
    "v_cvt_pk_bf16_f32 v34, v36, v37\n"
    "v_cvt_pk_bf16_f32 v35, v38, v39\n"
    "buffer_store_dwordx4 v[32:35], v23, s[44:47], s99 offen sc1\n"
    "s_nop 0\n"
    "ds_read2_b64 v[32:35], v20 offset0:64 offset1:96\n"
    "v_pk_fma_f32 v[24:25], v[0:1], v[18:19], v[8:9] op_sel:[0,1,0] op_sel_hi:[1,1,1]\n"
    "v_pk_fma_f32 v[26:27], v[2:3], v[18:19], v[10:11] op_sel:[0,1,0] op_sel_hi:[1,1,1]\n"
    "v_pk_fma_f32 v[28:29], v[4:5], v[18:19], v[12:13] op_sel:[0,1,0] op_sel_hi:[1,1,1]\n"
    "v_pk_fma_f32 v[30:31], v[6:7], v[18:19], v[14:15] op_sel:[0,1,0] op_sel_hi:[1,1,1]\n"
    "v_pk_fma_f32 v[56:57], v[56:57], v[18:19], v[24:25] op_sel_hi:[1,0,1]\n"
    "v_pk_fma_f32 v[58:59], v[58:59], v[18:19], v[26:27] op_sel_hi:[1,0,1]\n"
    "v_pk_fma_f32 v[60:61], v[60:61], v[18:19], v[28:29] op_sel_hi:[1,0,1]\n"
    "v_pk_fma_f32 v[62:63], v[62:63], v[18:19], v[30:31] op_sel_hi:[1,0,1]\n"
    ".Lk2_m1808:\n"
    ".Lk2_hw2:\n"
    "s_cbranch_execz .Lk2_m7320\n"
    "v_pk_mul_f32 v[24:25], v[56:57], v[56:57]\n"
    "v_pk_mul_f32 v[26:27], v[58:59], v[58:59]\n"
    "v_pk_mul_f32 v[28:29], v[60:61], v[60:61]\n"
    "v_pk_mul_f32 v[30:31], v[62:63], v[62:63]\n"
    "v_pk_fma_f32 v[24:25], v[24:25], s[96:97], s[96:97] op_sel:[0,1,0] op_sel_hi:[1,1,0]\n"
    "v_pk_fma_f32 v[26:27], v[26:27], s[96:97], s[96:97] op_sel:[0,1,0] op_sel_hi:[1,1,0]\n"
    "v_pk_fma_f32 v[28:29], v[28:29], s[96:97], s[96:97] op_sel:[0,1,0] op_sel_hi:[1,1,0]\n"
    "v_pk_fma_f32 v[30:31], v[30:31], s[96:97], s[96:97] op_sel:[0,1,0] op_sel_hi:[1,1,0]\n"
    "v_pk_mul_f32 v[24:25], v[24:25], v[56:57]\n"
    "v_pk_mul_f32 v[26:27], v[26:27], v[58:59]\n"
    "v_pk_mul_f32 v[28:29], v[28:29], v[60:61]\n"
    "v_pk_mul_f32 v[30:31], v[30:31], v[62:63]\n"
    "v_exp_f32 v24, v24\n"
    "v_exp_f32 v25, v25\n"
    "v_exp_f32 v26, v26\n"
    "v_exp_f32 v27, v27\n"
    "v_exp_f32 v28, v28\n"
    "v_exp_f32 v29, v29\n"
    "v_exp_f32 v30, v30\n"
    "v_exp_f32 v31, v31\n"
    "v_pk_add_f32 v[24:25], v[24:25], 1.0 op_sel_hi:[1,0]\n"
    "v_pk_add_f32 v[26:27], v[26:27], 1.0 op_sel_hi:[1,0]\n"
    "v_pk_add_f32 v[28:29], v[28:29], 1.0 op_sel_hi:[1,0]\n"
    "v_pk_add_f32 v[30:31], v[30:31], 1.0 op_sel_hi:[1,0]\n"
    "v_rcp_f32 v24, v24\n"
    "v_rcp_f32 v25, v25\n"
    "v_rcp_f32 v26, v26\n"
    "v_rcp_f32 v27, v27\n"
    "v_rcp_f32 v28, v28\n"
    "v_rcp_f32 v29, v29\n"
    "v_rcp_f32 v30, v30\n"
    "v_rcp_f32 v31, v31\n"
    "v_pk_mul_f32 v[56:57], v[56:57], v[24:25]\n"
    "v_pk_mul_f32 v[58:59], v[58:59], v[26:27]\n"
    "v_pk_mul_f32 v[60:61], v[60:61], v[28:29]\n"
    "v_pk_mul_f32 v[62:63], v[62:63], v[30:31]\n"
    "v_cvt_pk_bf16_f32 v56, v56, v57\n"
    "v_cvt_pk_bf16_f32 v57, v58, v59\n"
    "v_cvt_pk_bf16_f32 v58, v60, v61\n"
    "v_cvt_pk_bf16_f32 v59, v62, v63\n"
    "buffer_store_dwordx4 v[56:59], v23, s[44:47], s99 offen offset:1024 sc1\n"
    "s_nop 0\n"
    "ds_read2_b64 v[56:59], v21 offset1:32\n"
    "s_waitcnt lgkmcnt(1)\n"
    "v_pk_fma_f32 v[24:25], v[0:1], v[32:33], v[8:9] op_sel:[0,1,0] op_sel_hi:[1,1,1]\n"
    "v_pk_fma_f32 v[26:27], v[2:3], v[32:33], v[10:11] op_sel:[0,1,0] op_sel_hi:[1,1,1]\n"
    "v_pk_fma_f32 v[28:29], v[4:5], v[32:33], v[12:13] op_sel:[0,1,0] op_sel_hi:[1,1,1]\n"
    "v_pk_fma_f32 v[30:31], v[6:7], v[32:33], v[14:15] op_sel:[0,1,0] op_sel_hi:[1,1,1]\n"
    "v_pk_fma_f32 v[80:81], v[80:81], v[32:33], v[24:25] op_sel_hi:[1,0,1]\n"
    "v_pk_fma_f32 v[82:83], v[82:83], v[32:33], v[26:27] op_sel_hi:[1,0,1]\n"
    "v_pk_fma_f32 v[84:85], v[84:85], v[32:33], v[28:29] op_sel_hi:[1,0,1]\n"
    "v_pk_fma_f32 v[86:87], v[86:87], v[32:33], v[30:31] op_sel_hi:[1,0,1]\n"
    "v_pk_mul_f32 v[24:25], v[80:81], v[80:81]\n"
    "v_pk_mul_f32 v[26:27], v[82:83], v[82:83]\n"
    "v_pk_mul_f32 v[28:29], v[84:85], v[84:85]\n"
    "v_pk_mul_f32 v[30:31], v[86:87], v[86:87]\n"
    "v_pk_fma_f32 v[24:25], v[24:25], s[96:97], s[96:97] op_sel:[0,1,0] op_sel_hi:[1,1,0]\n"
    "v_pk_fma_f32 v[26:27], v[26:27], s[96:97], s[96:97] op_sel:[0,1,0] op_sel_hi:[1,1,0]\n"
    "v_pk_fma_f32 v[28:29], v[28:29], s[96:97], s[96:97] op_sel:[0,1,0] op_sel_hi:[1,1,0]\n"
    "v_pk_fma_f32 v[30:31], v[30:31], s[96:97], s[96:97] op_sel:[0,1,0] op_sel_hi:[1,1,0]\n"
    "v_pk_mul_f32 v[24:25], v[24:25], v[80:81]\n"
    "v_pk_mul_f32 v[26:27], v[26:27], v[82:83]\n"
    "v_pk_mul_f32 v[28:29], v[28:29], v[84:85]\n"
    "v_pk_mul_f32 v[30:31], v[30:31], v[86:87]\n"
    "v_exp_f32 v24, v24\n"
    "v_exp_f32 v25, v25\n"
    "v_exp_f32 v26, v26\n"
    "v_exp_f32 v27, v27\n"
    "v_exp_f32 v28, v28\n"
    "v_exp_f32 v29, v29\n"
    "v_exp_f32 v30, v30\n"
    "v_exp_f32 v31, v31\n"
    "v_pk_add_f32 v[24:25], v[24:25], 1.0 op_sel_hi:[1,0]\n"
    "v_pk_add_f32 v[26:27], v[26:27], 1.0 op_sel_hi:[1,0]\n"
    "v_pk_add_f32 v[28:29], v[28:29], 1.0 op_sel_hi:[1,0]\n"
    "v_pk_add_f32 v[30:31], v[30:31], 1.0 op_sel_hi:[1,0]\n"
    "v_rcp_f32 v24, v24\n"
    "v_rcp_f32 v25, v25\n"
    "v_rcp_f32 v26, v26\n"
    "v_rcp_f32 v27, v27\n"
    "v_rcp_f32 v28, v28\n"
    "v_rcp_f32 v29, v29\n"
    "v_rcp_f32 v30, v30\n"
    "v_rcp_f32 v31, v31\n"
    "v_pk_mul_f32 v[80:81], v[80:81], v[24:25]\n"
    "v_pk_mul_f32 v[82:83], v[82:83], v[26:27]\n"
    "v_pk_mul_f32 v[84:85], v[84:85], v[28:29]\n"
    "v_pk_mul_f32 v[86:87], v[86:87], v[30:31]\n"
    "v_cvt_pk_bf16_f32 v80, v80, v81\n"
    "v_cvt_pk_bf16_f32 v81, v82, v83\n"
    "v_cvt_pk_bf16_f32 v82, v84, v85\n"
    "v_cvt_pk_bf16_f32 v83, v86, v87\n"
    "buffer_store_dwordx4 v[80:83], v23, s[44:47], s99 offen offset:2048 sc1\n"
    "s_nop 0\n"
    "ds_read2_b64 v[80:83], v21 offset0:64 offset1:96\n"
    "v_pk_fma_f32 v[24:25], v[0:1], v[34:35], v[8:9] op_sel:[0,1,0] op_sel_hi:[1,1,1]\n"
    "v_pk_fma_f32 v[26:27], v[2:3], v[34:35], v[10:11] op_sel:[0,1,0] op_sel_hi:[1,1,1]\n"
    "v_pk_fma_f32 v[28:29], v[4:5], v[34:35], v[12:13] op_sel:[0,1,0] op_sel_hi:[1,1,1]\n"
    "v_pk_fma_f32 v[30:31], v[6:7], v[34:35], v[14:15] op_sel:[0,1,0] op_sel_hi:[1,1,1]\n"
    "v_pk_fma_f32 v[104:105], v[104:105], v[34:35], v[24:25] op_sel_hi:[1,0,1]\n"
    "v_pk_fma_f32 v[106:107], v[106:107], v[34:35], v[26:27] op_sel_hi:[1,0,1]\n"
    "v_pk_fma_f32 v[108:109], v[108:109], v[34:35], v[28:29] op_sel_hi:[1,0,1]\n"
    "v_pk_fma_f32 v[110:111], v[110:111], v[34:35], v[30:31] op_sel_hi:[1,0,1]\n"
    "v_pk_mul_f32 v[24:25], v[104:105], v[104:105]\n"
    "v_pk_mul_f32 v[26:27], v[106:107], v[106:107]\n"
    "v_pk_mul_f32 v[28:29], v[108:109], v[108:109]\n"
    "v_pk_mul_f32 v[30:31], v[110:111], v[110:111]\n"
    "v_pk_fma_f32 v[24:25], v[24:25], s[96:97], s[96:97] op_sel:[0,1,0] op_sel_hi:[1,1,0]\n"
    "v_pk_fma_f32 v[26:27], v[26:27], s[96:97], s[96:97] op_sel:[0,1,0] op_sel_hi:[1,1,0]\n"
    "v_pk_fma_f32 v[28:29], v[28:29], s[96:97], s[96:97] op_sel:[0,1,0] op_sel_hi:[1,1,0]\n"
    "v_pk_fma_f32 v[30:31], v[30:31], s[96:97], s[96:97] op_sel:[0,1,0] op_sel_hi:[1,1,0]\n"
    "v_pk_mul_f32 v[24:25], v[24:25], v[104:105]\n"
    "v_pk_mul_f32 v[26:27], v[26:27], v[106:107]\n"
    "v_pk_mul_f32 v[28:29], v[28:29], v[108:109]\n"
    "v_pk_mul_f32 v[30:31], v[30:31], v[110:111]\n"
    "v_exp_f32 v24, v24\n"
    "v_exp_f32 v25, v25\n"
    "v_exp_f32 v26, v26\n"
    "v_exp_f32 v27, v27\n"
    "v_exp_f32 v28, v28\n"
    "v_exp_f32 v29, v29\n"
    "v_exp_f32 v30, v30\n"
    "v_exp_f32 v31, v31\n"
    "v_pk_add_f32 v[24:25], v[24:25], 1.0 op_sel_hi:[1,0]\n"
    "v_pk_add_f32 v[26:27], v[26:27], 1.0 op_sel_hi:[1,0]\n"
    "v_pk_add_f32 v[28:29], v[28:29], 1.0 op_sel_hi:[1,0]\n"
    "v_pk_add_f32 v[30:31], v[30:31], 1.0 op_sel_hi:[1,0]\n"
    "v_rcp_f32 v24, v24\n"
    "v_rcp_f32 v25, v25\n"
    "v_rcp_f32 v26, v26\n"
    "v_rcp_f32 v27, v27\n"
    "v_rcp_f32 v28, v28\n"
    "v_rcp_f32 v29, v29\n"
    "v_rcp_f32 v30, v30\n"
    "v_rcp_f32 v31, v31\n"
    "v_pk_mul_f32 v[104:105], v[104:105], v[24:25]\n"
    "v_pk_mul_f32 v[106:107], v[106:107], v[26:27]\n"
    "v_pk_mul_f32 v[108:109], v[108:109], v[28:29]\n"
    "v_pk_mul_f32 v[110:111], v[110:111], v[30:31]\n"
    "v_cvt_pk_bf16_f32 v104, v104, v105\n"
    "v_cvt_pk_bf16_f32 v105, v106, v107\n"
    "v_cvt_pk_bf16_f32 v106, v108, v109\n"
    "v_cvt_pk_bf16_f32 v107, v110, v111\n"
    "buffer_store_dwordx4 v[104:107], v23, s[44:47], s99 offen offset:3072 sc1\n"
    "s_waitcnt lgkmcnt(0)\n"
    "s_add_u32 s99, s61, s62\n"
    "v_pk_fma_f32 v[24:25], v[0:1], v[56:57], v[8:9] op_sel:[0,1,0] op_sel_hi:[1,1,1]\n"
    "v_pk_fma_f32 v[26:27], v[2:3], v[56:57], v[10:11] op_sel:[0,1,0] op_sel_hi:[1,1,1]\n"
    "v_pk_fma_f32 v[28:29], v[4:5], v[56:57], v[12:13] op_sel:[0,1,0] op_sel_hi:[1,1,1]\n"
    "v_pk_fma_f32 v[30:31], v[6:7], v[56:57], v[14:15] op_sel:[0,1,0] op_sel_hi:[1,1,1]\n"
    "v_pk_fma_f32 v[128:129], v[128:129], v[56:57], v[24:25] op_sel_hi:[1,0,1]\n"
    "v_pk_fma_f32 v[130:131], v[130:131], v[56:57], v[26:27] op_sel_hi:[1,0,1]\n"
    "v_pk_fma_f32 v[132:133], v[132:133], v[56:57], v[28:29] op_sel_hi:[1,0,1]\n"
    "v_pk_fma_f32 v[134:135], v[134:135], v[56:57], v[30:31] op_sel_hi:[1,0,1]\n"
    "v_pk_mul_f32 v[24:25], v[128:129], v[128:129]\n"
    "v_pk_mul_f32 v[26:27], v[130:131], v[130:131]\n"
    "v_pk_mul_f32 v[28:29], v[132:133], v[132:133]\n"
    "v_pk_mul_f32 v[30:31], v[134:135], v[134:135]\n"
    "v_pk_fma_f32 v[24:25], v[24:25], s[96:97], s[96:97] op_sel:[0,1,0] op_sel_hi:[1,1,0]\n"
    "v_pk_fma_f32 v[26:27], v[26:27], s[96:97], s[96:97] op_sel:[0,1,0] op_sel_hi:[1,1,0]\n"
    "v_pk_fma_f32 v[28:29], v[28:29], s[96:97], s[96:97] op_sel:[0,1,0] op_sel_hi:[1,1,0]\n"
    "v_pk_fma_f32 v[30:31], v[30:31], s[96:97], s[96:97] op_sel:[0,1,0] op_sel_hi:[1,1,0]\n"
    "v_pk_mul_f32 v[24:25], v[24:25], v[128:129]\n"
    "v_pk_mul_f32 v[26:27], v[26:27], v[130:131]\n"
    "v_pk_mul_f32 v[28:29], v[28:29], v[132:133]\n"
    "v_pk_mul_f32 v[30:31], v[30:31], v[134:135]\n"
    "v_exp_f32 v24, v24\n"
    "v_exp_f32 v25, v25\n"
    "v_exp_f32 v26, v26\n"
    "v_exp_f32 v27, v27\n"
    "v_exp_f32 v28, v28\n"
    "v_exp_f32 v29, v29\n"
    "v_exp_f32 v30, v30\n"
    "v_exp_f32 v31, v31\n"
    "v_pk_add_f32 v[24:25], v[24:25], 1.0 op_sel_hi:[1,0]\n"
    "v_pk_add_f32 v[26:27], v[26:27], 1.0 op_sel_hi:[1,0]\n"
    "v_pk_add_f32 v[28:29], v[28:29], 1.0 op_sel_hi:[1,0]\n"
    "v_pk_add_f32 v[30:31], v[30:31], 1.0 op_sel_hi:[1,0]\n"
    "v_rcp_f32 v24, v24\n"
    "v_rcp_f32 v25, v25\n"
    "v_rcp_f32 v26, v26\n"
    "v_rcp_f32 v27, v27\n"
    "v_rcp_f32 v28, v28\n"
    "v_rcp_f32 v29, v29\n"
    "v_rcp_f32 v30, v30\n"
    "v_rcp_f32 v31, v31\n"
    "v_pk_mul_f32 v[128:129], v[128:129], v[24:25]\n"
    "v_pk_mul_f32 v[130:131], v[130:131], v[26:27]\n"
    "v_pk_mul_f32 v[132:133], v[132:133], v[28:29]\n"
    "v_pk_mul_f32 v[134:135], v[134:135], v[30:31]\n"
    "v_cvt_pk_bf16_f32 v128, v128, v129\n"
    "v_cvt_pk_bf16_f32 v129, v130, v131\n"
    "v_cvt_pk_bf16_f32 v130, v132, v133\n"
    "v_cvt_pk_bf16_f32 v131, v134, v135\n"
    "buffer_store_dwordx4 v[128:131], v23, s[44:47], s99 offen sc1\n"
    "v_pk_fma_f32 v[24:25], v[0:1], v[58:59], v[8:9] op_sel:[0,1,0] op_sel_hi:[1,1,1]\n"
    "v_pk_fma_f32 v[26:27], v[2:3], v[58:59], v[10:11] op_sel:[0,1,0] op_sel_hi:[1,1,1]\n"
    "v_pk_fma_f32 v[28:29], v[4:5], v[58:59], v[12:13] op_sel:[0,1,0] op_sel_hi:[1,1,1]\n"
    "v_pk_fma_f32 v[30:31], v[6:7], v[58:59], v[14:15] op_sel:[0,1,0] op_sel_hi:[1,1,1]\n"
    "v_pk_fma_f32 v[152:153], v[152:153], v[58:59], v[24:25] op_sel_hi:[1,0,1]\n"
    "v_pk_fma_f32 v[154:155], v[154:155], v[58:59], v[26:27] op_sel_hi:[1,0,1]\n"
    "v_pk_fma_f32 v[156:157], v[156:157], v[58:59], v[28:29] op_sel_hi:[1,0,1]\n"
    "v_pk_fma_f32 v[158:159], v[158:159], v[58:59], v[30:31] op_sel_hi:[1,0,1]\n"
    "v_pk_mul_f32 v[24:25], v[152:153], v[152:153]\n"
    "v_pk_mul_f32 v[26:27], v[154:155], v[154:155]\n"
    "v_pk_mul_f32 v[28:29], v[156:157], v[156:157]\n"
    "v_pk_mul_f32 v[30:31], v[158:159], v[158:159]\n"
    "v_pk_fma_f32 v[24:25], v[24:25], s[96:97], s[96:97] op_sel:[0,1,0] op_sel_hi:[1,1,0]\n"
    "v_pk_fma_f32 v[26:27], v[26:27], s[96:97], s[96:97] op_sel:[0,1,0] op_sel_hi:[1,1,0]\n"
    "v_pk_fma_f32 v[28:29], v[28:29], s[96:97], s[96:97] op_sel:[0,1,0] op_sel_hi:[1,1,0]\n"
    "v_pk_fma_f32 v[30:31], v[30:31], s[96:97], s[96:97] op_sel:[0,1,0] op_sel_hi:[1,1,0]\n"
    "v_pk_mul_f32 v[24:25], v[24:25], v[152:153]\n"
    "v_pk_mul_f32 v[26:27], v[26:27], v[154:155]\n"
    "v_pk_mul_f32 v[28:29], v[28:29], v[156:157]\n"
    "v_pk_mul_f32 v[30:31], v[30:31], v[158:159]\n"
    "v_exp_f32 v24, v24\n"
    "v_exp_f32 v25, v25\n"
    "v_exp_f32 v26, v26\n"
    "v_exp_f32 v27, v27\n"
    "v_exp_f32 v28, v28\n"
    "v_exp_f32 v29, v29\n"
    "v_exp_f32 v30, v30\n"
    "v_exp_f32 v31, v31\n"
    "v_pk_add_f32 v[24:25], v[24:25], 1.0 op_sel_hi:[1,0]\n"
    "v_pk_add_f32 v[26:27], v[26:27], 1.0 op_sel_hi:[1,0]\n"
    "v_pk_add_f32 v[28:29], v[28:29], 1.0 op_sel_hi:[1,0]\n"
    "v_pk_add_f32 v[30:31], v[30:31], 1.0 op_sel_hi:[1,0]\n"
    "v_rcp_f32 v24, v24\n"
    "v_rcp_f32 v25, v25\n"
    "v_rcp_f32 v26, v26\n"
    "v_rcp_f32 v27, v27\n"
    "v_rcp_f32 v28, v28\n"
    "v_rcp_f32 v29, v29\n"
    "v_rcp_f32 v30, v30\n"
    "v_rcp_f32 v31, v31\n"
    "v_pk_mul_f32 v[152:153], v[152:153], v[24:25]\n"
    "v_pk_mul_f32 v[154:155], v[154:155], v[26:27]\n"
    "v_pk_mul_f32 v[156:157], v[156:157], v[28:29]\n"
    "v_pk_mul_f32 v[158:159], v[158:159], v[30:31]\n"
    "v_cvt_pk_bf16_f32 v152, v152, v153\n"
    "v_cvt_pk_bf16_f32 v153, v154, v155\n"
    "v_cvt_pk_bf16_f32 v154, v156, v157\n"
    "v_cvt_pk_bf16_f32 v155, v158, v159\n"
    "buffer_store_dwordx4 v[152:155], v23, s[44:47], s99 offen offset:1024 sc1\n"
    "v_pk_fma_f32 v[24:25], v[0:1], v[80:81], v[8:9] op_sel:[0,1,0] op_sel_hi:[1,1,1]\n"
    "v_pk_fma_f32 v[26:27], v[2:3], v[80:81], v[10:11] op_sel:[0,1,0] op_sel_hi:[1,1,1]\n"
    "v_pk_fma_f32 v[28:29], v[4:5], v[80:81], v[12:13] op_sel:[0,1,0] op_sel_hi:[1,1,1]\n"
    "v_pk_fma_f32 v[30:31], v[6:7], v[80:81], v[14:15] op_sel:[0,1,0] op_sel_hi:[1,1,1]\n"
    "v_pk_fma_f32 v[176:177], v[176:177], v[80:81], v[24:25] op_sel_hi:[1,0,1]\n"
    "v_pk_fma_f32 v[178:179], v[178:179], v[80:81], v[26:27] op_sel_hi:[1,0,1]\n"
    "v_pk_fma_f32 v[180:181], v[180:181], v[80:81], v[28:29] op_sel_hi:[1,0,1]\n"
    "v_pk_fma_f32 v[182:183], v[182:183], v[80:81], v[30:31] op_sel_hi:[1,0,1]\n"
    "v_pk_mul_f32 v[24:25], v[176:177], v[176:177]\n"
    "v_pk_mul_f32 v[26:27], v[178:179], v[178:179]\n"
    "v_pk_mul_f32 v[28:29], v[180:181], v[180:181]\n"
    "v_pk_mul_f32 v[30:31], v[182:183], v[182:183]\n"
    "v_pk_fma_f32 v[24:25], v[24:25], s[96:97], s[96:97] op_sel:[0,1,0] op_sel_hi:[1,1,0]\n"
    "v_pk_fma_f32 v[26:27], v[26:27], s[96:97], s[96:97] op_sel:[0,1,0] op_sel_hi:[1,1,0]\n"
    "v_pk_fma_f32 v[28:29], v[28:29], s[96:97], s[96:97] op_sel:[0,1,0] op_sel_hi:[1,1,0]\n"
    "v_pk_fma_f32 v[30:31], v[30:31], s[96:97], s[96:97] op_sel:[0,1,0] op_sel_hi:[1,1,0]\n"
    "v_pk_mul_f32 v[24:25], v[24:25], v[176:177]\n"
    "v_pk_mul_f32 v[26:27], v[26:27], v[178:179]\n"
    "v_pk_mul_f32 v[28:29], v[28:29], v[180:181]\n"
    "v_pk_mul_f32 v[30:31], v[30:31], v[182:183]\n"
    "v_exp_f32 v24, v24\n"
    "v_exp_f32 v25, v25\n"
    "v_exp_f32 v26, v26\n"
    "v_exp_f32 v27, v27\n"
    "v_exp_f32 v28, v28\n"
    "v_exp_f32 v29, v29\n"
    "v_exp_f32 v30, v30\n"
    "v_exp_f32 v31, v31\n"
    "v_pk_add_f32 v[24:25], v[24:25], 1.0 op_sel_hi:[1,0]\n"
    "v_pk_add_f32 v[26:27], v[26:27], 1.0 op_sel_hi:[1,0]\n"
    "v_pk_add_f32 v[28:29], v[28:29], 1.0 op_sel_hi:[1,0]\n"
    "v_pk_add_f32 v[30:31], v[30:31], 1.0 op_sel_hi:[1,0]\n"
    "v_rcp_f32 v24, v24\n"
    "v_rcp_f32 v25, v25\n"
    "v_rcp_f32 v26, v26\n"
    "v_rcp_f32 v27, v27\n"
    "v_rcp_f32 v28, v28\n"
    "v_rcp_f32 v29, v29\n"
    "v_rcp_f32 v30, v30\n"
    "v_rcp_f32 v31, v31\n"
    "v_pk_mul_f32 v[176:177], v[176:177], v[24:25]\n"
    "v_pk_mul_f32 v[178:179], v[178:179], v[26:27]\n"
    "v_pk_mul_f32 v[180:181], v[180:181], v[28:29]\n"
    "v_pk_mul_f32 v[182:183], v[182:183], v[30:31]\n"
    "v_cvt_pk_bf16_f32 v176, v176, v177\n"
    "v_cvt_pk_bf16_f32 v177, v178, v179\n"
    "v_cvt_pk_bf16_f32 v178, v180, v181\n"
    "v_cvt_pk_bf16_f32 v179, v182, v183\n"
    "buffer_store_dwordx4 v[176:179], v23, s[44:47], s99 offen offset:2048 sc1\n"
    "ds_read_b128 v[108:111], v22 offset:256\n"
    "ds_read_b128 v[132:135], v22 offset:320\n"
    "ds_read_b128 v[156:159], v22 offset:1280\n"
    "ds_read_b128 v[180:183], v22 offset:1344\n"
    "v_pk_fma_f32 v[24:25], v[0:1], v[82:83], v[8:9] op_sel:[0,1,0] op_sel_hi:[1,1,1]\n"
    "v_pk_fma_f32 v[26:27], v[2:3], v[82:83], v[10:11] op_sel:[0,1,0] op_sel_hi:[1,1,1]\n"
    "v_pk_fma_f32 v[28:29], v[4:5], v[82:83], v[12:13] op_sel:[0,1,0] op_sel_hi:[1,1,1]\n"
    "v_pk_fma_f32 v[30:31], v[6:7], v[82:83], v[14:15] op_sel:[0,1,0] op_sel_hi:[1,1,1]\n"
    "v_pk_fma_f32 v[200:201], v[200:201], v[82:83], v[24:25] op_sel_hi:[1,0,1]\n"
    "v_pk_fma_f32 v[202:203], v[202:203], v[82:83], v[26:27] op_sel_hi:[1,0,1]\n"
    "v_pk_fma_f32 v[204:205], v[204:205], v[82:83], v[28:29] op_sel_hi:[1,0,1]\n"
    "v_pk_fma_f32 v[206:207], v[206:207], v[82:83], v[30:31] op_sel_hi:[1,0,1]\n"
    "v_pk_mul_f32 v[24:25], v[200:201], v[200:201]\n"
    "v_pk_mul_f32 v[26:27], v[202:203], v[202:203]\n"
    "v_pk_mul_f32 v[28:29], v[204:205], v[204:205]\n"
    "v_pk_mul_f32 v[30:31], v[206:207], v[206:207]\n"
    "v_pk_fma_f32 v[24:25], v[24:25], s[96:97], s[96:97] op_sel:[0,1,0] op_sel_hi:[1,1,0]\n"
    "v_pk_fma_f32 v[26:27], v[26:27], s[96:97], s[96:97] op_sel:[0,1,0] op_sel_hi:[1,1,0]\n"
    "v_pk_fma_f32 v[28:29], v[28:29], s[96:97], s[96:97] op_sel:[0,1,0] op_sel_hi:[1,1,0]\n"
    "v_pk_fma_f32 v[30:31], v[30:31], s[96:97], s[96:97] op_sel:[0,1,0] op_sel_hi:[1,1,0]\n"
    "v_pk_mul_f32 v[24:25], v[24:25], v[200:201]\n"
    "v_pk_mul_f32 v[26:27], v[26:27], v[202:203]\n"
    "v_pk_mul_f32 v[28:29], v[28:29], v[204:205]\n"
    "v_pk_mul_f32 v[30:31], v[30:31], v[206:207]\n"
    "v_exp_f32 v24, v24\n"
    "v_exp_f32 v25, v25\n"
    "v_exp_f32 v26, v26\n"
    "v_exp_f32 v27, v27\n"
    "v_exp_f32 v28, v28\n"
    "v_exp_f32 v29, v29\n"
    "v_exp_f32 v30, v30\n"
    "v_exp_f32 v31, v31\n"
    "v_pk_add_f32 v[24:25], v[24:25], 1.0 op_sel_hi:[1,0]\n"
    "v_pk_add_f32 v[26:27], v[26:27], 1.0 op_sel_hi:[1,0]\n"
    "v_pk_add_f32 v[28:29], v[28:29], 1.0 op_sel_hi:[1,0]\n"
    "v_pk_add_f32 v[30:31], v[30:31], 1.0 op_sel_hi:[1,0]\n"
    "v_rcp_f32 v24, v24\n"
    "v_rcp_f32 v25, v25\n"
    "v_rcp_f32 v26, v26\n"
    "v_rcp_f32 v27, v27\n"
    "v_rcp_f32 v28, v28\n"
    "v_rcp_f32 v29, v29\n"
    "v_rcp_f32 v30, v30\n"
    "v_rcp_f32 v31, v31\n"
    "v_pk_mul_f32 v[200:201], v[200:201], v[24:25]\n"
    "v_pk_mul_f32 v[202:203], v[202:203], v[26:27]\n"
    "v_pk_mul_f32 v[204:205], v[204:205], v[28:29]\n"
    "v_pk_mul_f32 v[206:207], v[206:207], v[30:31]\n"
    "v_cvt_pk_bf16_f32 v200, v200, v201\n"
    "v_cvt_pk_bf16_f32 v201, v202, v203\n"
    "v_cvt_pk_bf16_f32 v202, v204, v205\n"
    "v_cvt_pk_bf16_f32 v203, v206, v207\n"
    "buffer_store_dwordx4 v[200:203], v23, s[44:47], s99 offen offset:3072 sc1\n"
    "s_waitcnt lgkmcnt(0)\n"
    "s_add_u32 s99, s60, s63\n"
    "v_pk_fma_f32 v[24:25], v[108:109], v[16:17], v[156:157] op_sel:[0,1,0] op_sel_hi:[1,1,1]\n"
    "v_pk_fma_f32 v[26:27], v[110:111], v[16:17], v[158:159] op_sel:[0,1,0] op_sel_hi:[1,1,1]\n"
    "v_pk_fma_f32 v[28:29], v[132:133], v[16:17], v[180:181] op_sel:[0,1,0] op_sel_hi:[1,1,1]\n"
    "v_pk_fma_f32 v[30:31], v[134:135], v[16:17], v[182:183] op_sel:[0,1,0] op_sel_hi:[1,1,1]\n"
    "v_pk_fma_f32 v[40:41], v[40:41], v[16:17], v[24:25] op_sel_hi:[1,0,1]\n"
    "v_pk_fma_f32 v[42:43], v[42:43], v[16:17], v[26:27] op_sel_hi:[1,0,1]\n"
    "v_pk_fma_f32 v[44:45], v[44:45], v[16:17], v[28:29] op_sel_hi:[1,0,1]\n"
    "v_pk_fma_f32 v[46:47], v[46:47], v[16:17], v[30:31] op_sel_hi:[1,0,1]\n"
    "v_pk_mul_f32 v[24:25], v[40:41], v[40:41]\n"
    "v_pk_mul_f32 v[26:27], v[42:43], v[42:43]\n"
    "v_pk_mul_f32 v[28:29], v[44:45], v[44:45]\n"
    "v_pk_mul_f32 v[30:31], v[46:47], v[46:47]\n"
    "v_pk_fma_f32 v[24:25], v[24:25], s[96:97], s[96:97] op_sel:[0,1,0] op_sel_hi:[1,1,0]\n"
    "v_pk_fma_f32 v[26:27], v[26:27], s[96:97], s[96:97] op_sel:[0,1,0] op_sel_hi:[1,1,0]\n"
    "v_pk_fma_f32 v[28:29], v[28:29], s[96:97], s[96:97] op_sel:[0,1,0] op_sel_hi:[1,1,0]\n"
    "v_pk_fma_f32 v[30:31], v[30:31], s[96:97], s[96:97] op_sel:[0,1,0] op_sel_hi:[1,1,0]\n"
    "v_pk_mul_f32 v[24:25], v[24:25], v[40:41]\n"
    "v_pk_mul_f32 v[26:27], v[26:27], v[42:43]\n"
    "v_pk_mul_f32 v[28:29], v[28:29], v[44:45]\n"
    "v_pk_mul_f32 v[30:31], v[30:31], v[46:47]\n"
    "v_exp_f32 v24, v24\n"
    "v_exp_f32 v25, v25\n"
    "v_exp_f32 v26, v26\n"
    "v_exp_f32 v27, v27\n"
    "v_exp_f32 v28, v28\n"
    "v_exp_f32 v29, v29\n"
    "v_exp_f32 v30, v30\n"
    "v_exp_f32 v31, v31\n"
    "v_pk_add_f32 v[24:25], v[24:25], 1.0 op_sel_hi:[1,0]\n"
    "v_pk_add_f32 v[26:27], v[26:27], 1.0 op_sel_hi:[1,0]\n"
    "v_pk_add_f32 v[28:29], v[28:29], 1.0 op_sel_hi:[1,0]\n"
    "v_pk_add_f32 v[30:31], v[30:31], 1.0 op_sel_hi:[1,0]\n"
    "v_rcp_f32 v24, v24\n"
    "v_rcp_f32 v25, v25\n"
    "v_rcp_f32 v26, v26\n"
    "v_rcp_f32 v27, v27\n"
    "v_rcp_f32 v28, v28\n"
    "v_rcp_f32 v29, v29\n"
    "v_rcp_f32 v30, v30\n"
    "v_rcp_f32 v31, v31\n"
    "v_pk_mul_f32 v[40:41], v[40:41], v[24:25]\n"
    "v_pk_mul_f32 v[42:43], v[42:43], v[26:27]\n"
    "v_pk_mul_f32 v[44:45], v[44:45], v[28:29]\n"
    "v_pk_mul_f32 v[46:47], v[46:47], v[30:31]\n"
    "v_cvt_pk_bf16_f32 v40, v40, v41\n"
    "v_cvt_pk_bf16_f32 v41, v42, v43\n"
    "v_cvt_pk_bf16_f32 v42, v44, v45\n"
    "v_cvt_pk_bf16_f32 v43, v46, v47\n"
    "buffer_store_dwordx4 v[40:43], v23, s[44:47], s99 offen sc1\n"
    "v_pk_fma_f32 v[24:25], v[108:109], v[18:19], v[156:157] op_sel:[0,1,0] op_sel_hi:[1,1,1]\n"
    "v_pk_fma_f32 v[26:27], v[110:111], v[18:19], v[158:159] op_sel:[0,1,0] op_sel_hi:[1,1,1]\n"
    "v_pk_fma_f32 v[28:29], v[132:133], v[18:19], v[180:181] op_sel:[0,1,0] op_sel_hi:[1,1,1]\n"
    "v_pk_fma_f32 v[30:31], v[134:135], v[18:19], v[182:183] op_sel:[0,1,0] op_sel_hi:[1,1,1]\n"
    "v_pk_fma_f32 v[64:65], v[64:65], v[18:19], v[24:25] op_sel_hi:[1,0,1]\n"
    "v_pk_fma_f32 v[66:67], v[66:67], v[18:19], v[26:27] op_sel_hi:[1,0,1]\n"
    "v_pk_fma_f32 v[68:69], v[68:69], v[18:19], v[28:29] op_sel_hi:[1,0,1]\n"
    "v_pk_fma_f32 v[70:71], v[70:71], v[18:19], v[30:31] op_sel_hi:[1,0,1]\n"
    "v_pk_mul_f32 v[24:25], v[64:65], v[64:65]\n"
    "v_pk_mul_f32 v[26:27], v[66:67], v[66:67]\n"
    "v_pk_mul_f32 v[28:29], v[68:69], v[68:69]\n"
    "v_pk_mul_f32 v[30:31], v[70:71], v[70:71]\n"
    "v_pk_fma_f32 v[24:25], v[24:25], s[96:97], s[96:97] op_sel:[0,1,0] op_sel_hi:[1,1,0]\n"
    "v_pk_fma_f32 v[26:27], v[26:27], s[96:97], s[96:97] op_sel:[0,1,0] op_sel_hi:[1,1,0]\n"
    "v_pk_fma_f32 v[28:29], v[28:29], s[96:97], s[96:97] op_sel:[0,1,0] op_sel_hi:[1,1,0]\n"
    "v_pk_fma_f32 v[30:31], v[30:31], s[96:97], s[96:97] op_sel:[0,1,0] op_sel_hi:[1,1,0]\n"
    "v_pk_mul_f32 v[24:25], v[24:25], v[64:65]\n"
    "v_pk_mul_f32 v[26:27], v[26:27], v[66:67]\n"
    "v_pk_mul_f32 v[28:29], v[28:29], v[68:69]\n"
    "v_pk_mul_f32 v[30:31], v[30:31], v[70:71]\n"
    "v_exp_f32 v24, v24\n"
    "v_exp_f32 v25, v25\n"
    "v_exp_f32 v26, v26\n"
    "v_exp_f32 v27, v27\n"
    "v_exp_f32 v28, v28\n"
    "v_exp_f32 v29, v29\n"
    "v_exp_f32 v30, v30\n"
    "v_exp_f32 v31, v31\n"
    "v_pk_add_f32 v[24:25], v[24:25], 1.0 op_sel_hi:[1,0]\n"
    "v_pk_add_f32 v[26:27], v[26:27], 1.0 op_sel_hi:[1,0]\n"
    "v_pk_add_f32 v[28:29], v[28:29], 1.0 op_sel_hi:[1,0]\n"
    "v_pk_add_f32 v[30:31], v[30:31], 1.0 op_sel_hi:[1,0]\n"
    "v_rcp_f32 v24, v24\n"
    "v_rcp_f32 v25, v25\n"
    "v_rcp_f32 v26, v26\n"
    "v_rcp_f32 v27, v27\n"
    "v_rcp_f32 v28, v28\n"
    "v_rcp_f32 v29, v29\n"
    "v_rcp_f32 v30, v30\n"
    "v_rcp_f32 v31, v31\n"
    "v_pk_mul_f32 v[64:65], v[64:65], v[24:25]\n"
    "v_pk_mul_f32 v[66:67], v[66:67], v[26:27]\n"
    "v_pk_mul_f32 v[68:69], v[68:69], v[28:29]\n"
    "v_pk_mul_f32 v[70:71], v[70:71], v[30:31]\n"
    "v_cvt_pk_bf16_f32 v64, v64, v65\n"
    "v_cvt_pk_bf16_f32 v65, v66, v67\n"
    "v_cvt_pk_bf16_f32 v66, v68, v69\n"
    "v_cvt_pk_bf16_f32 v67, v70, v71\n"
    "buffer_store_dwordx4 v[64:67], v23, s[44:47], s99 offen offset:1024 sc1\n"
    "v_pk_fma_f32 v[24:25], v[108:109], v[32:33], v[156:157] op_sel:[0,1,0] op_sel_hi:[1,1,1]\n"
    "v_pk_fma_f32 v[26:27], v[110:111], v[32:33], v[158:159] op_sel:[0,1,0] op_sel_hi:[1,1,1]\n"
    "v_pk_fma_f32 v[28:29], v[132:133], v[32:33], v[180:181] op_sel:[0,1,0] op_sel_hi:[1,1,1]\n"
    "v_pk_fma_f32 v[30:31], v[134:135], v[32:33], v[182:183] op_sel:[0,1,0] op_sel_hi:[1,1,1]\n"
    "v_pk_fma_f32 v[88:89], v[88:89], v[32:33], v[24:25] op_sel_hi:[1,0,1]\n"
    "v_pk_fma_f32 v[90:91], v[90:91], v[32:33], v[26:27] op_sel_hi:[1,0,1]\n"
    "v_pk_fma_f32 v[92:93], v[92:93], v[32:33], v[28:29] op_sel_hi:[1,0,1]\n"
    "v_pk_fma_f32 v[94:95], v[94:95], v[32:33], v[30:31] op_sel_hi:[1,0,1]\n"
    "v_pk_mul_f32 v[24:25], v[88:89], v[88:89]\n"
    "v_pk_mul_f32 v[26:27], v[90:91], v[90:91]\n"
    "v_pk_mul_f32 v[28:29], v[92:93], v[92:93]\n"
    "v_pk_mul_f32 v[30:31], v[94:95], v[94:95]\n"
    "v_pk_fma_f32 v[24:25], v[24:25], s[96:97], s[96:97] op_sel:[0,1,0] op_sel_hi:[1,1,0]\n"
    "v_pk_fma_f32 v[26:27], v[26:27], s[96:97], s[96:97] op_sel:[0,1,0] op_sel_hi:[1,1,0]\n"
    "v_pk_fma_f32 v[28:29], v[28:29], s[96:97], s[96:97] op_sel:[0,1,0] op_sel_hi:[1,1,0]\n"
    "v_pk_fma_f32 v[30:31], v[30:31], s[96:97], s[96:97] op_sel:[0,1,0] op_sel_hi:[1,1,0]\n"
    "v_pk_mul_f32 v[24:25], v[24:25], v[88:89]\n"
    "v_pk_mul_f32 v[26:27], v[26:27], v[90:91]\n"
    "v_pk_mul_f32 v[28:29], v[28:29], v[92:93]\n"
    "v_pk_mul_f32 v[30:31], v[30:31], v[94:95]\n"
    "v_exp_f32 v24, v24\n"
    "v_exp_f32 v25, v25\n"
    "v_exp_f32 v26, v26\n"
    "v_exp_f32 v27, v27\n"
    "v_exp_f32 v28, v28\n"
    "v_exp_f32 v29, v29\n"
    "v_exp_f32 v30, v30\n"
    "v_exp_f32 v31, v31\n"
    "v_pk_add_f32 v[24:25], v[24:25], 1.0 op_sel_hi:[1,0]\n"
    "v_pk_add_f32 v[26:27], v[26:27], 1.0 op_sel_hi:[1,0]\n"
    "v_pk_add_f32 v[28:29], v[28:29], 1.0 op_sel_hi:[1,0]\n"
    "v_pk_add_f32 v[30:31], v[30:31], 1.0 op_sel_hi:[1,0]\n"
    "v_rcp_f32 v24, v24\n"
    "v_rcp_f32 v25, v25\n"
    "v_rcp_f32 v26, v26\n"
    "v_rcp_f32 v27, v27\n"
    "v_rcp_f32 v28, v28\n"
    "v_rcp_f32 v29, v29\n"
    "v_rcp_f32 v30, v30\n"
    "v_rcp_f32 v31, v31\n"
    "v_pk_mul_f32 v[88:89], v[88:89], v[24:25]\n"
    "v_pk_mul_f32 v[90:91], v[90:91], v[26:27]\n"
    "v_pk_mul_f32 v[92:93], v[92:93], v[28:29]\n"
    "v_pk_mul_f32 v[94:95], v[94:95], v[30:31]\n"
    "v_cvt_pk_bf16_f32 v88, v88, v89\n"
    "v_cvt_pk_bf16_f32 v89, v90, v91\n"
    "v_cvt_pk_bf16_f32 v90, v92, v93\n"
    "v_cvt_pk_bf16_f32 v91, v94, v95\n"
    "buffer_store_dwordx4 v[88:91], v23, s[44:47], s99 offen offset:2048 sc1\n"
    "s_waitcnt vmcnt(3)\n"
    "s_add_u32 s33, s92, 2\n"
    "v_mov_b32 v24, s33\n"
    "s_mov_b64 exec, 1\n"
    "buffer_store_dword v24, off, s[44:47], s66 sc1\n"
    "s_mov_b64 exec, -1\n"
    "v_pk_fma_f32 v[24:25], v[108:109], v[34:35], v[156:157] op_sel:[0,1,0] op_sel_hi:[1,1,1]\n"
    "v_pk_fma_f32 v[26:27], v[110:111], v[34:35], v[158:159] op_sel:[0,1,0] op_sel_hi:[1,1,1]\n"
    "v_pk_fma_f32 v[28:29], v[132:133], v[34:35], v[180:181] op_sel:[0,1,0] op_sel_hi:[1,1,1]\n"
    "v_pk_fma_f32 v[30:31], v[134:135], v[34:35], v[182:183] op_sel:[0,1,0] op_sel_hi:[1,1,1]\n"
    "v_pk_fma_f32 v[112:113], v[112:113], v[34:35], v[24:25] op_sel_hi:[1,0,1]\n"
    "v_pk_fma_f32 v[114:115], v[114:115], v[34:35], v[26:27] op_sel_hi:[1,0,1]\n"
    "v_pk_fma_f32 v[116:117], v[116:117], v[34:35], v[28:29] op_sel_hi:[1,0,1]\n"
    "v_pk_fma_f32 v[118:119], v[118:119], v[34:35], v[30:31] op_sel_hi:[1,0,1]\n"
    "v_pk_mul_f32 v[24:25], v[112:113], v[112:113]\n"
    "v_pk_mul_f32 v[26:27], v[114:115], v[114:115]\n"
    "v_pk_mul_f32 v[28:29], v[116:117], v[116:117]\n"
    "v_pk_mul_f32 v[30:31], v[118:119], v[118:119]\n"
    "v_pk_fma_f32 v[24:25], v[24:25], s[96:97], s[96:97] op_sel:[0,1,0] op_sel_hi:[1,1,0]\n"
    "v_pk_fma_f32 v[26:27], v[26:27], s[96:97], s[96:97] op_sel:[0,1,0] op_sel_hi:[1,1,0]\n"
    "v_pk_fma_f32 v[28:29], v[28:29], s[96:97], s[96:97] op_sel:[0,1,0] op_sel_hi:[1,1,0]\n"
    "v_pk_fma_f32 v[30:31], v[30:31], s[96:97], s[96:97] op_sel:[0,1,0] op_sel_hi:[1,1,0]\n"
    "v_pk_mul_f32 v[24:25], v[24:25], v[112:113]\n"
    "v_pk_mul_f32 v[26:27], v[26:27], v[114:115]\n"
    "v_pk_mul_f32 v[28:29], v[28:29], v[116:117]\n"
    "v_pk_mul_f32 v[30:31], v[30:31], v[118:119]\n"
    "v_exp_f32 v24, v24\n"
    "v_exp_f32 v25, v25\n"
    "v_exp_f32 v26, v26\n"
    "v_exp_f32 v27, v27\n"
    "v_exp_f32 v28, v28\n"
    "v_exp_f32 v29, v29\n"
    "v_exp_f32 v30, v30\n"
    "v_exp_f32 v31, v31\n"
    "v_pk_add_f32 v[24:25], v[24:25], 1.0 op_sel_hi:[1,0]\n"
    "v_pk_add_f32 v[26:27], v[26:27], 1.0 op_sel_hi:[1,0]\n"
    "v_pk_add_f32 v[28:29], v[28:29], 1.0 op_sel_hi:[1,0]\n"
    "v_pk_add_f32 v[30:31], v[30:31], 1.0 op_sel_hi:[1,0]\n"
    "v_rcp_f32 v24, v24\n"
    "v_rcp_f32 v25, v25\n"
    "v_rcp_f32 v26, v26\n"
    "v_rcp_f32 v27, v27\n"
    "v_rcp_f32 v28, v28\n"
    "v_rcp_f32 v29, v29\n"
    "v_rcp_f32 v30, v30\n"
    "v_rcp_f32 v31, v31\n"
    "v_pk_mul_f32 v[112:113], v[112:113], v[24:25]\n"
    "v_pk_mul_f32 v[114:115], v[114:115], v[26:27]\n"
    "v_pk_mul_f32 v[116:117], v[116:117], v[28:29]\n"
    "v_pk_mul_f32 v[118:119], v[118:119], v[30:31]\n"
    "v_cvt_pk_bf16_f32 v112, v112, v113\n"
    "v_cvt_pk_bf16_f32 v113, v114, v115\n"
    "v_cvt_pk_bf16_f32 v114, v116, v117\n"
    "v_cvt_pk_bf16_f32 v115, v118, v119\n"
    "buffer_store_dwordx4 v[112:115], v23, s[44:47], s99 offen offset:3072 sc1\n"
    "s_add_u32 s99, s61, s63\n"
    "v_pk_fma_f32 v[24:25], v[108:109], v[56:57], v[156:157] op_sel:[0,1,0] op_sel_hi:[1,1,1]\n"
    "v_pk_fma_f32 v[26:27], v[110:111], v[56:57], v[158:159] op_sel:[0,1,0] op_sel_hi:[1,1,1]\n"
    "v_pk_fma_f32 v[28:29], v[132:133], v[56:57], v[180:181] op_sel:[0,1,0] op_sel_hi:[1,1,1]\n"
    "v_pk_fma_f32 v[30:31], v[134:135], v[56:57], v[182:183] op_sel:[0,1,0] op_sel_hi:[1,1,1]\n"
    "v_pk_fma_f32 v[136:137], v[136:137], v[56:57], v[24:25] op_sel_hi:[1,0,1]\n"
    "v_pk_fma_f32 v[138:139], v[138:139], v[56:57], v[26:27] op_sel_hi:[1,0,1]\n"
    "v_pk_fma_f32 v[140:141], v[140:141], v[56:57], v[28:29] op_sel_hi:[1,0,1]\n"
    "v_pk_fma_f32 v[142:143], v[142:143], v[56:57], v[30:31] op_sel_hi:[1,0,1]\n"
    "v_pk_mul_f32 v[24:25], v[136:137], v[136:137]\n"
    "v_pk_mul_f32 v[26:27], v[138:139], v[138:139]\n"
    "v_pk_mul_f32 v[28:29], v[140:141], v[140:141]\n"
    "v_pk_mul_f32 v[30:31], v[142:143], v[142:143]\n"
    "v_pk_fma_f32 v[24:25], v[24:25], s[96:97], s[96:97] op_sel:[0,1,0] op_sel_hi:[1,1,0]\n"
    "v_pk_fma_f32 v[26:27], v[26:27], s[96:97], s[96:97] op_sel:[0,1,0] op_sel_hi:[1,1,0]\n"
    "v_pk_fma_f32 v[28:29], v[28:29], s[96:97], s[96:97] op_sel:[0,1,0] op_sel_hi:[1,1,0]\n"
    "v_pk_fma_f32 v[30:31], v[30:31], s[96:97], s[96:97] op_sel:[0,1,0] op_sel_hi:[1,1,0]\n"
    "v_pk_mul_f32 v[24:25], v[24:25], v[136:137]\n"
    "v_pk_mul_f32 v[26:27], v[26:27], v[138:139]\n"
    "v_pk_mul_f32 v[28:29], v[28:29], v[140:141]\n"
    "v_pk_mul_f32 v[30:31], v[30:31], v[142:143]\n"
    "v_exp_f32 v24, v24\n"
    "v_exp_f32 v25, v25\n"
    "v_exp_f32 v26, v26\n"
    "v_exp_f32 v27, v27\n"
    "v_exp_f32 v28, v28\n"
    "v_exp_f32 v29, v29\n"
    "v_exp_f32 v30, v30\n"
    "v_exp_f32 v31, v31\n"
    "v_pk_add_f32 v[24:25], v[24:25], 1.0 op_sel_hi:[1,0]\n"
    "v_pk_add_f32 v[26:27], v[26:27], 1.0 op_sel_hi:[1,0]\n"
    "v_pk_add_f32 v[28:29], v[28:29], 1.0 op_sel_hi:[1,0]\n"
    "v_pk_add_f32 v[30:31], v[30:31], 1.0 op_sel_hi:[1,0]\n"
    "v_rcp_f32 v24, v24\n"
    "v_rcp_f32 v25, v25\n"
    "v_rcp_f32 v26, v26\n"
    "v_rcp_f32 v27, v27\n"
    "v_rcp_f32 v28, v28\n"
    "v_rcp_f32 v29, v29\n"
    "v_rcp_f32 v30, v30\n"
    "v_rcp_f32 v31, v31\n"
    "v_pk_mul_f32 v[136:137], v[136:137], v[24:25]\n"
    "v_pk_mul_f32 v[138:139], v[138:139], v[26:27]\n"
    "v_pk_mul_f32 v[140:141], v[140:141], v[28:29]\n"
    "v_pk_mul_f32 v[142:143], v[142:143], v[30:31]\n"
    "v_cvt_pk_bf16_f32 v136, v136, v137\n"
    "v_cvt_pk_bf16_f32 v137, v138, v139\n"
    "v_cvt_pk_bf16_f32 v138, v140, v141\n"
    "v_cvt_pk_bf16_f32 v139, v142, v143\n"
    "buffer_store_dwordx4 v[136:139], v23, s[44:47], s99 offen sc1\n"
    "ds_read_b128 v[0:3], v22 offset:512\n"
    "ds_read_b128 v[4:7], v22 offset:576\n"
    "ds_read_b128 v[8:11], v22 offset:1536\n"
    "ds_read_b128 v[12:15], v22 offset:1600\n"
    "v_accvgpr_read_b32 v22, a174\n"
    "ds_read_b128 v[44:47], v22 offset:0\n"
    "ds_read_b128 v[68:71], v22 offset:64\n"
    "ds_read_b128 v[92:95], v22 offset:1024\n"
    "ds_read_b128 v[116:119], v22 offset:1088\n"
    "v_pk_fma_f32 v[24:25], v[108:109], v[58:59], v[156:157] op_sel:[0,1,0] op_sel_hi:[1,1,1]\n"
    "v_pk_fma_f32 v[26:27], v[110:111], v[58:59], v[158:159] op_sel:[0,1,0] op_sel_hi:[1,1,1]\n"
    "v_pk_fma_f32 v[28:29], v[132:133], v[58:59], v[180:181] op_sel:[0,1,0] op_sel_hi:[1,1,1]\n"
    "v_pk_fma_f32 v[30:31], v[134:135], v[58:59], v[182:183] op_sel:[0,1,0] op_sel_hi:[1,1,1]\n"
    "v_pk_fma_f32 v[160:161], v[160:161], v[58:59], v[24:25] op_sel_hi:[1,0,1]\n"
    ".Lk2_m2423:\n"
    ".Lk2_hw3:\n"
    "s_cbranch_execz .Lk2_m6825\n"
    "v_pk_fma_f32 v[162:163], v[162:163], v[58:59], v[26:27] op_sel_hi:[1,0,1]\n"
    "v_pk_fma_f32 v[164:165], v[164:165], v[58:59], v[28:29] op_sel_hi:[1,0,1]\n"
    "v_pk_fma_f32 v[166:167], v[166:167], v[58:59], v[30:31] op_sel_hi:[1,0,1]\n"
    "v_pk_mul_f32 v[24:25], v[160:161], v[160:161]\n"
    "v_pk_mul_f32 v[26:27], v[162:163], v[162:163]\n"
    "v_pk_mul_f32 v[28:29], v[164:165], v[164:165]\n"
    "v_pk_mul_f32 v[30:31], v[166:167], v[166:167]\n"
    "v_pk_fma_f32 v[24:25], v[24:25], s[96:97], s[96:97] op_sel:[0,1,0] op_sel_hi:[1,1,0]\n"
    "v_pk_fma_f32 v[26:27], v[26:27], s[96:97], s[96:97] op_sel:[0,1,0] op_sel_hi:[1,1,0]\n"
    "v_pk_fma_f32 v[28:29], v[28:29], s[96:97], s[96:97] op_sel:[0,1,0] op_sel_hi:[1,1,0]\n"
    "v_pk_fma_f32 v[30:31], v[30:31], s[96:97], s[96:97] op_sel:[0,1,0] op_sel_hi:[1,1,0]\n"
    "v_pk_mul_f32 v[24:25], v[24:25], v[160:161]\n"
    "v_pk_mul_f32 v[26:27], v[26:27], v[162:163]\n"
    "v_pk_mul_f32 v[28:29], v[28:29], v[164:165]\n"
    "v_pk_mul_f32 v[30:31], v[30:31], v[166:167]\n"
    "v_exp_f32 v24, v24\n"
    "v_exp_f32 v25, v25\n"
    "v_exp_f32 v26, v26\n"
    "v_exp_f32 v27, v27\n"
    "v_exp_f32 v28, v28\n"
    "v_exp_f32 v29, v29\n"
    "v_exp_f32 v30, v30\n"
    "v_exp_f32 v31, v31\n"
    "v_pk_add_f32 v[24:25], v[24:25], 1.0 op_sel_hi:[1,0]\n"
    "v_pk_add_f32 v[26:27], v[26:27], 1.0 op_sel_hi:[1,0]\n"
    "v_pk_add_f32 v[28:29], v[28:29], 1.0 op_sel_hi:[1,0]\n"
    "v_pk_add_f32 v[30:31], v[30:31], 1.0 op_sel_hi:[1,0]\n"
    "v_rcp_f32 v24, v24\n"
    "v_rcp_f32 v25, v25\n"
    "v_rcp_f32 v26, v26\n"
    "v_rcp_f32 v27, v27\n"
    "v_rcp_f32 v28, v28\n"
    "v_rcp_f32 v29, v29\n"
    "v_rcp_f32 v30, v30\n"
    "v_rcp_f32 v31, v31\n"
    "v_pk_mul_f32 v[160:161], v[160:161], v[24:25]\n"
    "v_pk_mul_f32 v[162:163], v[162:163], v[26:27]\n"
    "v_pk_mul_f32 v[164:165], v[164:165], v[28:29]\n"
    "v_pk_mul_f32 v[166:167], v[166:167], v[30:31]\n"
    "v_cvt_pk_bf16_f32 v160, v160, v161\n"
    "v_cvt_pk_bf16_f32 v161, v162, v163\n"
    "v_cvt_pk_bf16_f32 v162, v164, v165\n"
    "v_cvt_pk_bf16_f32 v163, v166, v167\n"
    "buffer_store_dwordx4 v[160:163], v23, s[44:47], s99 offen offset:1024 sc1\n"
    "v_pk_fma_f32 v[24:25], v[108:109], v[80:81], v[156:157] op_sel:[0,1,0] op_sel_hi:[1,1,1]\n"
    "v_pk_fma_f32 v[26:27], v[110:111], v[80:81], v[158:159] op_sel:[0,1,0] op_sel_hi:[1,1,1]\n"
    "v_pk_fma_f32 v[28:29], v[132:133], v[80:81], v[180:181] op_sel:[0,1,0] op_sel_hi:[1,1,1]\n"
    "v_pk_fma_f32 v[30:31], v[134:135], v[80:81], v[182:183] op_sel:[0,1,0] op_sel_hi:[1,1,1]\n"
    "v_pk_fma_f32 v[184:185], v[184:185], v[80:81], v[24:25] op_sel_hi:[1,0,1]\n"
    "v_pk_fma_f32 v[186:187], v[186:187], v[80:81], v[26:27] op_sel_hi:[1,0,1]\n"
    "v_pk_fma_f32 v[188:189], v[188:189], v[80:81], v[28:29] op_sel_hi:[1,0,1]\n"
    "v_pk_fma_f32 v[190:191], v[190:191], v[80:81], v[30:31] op_sel_hi:[1,0,1]\n"
    "v_pk_mul_f32 v[24:25], v[184:185], v[184:185]\n"
    "v_pk_mul_f32 v[26:27], v[186:187], v[186:187]\n"
    "v_pk_mul_f32 v[28:29], v[188:189], v[188:189]\n"
    "v_pk_mul_f32 v[30:31], v[190:191], v[190:191]\n"
    "v_pk_fma_f32 v[24:25], v[24:25], s[96:97], s[96:97] op_sel:[0,1,0] op_sel_hi:[1,1,0]\n"
    "v_pk_fma_f32 v[26:27], v[26:27], s[96:97], s[96:97] op_sel:[0,1,0] op_sel_hi:[1,1,0]\n"
    "v_pk_fma_f32 v[28:29], v[28:29], s[96:97], s[96:97] op_sel:[0,1,0] op_sel_hi:[1,1,0]\n"
    "v_pk_fma_f32 v[30:31], v[30:31], s[96:97], s[96:97] op_sel:[0,1,0] op_sel_hi:[1,1,0]\n"
    "v_pk_mul_f32 v[24:25], v[24:25], v[184:185]\n"
    "v_pk_mul_f32 v[26:27], v[26:27], v[186:187]\n"
    "v_pk_mul_f32 v[28:29], v[28:29], v[188:189]\n"
    "v_pk_mul_f32 v[30:31], v[30:31], v[190:191]\n"
    "v_exp_f32 v24, v24\n"
    "v_exp_f32 v25, v25\n"
    "v_exp_f32 v26, v26\n"
    "v_exp_f32 v27, v27\n"
    "v_exp_f32 v28, v28\n"
    "v_exp_f32 v29, v29\n"
    "v_exp_f32 v30, v30\n"
    "v_exp_f32 v31, v31\n"
    "v_pk_add_f32 v[24:25], v[24:25], 1.0 op_sel_hi:[1,0]\n"
    "v_pk_add_f32 v[26:27], v[26:27], 1.0 op_sel_hi:[1,0]\n"
    "v_pk_add_f32 v[28:29], v[28:29], 1.0 op_sel_hi:[1,0]\n"
    "v_pk_add_f32 v[30:31], v[30:31], 1.0 op_sel_hi:[1,0]\n"
    "v_rcp_f32 v24, v24\n"
    "v_rcp_f32 v25, v25\n"
    "v_rcp_f32 v26, v26\n"
    "v_rcp_f32 v27, v27\n"
    "v_rcp_f32 v28, v28\n"
    "v_rcp_f32 v29, v29\n"
    "v_rcp_f32 v30, v30\n"
    "v_rcp_f32 v31, v31\n"
    "v_pk_mul_f32 v[184:185], v[184:185], v[24:25]\n"
    "v_pk_mul_f32 v[186:187], v[186:187], v[26:27]\n"
    "v_pk_mul_f32 v[188:189], v[188:189], v[28:29]\n"
    "v_pk_mul_f32 v[190:191], v[190:191], v[30:31]\n"
    "v_cvt_pk_bf16_f32 v184, v184, v185\n"
    "v_cvt_pk_bf16_f32 v185, v186, v187\n"
    "v_cvt_pk_bf16_f32 v186, v188, v189\n"
    "v_cvt_pk_bf16_f32 v187, v190, v191\n"
    "buffer_store_dwordx4 v[184:187], v23, s[44:47], s99 offen offset:2048 sc1\n"
    "v_pk_fma_f32 v[24:25], v[108:109], v[82:83], v[156:157] op_sel:[0,1,0] op_sel_hi:[1,1,1]\n"
    "v_pk_fma_f32 v[26:27], v[110:111], v[82:83], v[158:159] op_sel:[0,1,0] op_sel_hi:[1,1,1]\n"
    "v_pk_fma_f32 v[28:29], v[132:133], v[82:83], v[180:181] op_sel:[0,1,0] op_sel_hi:[1,1,1]\n"
    "v_pk_fma_f32 v[30:31], v[134:135], v[82:83], v[182:183] op_sel:[0,1,0] op_sel_hi:[1,1,1]\n"
    "v_pk_fma_f32 v[208:209], v[208:209], v[82:83], v[24:25] op_sel_hi:[1,0,1]\n"
    "v_pk_fma_f32 v[210:211], v[210:211], v[82:83], v[26:27] op_sel_hi:[1,0,1]\n"
    "v_pk_fma_f32 v[212:213], v[212:213], v[82:83], v[28:29] op_sel_hi:[1,0,1]\n"
    "v_pk_fma_f32 v[214:215], v[214:215], v[82:83], v[30:31] op_sel_hi:[1,0,1]\n"
    "v_pk_mul_f32 v[24:25], v[208:209], v[208:209]\n"
    "v_pk_mul_f32 v[26:27], v[210:211], v[210:211]\n"
    "v_pk_mul_f32 v[28:29], v[212:213], v[212:213]\n"
    "v_pk_mul_f32 v[30:31], v[214:215], v[214:215]\n"
    "v_pk_fma_f32 v[24:25], v[24:25], s[96:97], s[96:97] op_sel:[0,1,0] op_sel_hi:[1,1,0]\n"
    "v_pk_fma_f32 v[26:27], v[26:27], s[96:97], s[96:97] op_sel:[0,1,0] op_sel_hi:[1,1,0]\n"
    "v_pk_fma_f32 v[28:29], v[28:29], s[96:97], s[96:97] op_sel:[0,1,0] op_sel_hi:[1,1,0]\n"
    "v_pk_fma_f32 v[30:31], v[30:31], s[96:97], s[96:97] op_sel:[0,1,0] op_sel_hi:[1,1,0]\n"
    "v_pk_mul_f32 v[24:25], v[24:25], v[208:209]\n"
    "v_pk_mul_f32 v[26:27], v[26:27], v[210:211]\n"
    "v_pk_mul_f32 v[28:29], v[28:29], v[212:213]\n"
    "v_pk_mul_f32 v[30:31], v[30:31], v[214:215]\n"
    "v_exp_f32 v24, v24\n"
    "v_exp_f32 v25, v25\n"
    "v_exp_f32 v26, v26\n"
    "v_exp_f32 v27, v27\n"
    "v_exp_f32 v28, v28\n"
    "v_exp_f32 v29, v29\n"
    "v_exp_f32 v30, v30\n"
    "v_exp_f32 v31, v31\n"
    "v_pk_add_f32 v[24:25], v[24:25], 1.0 op_sel_hi:[1,0]\n"
    "v_pk_add_f32 v[26:27], v[26:27], 1.0 op_sel_hi:[1,0]\n"
    "v_pk_add_f32 v[28:29], v[28:29], 1.0 op_sel_hi:[1,0]\n"
    "v_pk_add_f32 v[30:31], v[30:31], 1.0 op_sel_hi:[1,0]\n"
    "v_rcp_f32 v24, v24\n"
    "v_rcp_f32 v25, v25\n"
    "v_rcp_f32 v26, v26\n"
    "v_rcp_f32 v27, v27\n"
    "v_rcp_f32 v28, v28\n"
    "v_rcp_f32 v29, v29\n"
    "v_rcp_f32 v30, v30\n"
    "v_rcp_f32 v31, v31\n"
    "v_pk_mul_f32 v[208:209], v[208:209], v[24:25]\n"
    "v_pk_mul_f32 v[210:211], v[210:211], v[26:27]\n"
    "v_pk_mul_f32 v[212:213], v[212:213], v[28:29]\n"
    "v_pk_mul_f32 v[214:215], v[214:215], v[30:31]\n"
    "v_cvt_pk_bf16_f32 v208, v208, v209\n"
    "v_cvt_pk_bf16_f32 v209, v210, v211\n"
    "v_cvt_pk_bf16_f32 v210, v212, v213\n"
    "v_cvt_pk_bf16_f32 v211, v214, v215\n"
    "buffer_store_dwordx4 v[208:211], v23, s[44:47], s99 offen offset:3072 sc1\n"
    "s_waitcnt lgkmcnt(0)\n"
    "s_add_u32 s99, s60, s64\n"
    "v_pk_fma_f32 v[24:25], v[0:1], v[16:17], v[8:9] op_sel:[0,1,0] op_sel_hi:[1,1,1]\n"
    "v_pk_fma_f32 v[26:27], v[2:3], v[16:17], v[10:11] op_sel:[0,1,0] op_sel_hi:[1,1,1]\n"
    "v_pk_fma_f32 v[28:29], v[4:5], v[16:17], v[12:13] op_sel:[0,1,0] op_sel_hi:[1,1,1]\n"
    "v_pk_fma_f32 v[30:31], v[6:7], v[16:17], v[14:15] op_sel:[0,1,0] op_sel_hi:[1,1,1]\n"
    "v_pk_fma_f32 v[48:49], v[48:49], v[16:17], v[24:25] op_sel_hi:[1,0,1]\n"
    "v_pk_fma_f32 v[50:51], v[50:51], v[16:17], v[26:27] op_sel_hi:[1,0,1]\n"
    "v_pk_fma_f32 v[52:53], v[52:53], v[16:17], v[28:29] op_sel_hi:[1,0,1]\n"
    "v_pk_fma_f32 v[54:55], v[54:55], v[16:17], v[30:31] op_sel_hi:[1,0,1]\n"
    "v_pk_mul_f32 v[24:25], v[48:49], v[48:49]\n"
    "v_pk_mul_f32 v[26:27], v[50:51], v[50:51]\n"
    "v_pk_mul_f32 v[28:29], v[52:53], v[52:53]\n"
    "v_pk_mul_f32 v[30:31], v[54:55], v[54:55]\n"
    "v_pk_fma_f32 v[24:25], v[24:25], s[96:97], s[96:97] op_sel:[0,1,0] op_sel_hi:[1,1,0]\n"
    "v_pk_fma_f32 v[26:27], v[26:27], s[96:97], s[96:97] op_sel:[0,1,0] op_sel_hi:[1,1,0]\n"
    "v_pk_fma_f32 v[28:29], v[28:29], s[96:97], s[96:97] op_sel:[0,1,0] op_sel_hi:[1,1,0]\n"
    "v_pk_fma_f32 v[30:31], v[30:31], s[96:97], s[96:97] op_sel:[0,1,0] op_sel_hi:[1,1,0]\n"
    "v_pk_mul_f32 v[24:25], v[24:25], v[48:49]\n"
    "v_pk_mul_f32 v[26:27], v[26:27], v[50:51]\n"
    "v_pk_mul_f32 v[28:29], v[28:29], v[52:53]\n"
    "v_pk_mul_f32 v[30:31], v[30:31], v[54:55]\n"
    "v_exp_f32 v24, v24\n"
    "v_exp_f32 v25, v25\n"
    "v_exp_f32 v26, v26\n"
    "v_exp_f32 v27, v27\n"
    "v_exp_f32 v28, v28\n"
    "v_exp_f32 v29, v29\n"
    "v_exp_f32 v30, v30\n"
    "v_exp_f32 v31, v31\n"
    "v_pk_add_f32 v[24:25], v[24:25], 1.0 op_sel_hi:[1,0]\n"
    "v_pk_add_f32 v[26:27], v[26:27], 1.0 op_sel_hi:[1,0]\n"
    "v_pk_add_f32 v[28:29], v[28:29], 1.0 op_sel_hi:[1,0]\n"
    "v_pk_add_f32 v[30:31], v[30:31], 1.0 op_sel_hi:[1,0]\n"
    "v_rcp_f32 v24, v24\n"
    "v_rcp_f32 v25, v25\n"
    "v_rcp_f32 v26, v26\n"
    "v_rcp_f32 v27, v27\n"
    "v_rcp_f32 v28, v28\n"
    "v_rcp_f32 v29, v29\n"
    "v_rcp_f32 v30, v30\n"
    "v_rcp_f32 v31, v31\n"
    "v_pk_mul_f32 v[48:49], v[48:49], v[24:25]\n"
    "v_pk_mul_f32 v[50:51], v[50:51], v[26:27]\n"
    "v_pk_mul_f32 v[52:53], v[52:53], v[28:29]\n"
    "v_pk_mul_f32 v[54:55], v[54:55], v[30:31]\n"
    "v_cvt_pk_bf16_f32 v36, v48, v49\n"
    "v_cvt_pk_bf16_f32 v37, v50, v51\n"
    "v_cvt_pk_bf16_f32 v38, v52, v53\n"
    "v_cvt_pk_bf16_f32 v39, v54, v55\n"
    "buffer_store_dwordx4 v[36:39], v23, s[44:47], s28 offen sc1\n"
    "v_pk_fma_f32 v[24:25], v[0:1], v[18:19], v[8:9] op_sel:[0,1,0] op_sel_hi:[1,1,1]\n"
    "v_pk_fma_f32 v[26:27], v[2:3], v[18:19], v[10:11] op_sel:[0,1,0] op_sel_hi:[1,1,1]\n"
    "v_pk_fma_f32 v[28:29], v[4:5], v[18:19], v[12:13] op_sel:[0,1,0] op_sel_hi:[1,1,1]\n"
    "v_pk_fma_f32 v[30:31], v[6:7], v[18:19], v[14:15] op_sel:[0,1,0] op_sel_hi:[1,1,1]\n"
    "v_pk_fma_f32 v[72:73], v[72:73], v[18:19], v[24:25] op_sel_hi:[1,0,1]\n"
    "v_pk_fma_f32 v[74:75], v[74:75], v[18:19], v[26:27] op_sel_hi:[1,0,1]\n"
    "v_pk_fma_f32 v[76:77], v[76:77], v[18:19], v[28:29] op_sel_hi:[1,0,1]\n"
    "v_pk_fma_f32 v[78:79], v[78:79], v[18:19], v[30:31] op_sel_hi:[1,0,1]\n"
    "v_pk_mul_f32 v[24:25], v[72:73], v[72:73]\n"
    "v_pk_mul_f32 v[26:27], v[74:75], v[74:75]\n"
    "v_pk_mul_f32 v[28:29], v[76:77], v[76:77]\n"
    "v_pk_mul_f32 v[30:31], v[78:79], v[78:79]\n"
    "v_pk_fma_f32 v[24:25], v[24:25], s[96:97], s[96:97] op_sel:[0,1,0] op_sel_hi:[1,1,0]\n"
    "v_pk_fma_f32 v[26:27], v[26:27], s[96:97], s[96:97] op_sel:[0,1,0] op_sel_hi:[1,1,0]\n"
    "v_pk_fma_f32 v[28:29], v[28:29], s[96:97], s[96:97] op_sel:[0,1,0] op_sel_hi:[1,1,0]\n"
    "v_pk_fma_f32 v[30:31], v[30:31], s[96:97], s[96:97] op_sel:[0,1,0] op_sel_hi:[1,1,0]\n"
    "v_pk_mul_f32 v[24:25], v[24:25], v[72:73]\n"
    "v_pk_mul_f32 v[26:27], v[26:27], v[74:75]\n"
    "v_pk_mul_f32 v[28:29], v[28:29], v[76:77]\n"
    "v_pk_mul_f32 v[30:31], v[30:31], v[78:79]\n"
    "v_exp_f32 v24, v24\n"
    "v_exp_f32 v25, v25\n"
    "v_exp_f32 v26, v26\n"
    "v_exp_f32 v27, v27\n"
    "v_exp_f32 v28, v28\n"
    "v_exp_f32 v29, v29\n"
    "v_exp_f32 v30, v30\n"
    "v_exp_f32 v31, v31\n"
    "v_pk_add_f32 v[24:25], v[24:25], 1.0 op_sel_hi:[1,0]\n"
    "v_pk_add_f32 v[26:27], v[26:27], 1.0 op_sel_hi:[1,0]\n"
    "v_pk_add_f32 v[28:29], v[28:29], 1.0 op_sel_hi:[1,0]\n"
    "v_pk_add_f32 v[30:31], v[30:31], 1.0 op_sel_hi:[1,0]\n"
    "v_rcp_f32 v24, v24\n"
    "v_rcp_f32 v25, v25\n"
    "v_rcp_f32 v26, v26\n"
    "v_rcp_f32 v27, v27\n"
    "v_rcp_f32 v28, v28\n"
    "v_rcp_f32 v29, v29\n"
    "v_rcp_f32 v30, v30\n"
    "v_rcp_f32 v31, v31\n"
    "v_pk_mul_f32 v[72:73], v[72:73], v[24:25]\n"
    "v_pk_mul_f32 v[74:75], v[74:75], v[26:27]\n"
    "v_pk_mul_f32 v[76:77], v[76:77], v[28:29]\n"
    "v_pk_mul_f32 v[78:79], v[78:79], v[30:31]\n"
    "v_cvt_pk_bf16_f32 v60, v72, v73\n"
    "v_cvt_pk_bf16_f32 v61, v74, v75\n"
    "v_cvt_pk_bf16_f32 v62, v76, v77\n"
    "v_cvt_pk_bf16_f32 v63, v78, v79\n"
    "buffer_store_dwordx4 v[60:63], v23, s[44:47], s28 offen offset:1024 sc1\n"
    "v_pk_fma_f32 v[24:25], v[0:1], v[32:33], v[8:9] op_sel:[0,1,0] op_sel_hi:[1,1,1]\n"
    "v_pk_fma_f32 v[26:27], v[2:3], v[32:33], v[10:11] op_sel:[0,1,0] op_sel_hi:[1,1,1]\n"
    "v_pk_fma_f32 v[28:29], v[4:5], v[32:33], v[12:13] op_sel:[0,1,0] op_sel_hi:[1,1,1]\n"
    "v_pk_fma_f32 v[30:31], v[6:7], v[32:33], v[14:15] op_sel:[0,1,0] op_sel_hi:[1,1,1]\n"
    "v_pk_fma_f32 v[96:97], v[96:97], v[32:33], v[24:25] op_sel_hi:[1,0,1]\n"
    "v_pk_fma_f32 v[98:99], v[98:99], v[32:33], v[26:27] op_sel_hi:[1,0,1]\n"
    "v_pk_fma_f32 v[100:101], v[100:101], v[32:33], v[28:29] op_sel_hi:[1,0,1]\n"
    "v_pk_fma_f32 v[102:103], v[102:103], v[32:33], v[30:31] op_sel_hi:[1,0,1]\n"
    "v_pk_mul_f32 v[24:25], v[96:97], v[96:97]\n"
    "v_pk_mul_f32 v[26:27], v[98:99], v[98:99]\n"
    "v_pk_mul_f32 v[28:29], v[100:101], v[100:101]\n"
    "v_pk_mul_f32 v[30:31], v[102:103], v[102:103]\n"
    "v_pk_fma_f32 v[24:25], v[24:25], s[96:97], s[96:97] op_sel:[0,1,0] op_sel_hi:[1,1,0]\n"
    "v_pk_fma_f32 v[26:27], v[26:27], s[96:97], s[96:97] op_sel:[0,1,0] op_sel_hi:[1,1,0]\n"
    "v_pk_fma_f32 v[28:29], v[28:29], s[96:97], s[96:97] op_sel:[0,1,0] op_sel_hi:[1,1,0]\n"
    "v_pk_fma_f32 v[30:31], v[30:31], s[96:97], s[96:97] op_sel:[0,1,0] op_sel_hi:[1,1,0]\n"
    "v_pk_mul_f32 v[24:25], v[24:25], v[96:97]\n"
    "v_pk_mul_f32 v[26:27], v[26:27], v[98:99]\n"
    "v_pk_mul_f32 v[28:29], v[28:29], v[100:101]\n"
    "v_pk_mul_f32 v[30:31], v[30:31], v[102:103]\n"
    "v_exp_f32 v24, v24\n"
    "v_exp_f32 v25, v25\n"
    "v_exp_f32 v26, v26\n"
    "v_exp_f32 v27, v27\n"
    "v_exp_f32 v28, v28\n"
    "v_exp_f32 v29, v29\n"
    "v_exp_f32 v30, v30\n"
    "v_exp_f32 v31, v31\n"
    "v_pk_add_f32 v[24:25], v[24:25], 1.0 op_sel_hi:[1,0]\n"
    "v_pk_add_f32 v[26:27], v[26:27], 1.0 op_sel_hi:[1,0]\n"
    "v_pk_add_f32 v[28:29], v[28:29], 1.0 op_sel_hi:[1,0]\n"
    "v_pk_add_f32 v[30:31], v[30:31], 1.0 op_sel_hi:[1,0]\n"
    "v_rcp_f32 v24, v24\n"
    "v_rcp_f32 v25, v25\n"
    "v_rcp_f32 v26, v26\n"
    "v_rcp_f32 v27, v27\n"
    "v_rcp_f32 v28, v28\n"
    "v_rcp_f32 v29, v29\n"
    "v_rcp_f32 v30, v30\n"
    "v_rcp_f32 v31, v31\n"
    "v_pk_mul_f32 v[96:97], v[96:97], v[24:25]\n"
    "v_pk_mul_f32 v[98:99], v[98:99], v[26:27]\n"
    "v_pk_mul_f32 v[100:101], v[100:101], v[28:29]\n"
    "v_pk_mul_f32 v[102:103], v[102:103], v[30:31]\n"
    "v_cvt_pk_bf16_f32 v84, v96, v97\n"
    "v_cvt_pk_bf16_f32 v85, v98, v99\n"
    "v_cvt_pk_bf16_f32 v86, v100, v101\n"
    "v_cvt_pk_bf16_f32 v87, v102, v103\n"
    "s_waitcnt vmcnt(2)\n"
    "s_add_u32 s33, s92, 4\n"
    "v_mov_b32 v24, s33\n"
    "s_mov_b64 exec, 1\n"
    "buffer_store_dword v24, off, s[44:47], s66 sc1\n"
    "s_mov_b64 exec, -1\n"
    "buffer_store_dwordx4 v[84:87], v23, s[44:47], s28 offen offset:2048 sc1\n"
    "v_pk_fma_f32 v[24:25], v[0:1], v[34:35], v[8:9] op_sel:[0,1,0] op_sel_hi:[1,1,1]\n"
    "v_pk_fma_f32 v[26:27], v[2:3], v[34:35], v[10:11] op_sel:[0,1,0] op_sel_hi:[1,1,1]\n"
    "v_pk_fma_f32 v[28:29], v[4:5], v[34:35], v[12:13] op_sel:[0,1,0] op_sel_hi:[1,1,1]\n"
    "v_pk_fma_f32 v[30:31], v[6:7], v[34:35], v[14:15] op_sel:[0,1,0] op_sel_hi:[1,1,1]\n"
    "v_pk_fma_f32 v[120:121], v[120:121], v[34:35], v[24:25] op_sel_hi:[1,0,1]\n"
    "v_pk_fma_f32 v[122:123], v[122:123], v[34:35], v[26:27] op_sel_hi:[1,0,1]\n"
    "v_pk_fma_f32 v[124:125], v[124:125], v[34:35], v[28:29] op_sel_hi:[1,0,1]\n"
    "v_pk_fma_f32 v[126:127], v[126:127], v[34:35], v[30:31] op_sel_hi:[1,0,1]\n"
    "v_pk_mul_f32 v[24:25], v[120:121], v[120:121]\n"
    "v_pk_mul_f32 v[26:27], v[122:123], v[122:123]\n"
    "v_pk_mul_f32 v[28:29], v[124:125], v[124:125]\n"
    "v_pk_mul_f32 v[30:31], v[126:127], v[126:127]\n"
    "v_pk_fma_f32 v[24:25], v[24:25], s[96:97], s[96:97] op_sel:[0,1,0] op_sel_hi:[1,1,0]\n"
    "v_pk_fma_f32 v[26:27], v[26:27], s[96:97], s[96:97] op_sel:[0,1,0] op_sel_hi:[1,1,0]\n"
    "v_pk_fma_f32 v[28:29], v[28:29], s[96:97], s[96:97] op_sel:[0,1,0] op_sel_hi:[1,1,0]\n"
    "v_pk_fma_f32 v[30:31], v[30:31], s[96:97], s[96:97] op_sel:[0,1,0] op_sel_hi:[1,1,0]\n"
    "v_pk_mul_f32 v[24:25], v[24:25], v[120:121]\n"
    "v_pk_mul_f32 v[26:27], v[26:27], v[122:123]\n"
    "v_pk_mul_f32 v[28:29], v[28:29], v[124:125]\n"
    "v_pk_mul_f32 v[30:31], v[30:31], v[126:127]\n"
    "v_exp_f32 v24, v24\n"
    "v_exp_f32 v25, v25\n"
    "v_exp_f32 v26, v26\n"
    "v_exp_f32 v27, v27\n"
    "v_exp_f32 v28, v28\n"
    "v_exp_f32 v29, v29\n"
    "v_exp_f32 v30, v30\n"
    "v_exp_f32 v31, v31\n"
    "v_pk_add_f32 v[24:25], v[24:25], 1.0 op_sel_hi:[1,0]\n"
    "v_pk_add_f32 v[26:27], v[26:27], 1.0 op_sel_hi:[1,0]\n"
    "v_pk_add_f32 v[28:29], v[28:29], 1.0 op_sel_hi:[1,0]\n"
    "v_pk_add_f32 v[30:31], v[30:31], 1.0 op_sel_hi:[1,0]\n"
    "v_rcp_f32 v24, v24\n"
    "v_rcp_f32 v25, v25\n"
    "v_rcp_f32 v26, v26\n"
    "v_rcp_f32 v27, v27\n"
    "v_rcp_f32 v28, v28\n"
    "v_rcp_f32 v29, v29\n"
    "v_rcp_f32 v30, v30\n"
    "v_rcp_f32 v31, v31\n"
    "v_pk_mul_f32 v[120:121], v[120:121], v[24:25]\n"
    "v_pk_mul_f32 v[122:123], v[122:123], v[26:27]\n"
    "v_pk_mul_f32 v[124:125], v[124:125], v[28:29]\n"
    "v_pk_mul_f32 v[126:127], v[126:127], v[30:31]\n"
    "v_cvt_pk_bf16_f32 v108, v120, v121\n"
    "v_cvt_pk_bf16_f32 v109, v122, v123\n"
    "v_cvt_pk_bf16_f32 v110, v124, v125\n"
    "v_cvt_pk_bf16_f32 v111, v126, v127\n"
    "buffer_store_dwordx4 v[108:111], v23, s[44:47], s28 offen offset:3072 sc1\n"
    "s_add_u32 s99, s61, s64\n"
    "v_pk_fma_f32 v[24:25], v[0:1], v[56:57], v[8:9] op_sel:[0,1,0] op_sel_hi:[1,1,1]\n"
    "v_pk_fma_f32 v[26:27], v[2:3], v[56:57], v[10:11] op_sel:[0,1,0] op_sel_hi:[1,1,1]\n"
    "v_pk_fma_f32 v[28:29], v[4:5], v[56:57], v[12:13] op_sel:[0,1,0] op_sel_hi:[1,1,1]\n"
    "v_pk_fma_f32 v[30:31], v[6:7], v[56:57], v[14:15] op_sel:[0,1,0] op_sel_hi:[1,1,1]\n"
    "v_pk_fma_f32 v[144:145], v[144:145], v[56:57], v[24:25] op_sel_hi:[1,0,1]\n"
    "v_pk_fma_f32 v[146:147], v[146:147], v[56:57], v[26:27] op_sel_hi:[1,0,1]\n"
    "v_pk_fma_f32 v[148:149], v[148:149], v[56:57], v[28:29] op_sel_hi:[1,0,1]\n"
    "v_pk_fma_f32 v[150:151], v[150:151], v[56:57], v[30:31] op_sel_hi:[1,0,1]\n"
    "v_pk_mul_f32 v[24:25], v[144:145], v[144:145]\n"
    "v_pk_mul_f32 v[26:27], v[146:147], v[146:147]\n"
    "v_pk_mul_f32 v[28:29], v[148:149], v[148:149]\n"
    "v_pk_mul_f32 v[30:31], v[150:151], v[150:151]\n"
    "v_pk_fma_f32 v[24:25], v[24:25], s[96:97], s[96:97] op_sel:[0,1,0] op_sel_hi:[1,1,0]\n"
    "v_pk_fma_f32 v[26:27], v[26:27], s[96:97], s[96:97] op_sel:[0,1,0] op_sel_hi:[1,1,0]\n"
    "v_pk_fma_f32 v[28:29], v[28:29], s[96:97], s[96:97] op_sel:[0,1,0] op_sel_hi:[1,1,0]\n"
    "v_pk_fma_f32 v[30:31], v[30:31], s[96:97], s[96:97] op_sel:[0,1,0] op_sel_hi:[1,1,0]\n"
    "v_pk_mul_f32 v[24:25], v[24:25], v[144:145]\n"
    "v_pk_mul_f32 v[26:27], v[26:27], v[146:147]\n"
    "v_pk_mul_f32 v[28:29], v[28:29], v[148:149]\n"
    "v_pk_mul_f32 v[30:31], v[30:31], v[150:151]\n"
    "v_exp_f32 v24, v24\n"
    "v_exp_f32 v25, v25\n"
    "v_exp_f32 v26, v26\n"
    "v_exp_f32 v27, v27\n"
    "v_exp_f32 v28, v28\n"
    "v_exp_f32 v29, v29\n"
    "v_exp_f32 v30, v30\n"
    "v_exp_f32 v31, v31\n"
    "v_pk_add_f32 v[24:25], v[24:25], 1.0 op_sel_hi:[1,0]\n"
    "v_pk_add_f32 v[26:27], v[26:27], 1.0 op_sel_hi:[1,0]\n"
    "v_pk_add_f32 v[28:29], v[28:29], 1.0 op_sel_hi:[1,0]\n"
    "v_pk_add_f32 v[30:31], v[30:31], 1.0 op_sel_hi:[1,0]\n"
    "v_rcp_f32 v24, v24\n"
    "v_rcp_f32 v25, v25\n"
    "v_rcp_f32 v26, v26\n"
    "v_rcp_f32 v27, v27\n"
    "v_rcp_f32 v28, v28\n"
    "v_rcp_f32 v29, v29\n"
    "v_rcp_f32 v30, v30\n"
    "v_rcp_f32 v31, v31\n"
    "v_pk_mul_f32 v[144:145], v[144:145], v[24:25]\n"
    "v_pk_mul_f32 v[146:147], v[146:147], v[26:27]\n"
    "v_pk_mul_f32 v[148:149], v[148:149], v[28:29]\n"
    "v_pk_mul_f32 v[150:151], v[150:151], v[30:31]\n"
    "v_cvt_pk_bf16_f32 v132, v144, v145\n"
    "v_cvt_pk_bf16_f32 v133, v146, v147\n"
    "v_cvt_pk_bf16_f32 v134, v148, v149\n"
    "v_cvt_pk_bf16_f32 v135, v150, v151\n"
    "v_pk_fma_f32 v[24:25], v[0:1], v[58:59], v[8:9] op_sel:[0,1,0] op_sel_hi:[1,1,1]\n"
    "v_pk_fma_f32 v[26:27], v[2:3], v[58:59], v[10:11] op_sel:[0,1,0] op_sel_hi:[1,1,1]\n"
    "v_pk_fma_f32 v[28:29], v[4:5], v[58:59], v[12:13] op_sel:[0,1,0] op_sel_hi:[1,1,1]\n"
    "v_pk_fma_f32 v[30:31], v[6:7], v[58:59], v[14:15] op_sel:[0,1,0] op_sel_hi:[1,1,1]\n"
    "v_pk_fma_f32 v[168:169], v[168:169], v[58:59], v[24:25] op_sel_hi:[1,0,1]\n"
    "v_pk_fma_f32 v[170:171], v[170:171], v[58:59], v[26:27] op_sel_hi:[1,0,1]\n"
    "v_pk_fma_f32 v[172:173], v[172:173], v[58:59], v[28:29] op_sel_hi:[1,0,1]\n"
    "v_pk_fma_f32 v[174:175], v[174:175], v[58:59], v[30:31] op_sel_hi:[1,0,1]\n"
    "v_pk_mul_f32 v[24:25], v[168:169], v[168:169]\n"
    "v_pk_mul_f32 v[26:27], v[170:171], v[170:171]\n"
    "v_pk_mul_f32 v[28:29], v[172:173], v[172:173]\n"
    "v_pk_mul_f32 v[30:31], v[174:175], v[174:175]\n"
    "v_pk_fma_f32 v[24:25], v[24:25], s[96:97], s[96:97] op_sel:[0,1,0] op_sel_hi:[1,1,0]\n"
    "v_pk_fma_f32 v[26:27], v[26:27], s[96:97], s[96:97] op_sel:[0,1,0] op_sel_hi:[1,1,0]\n"
    "v_pk_fma_f32 v[28:29], v[28:29], s[96:97], s[96:97] op_sel:[0,1,0] op_sel_hi:[1,1,0]\n"
    "v_pk_fma_f32 v[30:31], v[30:31], s[96:97], s[96:97] op_sel:[0,1,0] op_sel_hi:[1,1,0]\n"
    "v_pk_mul_f32 v[24:25], v[24:25], v[168:169]\n"
    "v_pk_mul_f32 v[26:27], v[26:27], v[170:171]\n"
    "v_pk_mul_f32 v[28:29], v[28:29], v[172:173]\n"
    "v_pk_mul_f32 v[30:31], v[30:31], v[174:175]\n"
    "v_exp_f32 v24, v24\n"
    "v_exp_f32 v25, v25\n"
    "v_exp_f32 v26, v26\n"
    "v_exp_f32 v27, v27\n"
    "v_exp_f32 v28, v28\n"
    "v_exp_f32 v29, v29\n"
    "v_exp_f32 v30, v30\n"
    "v_exp_f32 v31, v31\n"
    "v_pk_add_f32 v[24:25], v[24:25], 1.0 op_sel_hi:[1,0]\n"
    "v_pk_add_f32 v[26:27], v[26:27], 1.0 op_sel_hi:[1,0]\n"
    "v_pk_add_f32 v[28:29], v[28:29], 1.0 op_sel_hi:[1,0]\n"
    "v_pk_add_f32 v[30:31], v[30:31], 1.0 op_sel_hi:[1,0]\n"
    "v_rcp_f32 v24, v24\n"
    "v_rcp_f32 v25, v25\n"
    "v_rcp_f32 v26, v26\n"
    "v_rcp_f32 v27, v27\n"
    "v_rcp_f32 v28, v28\n"
    "v_rcp_f32 v29, v29\n"
    "v_rcp_f32 v30, v30\n"
    "v_rcp_f32 v31, v31\n"
    "v_pk_mul_f32 v[168:169], v[168:169], v[24:25]\n"
    "v_pk_mul_f32 v[170:171], v[170:171], v[26:27]\n"
    "v_pk_mul_f32 v[172:173], v[172:173], v[28:29]\n"
    "v_pk_mul_f32 v[174:175], v[174:175], v[30:31]\n"
    "v_cvt_pk_bf16_f32 v156, v168, v169\n"
    "v_cvt_pk_bf16_f32 v157, v170, v171\n"
    "v_cvt_pk_bf16_f32 v158, v172, v173\n"
    "v_cvt_pk_bf16_f32 v159, v174, v175\n"
    "v_pk_fma_f32 v[24:25], v[0:1], v[80:81], v[8:9] op_sel:[0,1,0] op_sel_hi:[1,1,1]\n"
    "v_pk_fma_f32 v[26:27], v[2:3], v[80:81], v[10:11] op_sel:[0,1,0] op_sel_hi:[1,1,1]\n"
    "v_pk_fma_f32 v[28:29], v[4:5], v[80:81], v[12:13] op_sel:[0,1,0] op_sel_hi:[1,1,1]\n"
    "v_pk_fma_f32 v[30:31], v[6:7], v[80:81], v[14:15] op_sel:[0,1,0] op_sel_hi:[1,1,1]\n"
    "v_pk_fma_f32 v[192:193], v[192:193], v[80:81], v[24:25] op_sel_hi:[1,0,1]\n"
    "v_pk_fma_f32 v[194:195], v[194:195], v[80:81], v[26:27] op_sel_hi:[1,0,1]\n"
    "v_pk_fma_f32 v[196:197], v[196:197], v[80:81], v[28:29] op_sel_hi:[1,0,1]\n"
    "v_pk_fma_f32 v[198:199], v[198:199], v[80:81], v[30:31] op_sel_hi:[1,0,1]\n"
    "v_pk_mul_f32 v[24:25], v[192:193], v[192:193]\n"
    "v_pk_mul_f32 v[26:27], v[194:195], v[194:195]\n"
    "v_pk_mul_f32 v[28:29], v[196:197], v[196:197]\n"
    "v_pk_mul_f32 v[30:31], v[198:199], v[198:199]\n"
    "v_pk_fma_f32 v[24:25], v[24:25], s[96:97], s[96:97] op_sel:[0,1,0] op_sel_hi:[1,1,0]\n"
    "v_pk_fma_f32 v[26:27], v[26:27], s[96:97], s[96:97] op_sel:[0,1,0] op_sel_hi:[1,1,0]\n"
    "v_pk_fma_f32 v[28:29], v[28:29], s[96:97], s[96:97] op_sel:[0,1,0] op_sel_hi:[1,1,0]\n"
    "v_pk_fma_f32 v[30:31], v[30:31], s[96:97], s[96:97] op_sel:[0,1,0] op_sel_hi:[1,1,0]\n"
    "v_pk_mul_f32 v[24:25], v[24:25], v[192:193]\n"
    "v_pk_mul_f32 v[26:27], v[26:27], v[194:195]\n"
    "v_pk_mul_f32 v[28:29], v[28:29], v[196:197]\n"
    "v_pk_mul_f32 v[30:31], v[30:31], v[198:199]\n"
    "v_exp_f32 v24, v24\n"
    "v_exp_f32 v25, v25\n"
    "v_exp_f32 v26, v26\n"
    "v_exp_f32 v27, v27\n"
    "v_exp_f32 v28, v28\n"
    "v_exp_f32 v29, v29\n"
    "v_exp_f32 v30, v30\n"
    "v_exp_f32 v31, v31\n"
    "v_pk_add_f32 v[24:25], v[24:25], 1.0 op_sel_hi:[1,0]\n"
    "v_pk_add_f32 v[26:27], v[26:27], 1.0 op_sel_hi:[1,0]\n"
    "v_pk_add_f32 v[28:29], v[28:29], 1.0 op_sel_hi:[1,0]\n"
    "v_pk_add_f32 v[30:31], v[30:31], 1.0 op_sel_hi:[1,0]\n"
    "v_rcp_f32 v24, v24\n"
    "v_rcp_f32 v25, v25\n"
    "v_rcp_f32 v26, v26\n"
    "v_rcp_f32 v27, v27\n"
    "v_rcp_f32 v28, v28\n"
    "v_rcp_f32 v29, v29\n"
    "v_rcp_f32 v30, v30\n"
    "v_rcp_f32 v31, v31\n"
    "v_pk_mul_f32 v[192:193], v[192:193], v[24:25]\n"
    "v_pk_mul_f32 v[194:195], v[194:195], v[26:27]\n"
    "v_pk_mul_f32 v[196:197], v[196:197], v[28:29]\n"
    "v_pk_mul_f32 v[198:199], v[198:199], v[30:31]\n"
    "v_cvt_pk_bf16_f32 v180, v192, v193\n"
    "v_cvt_pk_bf16_f32 v181, v194, v195\n"
    "v_cvt_pk_bf16_f32 v182, v196, v197\n"
    "v_cvt_pk_bf16_f32 v183, v198, v199\n"
    "v_accvgpr_read_b32 v140, a167\n"
    "v_accvgpr_read_b32 v141, a166\n"
    "v_bfe_u32 v143, v23, 4, 2\n"
    "v_and_b32 v142, 0x3c0, v23\n"
    "v_lshlrev_b32 v143, 1, v143\n"
    "v_add_lshl_u32 v142, v142, v143, 1\n"
    "s_mov_b32 exec_hi, 0\n"
    "buffer_load_dword v143, v142, s[44:47], s82 offen sc1\n"
    "s_mov_b32 exec_hi, -1\n"
    "v_pk_fma_f32 v[24:25], v[0:1], v[82:83], v[8:9] op_sel:[0,1,0] op_sel_hi:[1,1,1]\n"
    "v_pk_fma_f32 v[26:27], v[2:3], v[82:83], v[10:11] op_sel:[0,1,0] op_sel_hi:[1,1,1]\n"
    "v_pk_fma_f32 v[28:29], v[4:5], v[82:83], v[12:13] op_sel:[0,1,0] op_sel_hi:[1,1,1]\n"
    "v_pk_fma_f32 v[30:31], v[6:7], v[82:83], v[14:15] op_sel:[0,1,0] op_sel_hi:[1,1,1]\n"
    "v_pk_fma_f32 v[216:217], v[216:217], v[82:83], v[24:25] op_sel_hi:[1,0,1]\n"
    "v_pk_fma_f32 v[218:219], v[218:219], v[82:83], v[26:27] op_sel_hi:[1,0,1]\n"
    "v_pk_fma_f32 v[220:221], v[220:221], v[82:83], v[28:29] op_sel_hi:[1,0,1]\n"
    "v_pk_fma_f32 v[222:223], v[222:223], v[82:83], v[30:31] op_sel_hi:[1,0,1]\n"
    "v_pk_mul_f32 v[24:25], v[216:217], v[216:217]\n"
    "v_pk_mul_f32 v[26:27], v[218:219], v[218:219]\n"
    "v_pk_mul_f32 v[28:29], v[220:221], v[220:221]\n"
    "v_pk_mul_f32 v[30:31], v[222:223], v[222:223]\n"
    "v_pk_fma_f32 v[24:25], v[24:25], s[96:97], s[96:97] op_sel:[0,1,0] op_sel_hi:[1,1,0]\n"
    "v_pk_fma_f32 v[26:27], v[26:27], s[96:97], s[96:97] op_sel:[0,1,0] op_sel_hi:[1,1,0]\n"
    "v_pk_fma_f32 v[28:29], v[28:29], s[96:97], s[96:97] op_sel:[0,1,0] op_sel_hi:[1,1,0]\n"
    "v_pk_fma_f32 v[30:31], v[30:31], s[96:97], s[96:97] op_sel:[0,1,0] op_sel_hi:[1,1,0]\n"
    "v_pk_mul_f32 v[24:25], v[24:25], v[216:217]\n"
    "v_pk_mul_f32 v[26:27], v[26:27], v[218:219]\n"
    "v_pk_mul_f32 v[28:29], v[28:29], v[220:221]\n"
    "v_pk_mul_f32 v[30:31], v[30:31], v[222:223]\n"
    "v_exp_f32 v24, v24\n"
    "v_exp_f32 v25, v25\n"
    "v_exp_f32 v26, v26\n"
    "v_exp_f32 v27, v27\n"
    "v_exp_f32 v28, v28\n"
    "v_exp_f32 v29, v29\n"
    "v_exp_f32 v30, v30\n"
    "v_exp_f32 v31, v31\n"
    "v_pk_add_f32 v[24:25], v[24:25], 1.0 op_sel_hi:[1,0]\n"
    "v_pk_add_f32 v[26:27], v[26:27], 1.0 op_sel_hi:[1,0]\n"
    "v_pk_add_f32 v[28:29], v[28:29], 1.0 op_sel_hi:[1,0]\n"
    "v_pk_add_f32 v[30:31], v[30:31], 1.0 op_sel_hi:[1,0]\n"
    "v_rcp_f32 v24, v24\n"
    "v_rcp_f32 v25, v25\n"
    "v_rcp_f32 v26, v26\n"
    "v_rcp_f32 v27, v27\n"
    "v_rcp_f32 v28, v28\n"
    "v_rcp_f32 v29, v29\n"
    "v_rcp_f32 v30, v30\n"
    "v_rcp_f32 v31, v31\n"
    "v_pk_mul_f32 v[216:217], v[216:217], v[24:25]\n"
    "v_pk_mul_f32 v[218:219], v[218:219], v[26:27]\n"
    "v_pk_mul_f32 v[220:221], v[220:221], v[28:29]\n"
    "v_pk_mul_f32 v[222:223], v[222:223], v[30:31]\n"
    "v_cvt_pk_bf16_f32 v204, v216, v217\n"
    "v_cvt_pk_bf16_f32 v205, v218, v219\n"
    "v_cvt_pk_bf16_f32 v206, v220, v221\n"
    "v_cvt_pk_bf16_f32 v207, v222, v223\n"
    "v_pk_fma_f32 v[24:25], v[44:45], v[16:17], v[92:93] op_sel:[0,1,0] op_sel_hi:[1,1,1]\n"
    "v_pk_fma_f32 v[26:27], v[46:47], v[16:17], v[94:95] op_sel:[0,1,0] op_sel_hi:[1,1,1]\n"
    "v_pk_fma_f32 v[28:29], v[68:69], v[16:17], v[116:117] op_sel:[0,1,0] op_sel_hi:[1,1,1]\n"
    "v_pk_fma_f32 v[30:31], v[70:71], v[16:17], v[118:119] op_sel:[0,1,0] op_sel_hi:[1,1,1]\n"
    "v_pk_fma_f32 v[224:225], v[224:225], v[16:17], v[24:25] op_sel_hi:[1,0,1]\n"
    "v_pk_fma_f32 v[226:227], v[226:227], v[16:17], v[26:27] op_sel_hi:[1,0,1]\n"
    "v_pk_fma_f32 v[228:229], v[228:229], v[16:17], v[28:29] op_sel_hi:[1,0,1]\n"
    "v_pk_fma_f32 v[230:231], v[230:231], v[16:17], v[30:31] op_sel_hi:[1,0,1]\n"
    "v_pk_mul_f32 v[24:25], v[224:225], v[224:225]\n"
    "v_pk_mul_f32 v[26:27], v[226:227], v[226:227]\n"
    "v_pk_mul_f32 v[28:29], v[228:229], v[228:229]\n"
    "v_pk_mul_f32 v[30:31], v[230:231], v[230:231]\n"
    "v_pk_fma_f32 v[24:25], v[24:25], s[96:97], s[96:97] op_sel:[0,1,0] op_sel_hi:[1,1,0]\n"
    "v_pk_fma_f32 v[26:27], v[26:27], s[96:97], s[96:97] op_sel:[0,1,0] op_sel_hi:[1,1,0]\n"
    "v_pk_fma_f32 v[28:29], v[28:29], s[96:97], s[96:97] op_sel:[0,1,0] op_sel_hi:[1,1,0]\n"
    "v_pk_fma_f32 v[30:31], v[30:31], s[96:97], s[96:97] op_sel:[0,1,0] op_sel_hi:[1,1,0]\n"
    "v_pk_mul_f32 v[24:25], v[24:25], v[224:225]\n"
    "v_pk_mul_f32 v[26:27], v[26:27], v[226:227]\n"
    "v_pk_mul_f32 v[28:29], v[28:29], v[228:229]\n"
    "v_pk_mul_f32 v[30:31], v[30:31], v[230:231]\n"
    "v_exp_f32 v24, v24\n"
    "v_exp_f32 v25, v25\n"
    "v_exp_f32 v26, v26\n"
    "v_exp_f32 v27, v27\n"
    "v_exp_f32 v28, v28\n"
    "v_exp_f32 v29, v29\n"
    "v_exp_f32 v30, v30\n"
    "v_exp_f32 v31, v31\n"
    "v_pk_add_f32 v[24:25], v[24:25], 1.0 op_sel_hi:[1,0]\n"
    "v_pk_add_f32 v[26:27], v[26:27], 1.0 op_sel_hi:[1,0]\n"
    "v_pk_add_f32 v[28:29], v[28:29], 1.0 op_sel_hi:[1,0]\n"
    "v_pk_add_f32 v[30:31], v[30:31], 1.0 op_sel_hi:[1,0]\n"
    "v_rcp_f32 v24, v24\n"
    "v_rcp_f32 v25, v25\n"
    "v_rcp_f32 v26, v26\n"
    "v_rcp_f32 v27, v27\n"
    "v_rcp_f32 v28, v28\n"
    "v_rcp_f32 v29, v29\n"
    "v_rcp_f32 v30, v30\n"
    "v_rcp_f32 v31, v31\n"
    "v_pk_mul_f32 v[224:225], v[224:225], v[24:25]\n"
    "v_pk_mul_f32 v[226:227], v[226:227], v[26:27]\n"
    "v_pk_mul_f32 v[228:229], v[228:229], v[28:29]\n"
    "v_pk_mul_f32 v[230:231], v[230:231], v[30:31]\n"
    "v_cvt_pk_bf16_f32 v224, v224, v225\n"
    "v_cvt_pk_bf16_f32 v225, v226, v227\n"
    "v_cvt_pk_bf16_f32 v226, v228, v229\n"
    "v_cvt_pk_bf16_f32 v227, v230, v231\n"
    "v_readlane_b32 s91, v140, 0\n"
    "s_waitcnt vmcnt(0)\n"
    "s_add_u32 s33, s92, 1\n"
    "v_subrev_u32 v164, s33, v143\n"
    "v_cmp_lt_u32 vcc, 2, v164\n"
    "s_nop 0\n"
    "s_cmp_eq_u32 vcc_lo, -1\n"
    "s_cselect_b32 s27, -1, 0\n"
    "v_cmp_gt_u32 vcc, 16, v164\n"
    "s_nop 0\n"
    "s_cmp_eq_u32 vcc_lo, -1\n"
    "s_cselect_b32 s88, s67, 1073741824\n"
    "s_add_u32 s84, s91, s88\n"
    "v_readlane_b32 s91, v140, 1\n"
    "s_xor_b32 s85, s84, 2048\n"
    "buffer_load_dwordx4 a[224:227], v141, s[44:47], s84 offen sc1\n"
    "buffer_load_dwordx4 a[228:231], v141, s[44:47], s84 offen offset:1024 sc1\n"
    "buffer_load_dwordx4 a[232:235], v141, s[44:47], s85 offen sc1\n"
    "buffer_load_dwordx4 a[236:239], v141, s[44:47], s85 offen offset:1024 sc1\n"
    "s_add_u32 s84, s91, s88\n"
    "s_xor_b32 s85, s84, 2048\n"
    "buffer_load_dwordx4 a[240:243], v141, s[44:47], s84 offen sc1\n"
    "buffer_load_dwordx4 a[244:247], v141, s[44:47], s84 offen offset:1024 sc1\n"
    "buffer_load_dwordx4 a[248:251], v141, s[44:47], s85 offen sc1\n"
    ".Lk2_m3044:\n"
    "s_cbranch_execz .Lk2_m8172\n"
    "buffer_load_dwordx4 a[252:255], v141, s[44:47], s85 offen offset:1024 sc1\n"
    "buffer_store_dwordx4 v[132:135], v23, s[44:47], s29 offen sc1\n"
    "buffer_store_dwordx4 v[156:159], v23, s[44:47], s29 offen offset:1024 sc1\n"
    "v_pk_fma_f32 v[24:25], v[44:45], v[18:19], v[92:93] op_sel:[0,1,0] op_sel_hi:[1,1,1]\n"
    "v_pk_fma_f32 v[26:27], v[46:47], v[18:19], v[94:95] op_sel:[0,1,0] op_sel_hi:[1,1,1]\n"
    "v_pk_fma_f32 v[28:29], v[68:69], v[18:19], v[116:117] op_sel:[0,1,0] op_sel_hi:[1,1,1]\n"
    "v_pk_fma_f32 v[30:31], v[70:71], v[18:19], v[118:119] op_sel:[0,1,0] op_sel_hi:[1,1,1]\n"
    "v_pk_fma_f32 v[232:233], v[232:233], v[18:19], v[24:25] op_sel_hi:[1,0,1]\n"
    "v_pk_fma_f32 v[234:235], v[234:235], v[18:19], v[26:27] op_sel_hi:[1,0,1]\n"
    "v_pk_fma_f32 v[236:237], v[236:237], v[18:19], v[28:29] op_sel_hi:[1,0,1]\n"
    "v_pk_fma_f32 v[238:239], v[238:239], v[18:19], v[30:31] op_sel_hi:[1,0,1]\n"
    "v_pk_mul_f32 v[24:25], v[232:233], v[232:233]\n"
    "v_pk_mul_f32 v[26:27], v[234:235], v[234:235]\n"
    "v_pk_mul_f32 v[28:29], v[236:237], v[236:237]\n"
    "v_pk_mul_f32 v[30:31], v[238:239], v[238:239]\n"
    "v_pk_fma_f32 v[24:25], v[24:25], s[96:97], s[96:97] op_sel:[0,1,0] op_sel_hi:[1,1,0]\n"
    "v_pk_fma_f32 v[26:27], v[26:27], s[96:97], s[96:97] op_sel:[0,1,0] op_sel_hi:[1,1,0]\n"
    "v_pk_fma_f32 v[28:29], v[28:29], s[96:97], s[96:97] op_sel:[0,1,0] op_sel_hi:[1,1,0]\n"
    "v_pk_fma_f32 v[30:31], v[30:31], s[96:97], s[96:97] op_sel:[0,1,0] op_sel_hi:[1,1,0]\n"
    "v_pk_mul_f32 v[24:25], v[24:25], v[232:233]\n"
    "v_pk_mul_f32 v[26:27], v[26:27], v[234:235]\n"
    "v_pk_mul_f32 v[28:29], v[28:29], v[236:237]\n"
    "v_pk_mul_f32 v[30:31], v[30:31], v[238:239]\n"
    "v_exp_f32 v24, v24\n"
    "v_exp_f32 v25, v25\n"
    "v_exp_f32 v26, v26\n"
    "v_exp_f32 v27, v27\n"
    "v_exp_f32 v28, v28\n"
    "v_exp_f32 v29, v29\n"
    "v_exp_f32 v30, v30\n"
    "v_exp_f32 v31, v31\n"
    "v_pk_add_f32 v[24:25], v[24:25], 1.0 op_sel_hi:[1,0]\n"
    "v_pk_add_f32 v[26:27], v[26:27], 1.0 op_sel_hi:[1,0]\n"
    "v_pk_add_f32 v[28:29], v[28:29], 1.0 op_sel_hi:[1,0]\n"
    "v_pk_add_f32 v[30:31], v[30:31], 1.0 op_sel_hi:[1,0]\n"
    "v_rcp_f32 v24, v24\n"
    "v_rcp_f32 v25, v25\n"
    "v_rcp_f32 v26, v26\n"
    "v_rcp_f32 v27, v27\n"
    "v_rcp_f32 v28, v28\n"
    "v_rcp_f32 v29, v29\n"
    "v_rcp_f32 v30, v30\n"
    "v_rcp_f32 v31, v31\n"
    "v_pk_mul_f32 v[232:233], v[232:233], v[24:25]\n"
    "v_pk_mul_f32 v[234:235], v[234:235], v[26:27]\n"
    "v_pk_mul_f32 v[236:237], v[236:237], v[28:29]\n"
    "v_pk_mul_f32 v[238:239], v[238:239], v[30:31]\n"
    "v_cvt_pk_bf16_f32 v232, v232, v233\n"
    "v_cvt_pk_bf16_f32 v233, v234, v235\n"
    "v_cvt_pk_bf16_f32 v234, v236, v237\n"
    "v_cvt_pk_bf16_f32 v235, v238, v239\n"
    "buffer_store_dwordx4 v[180:183], v23, s[44:47], s29 offen offset:2048 sc1\n"
    "buffer_store_dwordx4 v[204:207], v23, s[44:47], s29 offen offset:3072 sc1\n"
    "v_pk_fma_f32 v[24:25], v[44:45], v[32:33], v[92:93] op_sel:[0,1,0] op_sel_hi:[1,1,1]\n"
    "v_pk_fma_f32 v[26:27], v[46:47], v[32:33], v[94:95] op_sel:[0,1,0] op_sel_hi:[1,1,1]\n"
    "v_pk_fma_f32 v[28:29], v[68:69], v[32:33], v[116:117] op_sel:[0,1,0] op_sel_hi:[1,1,1]\n"
    "v_pk_fma_f32 v[30:31], v[70:71], v[32:33], v[118:119] op_sel:[0,1,0] op_sel_hi:[1,1,1]\n"
    "v_pk_fma_f32 v[240:241], v[240:241], v[32:33], v[24:25] op_sel_hi:[1,0,1]\n"
    "v_pk_fma_f32 v[242:243], v[242:243], v[32:33], v[26:27] op_sel_hi:[1,0,1]\n"
    "v_pk_fma_f32 v[244:245], v[244:245], v[32:33], v[28:29] op_sel_hi:[1,0,1]\n"
    "v_pk_fma_f32 v[246:247], v[246:247], v[32:33], v[30:31] op_sel_hi:[1,0,1]\n"
    "v_pk_mul_f32 v[24:25], v[240:241], v[240:241]\n"
    "v_pk_mul_f32 v[26:27], v[242:243], v[242:243]\n"
    "v_pk_mul_f32 v[28:29], v[244:245], v[244:245]\n"
    "v_pk_mul_f32 v[30:31], v[246:247], v[246:247]\n"
    "v_pk_fma_f32 v[24:25], v[24:25], s[96:97], s[96:97] op_sel:[0,1,0] op_sel_hi:[1,1,0]\n"
    "v_pk_fma_f32 v[26:27], v[26:27], s[96:97], s[96:97] op_sel:[0,1,0] op_sel_hi:[1,1,0]\n"
    "v_pk_fma_f32 v[28:29], v[28:29], s[96:97], s[96:97] op_sel:[0,1,0] op_sel_hi:[1,1,0]\n"
    "v_pk_fma_f32 v[30:31], v[30:31], s[96:97], s[96:97] op_sel:[0,1,0] op_sel_hi:[1,1,0]\n"
    "v_pk_mul_f32 v[24:25], v[24:25], v[240:241]\n"
    "v_pk_mul_f32 v[26:27], v[26:27], v[242:243]\n"
    "v_pk_mul_f32 v[28:29], v[28:29], v[244:245]\n"
    "v_pk_mul_f32 v[30:31], v[30:31], v[246:247]\n"
    "v_exp_f32 v24, v24\n"
    "v_exp_f32 v25, v25\n"
    "v_exp_f32 v26, v26\n"
    "v_exp_f32 v27, v27\n"
    "v_exp_f32 v28, v28\n"
    "v_exp_f32 v29, v29\n"
    "v_exp_f32 v30, v30\n"
    "v_exp_f32 v31, v31\n"
    "v_pk_add_f32 v[24:25], v[24:25], 1.0 op_sel_hi:[1,0]\n"
    "v_pk_add_f32 v[26:27], v[26:27], 1.0 op_sel_hi:[1,0]\n"
    "v_pk_add_f32 v[28:29], v[28:29], 1.0 op_sel_hi:[1,0]\n"
    "v_pk_add_f32 v[30:31], v[30:31], 1.0 op_sel_hi:[1,0]\n"
    "v_rcp_f32 v24, v24\n"
    "v_rcp_f32 v25, v25\n"
    "v_rcp_f32 v26, v26\n"
    "v_rcp_f32 v27, v27\n"
    "v_rcp_f32 v28, v28\n"
    "v_rcp_f32 v29, v29\n"
    "v_rcp_f32 v30, v30\n"
    "v_rcp_f32 v31, v31\n"
    "v_pk_mul_f32 v[240:241], v[240:241], v[24:25]\n"
    "v_pk_mul_f32 v[242:243], v[242:243], v[26:27]\n"
    "v_pk_mul_f32 v[244:245], v[244:245], v[28:29]\n"
    "v_pk_mul_f32 v[246:247], v[246:247], v[30:31]\n"
    "v_cvt_pk_bf16_f32 v240, v240, v241\n"
    "v_cvt_pk_bf16_f32 v241, v242, v243\n"
    "v_cvt_pk_bf16_f32 v242, v244, v245\n"
    "v_cvt_pk_bf16_f32 v243, v246, v247\n"
    "buffer_store_dwordx4 v[224:227], v23, s[44:47], s30 offen sc1\n"
    "buffer_store_dwordx4 v[232:235], v23, s[44:47], s30 offen offset:1024 sc1\n"
    "v_pk_fma_f32 v[24:25], v[44:45], v[34:35], v[92:93] op_sel:[0,1,0] op_sel_hi:[1,1,1]\n"
    "v_pk_fma_f32 v[26:27], v[46:47], v[34:35], v[94:95] op_sel:[0,1,0] op_sel_hi:[1,1,1]\n"
    "v_pk_fma_f32 v[28:29], v[68:69], v[34:35], v[116:117] op_sel:[0,1,0] op_sel_hi:[1,1,1]\n"
    "v_pk_fma_f32 v[30:31], v[70:71], v[34:35], v[118:119] op_sel:[0,1,0] op_sel_hi:[1,1,1]\n"
    "v_pk_fma_f32 v[248:249], v[248:249], v[34:35], v[24:25] op_sel_hi:[1,0,1]\n"
    "v_pk_fma_f32 v[250:251], v[250:251], v[34:35], v[26:27] op_sel_hi:[1,0,1]\n"
    "v_pk_fma_f32 v[252:253], v[252:253], v[34:35], v[28:29] op_sel_hi:[1,0,1]\n"
    "v_pk_fma_f32 v[254:255], v[254:255], v[34:35], v[30:31] op_sel_hi:[1,0,1]\n"
    "v_pk_mul_f32 v[24:25], v[248:249], v[248:249]\n"
    "v_pk_mul_f32 v[26:27], v[250:251], v[250:251]\n"
    "v_pk_mul_f32 v[28:29], v[252:253], v[252:253]\n"
    "v_pk_mul_f32 v[30:31], v[254:255], v[254:255]\n"
    "v_pk_fma_f32 v[24:25], v[24:25], s[96:97], s[96:97] op_sel:[0,1,0] op_sel_hi:[1,1,0]\n"
    "v_pk_fma_f32 v[26:27], v[26:27], s[96:97], s[96:97] op_sel:[0,1,0] op_sel_hi:[1,1,0]\n"
    "v_pk_fma_f32 v[28:29], v[28:29], s[96:97], s[96:97] op_sel:[0,1,0] op_sel_hi:[1,1,0]\n"
    "v_pk_fma_f32 v[30:31], v[30:31], s[96:97], s[96:97] op_sel:[0,1,0] op_sel_hi:[1,1,0]\n"
    "v_pk_mul_f32 v[24:25], v[24:25], v[248:249]\n"
    "v_pk_mul_f32 v[26:27], v[26:27], v[250:251]\n"
    "v_pk_mul_f32 v[28:29], v[28:29], v[252:253]\n"
    "v_pk_mul_f32 v[30:31], v[30:31], v[254:255]\n"
    "v_exp_f32 v24, v24\n"
    "v_exp_f32 v25, v25\n"
    "v_exp_f32 v26, v26\n"
    "v_exp_f32 v27, v27\n"
    "v_exp_f32 v28, v28\n"
    "v_exp_f32 v29, v29\n"
    "v_exp_f32 v30, v30\n"
    "v_exp_f32 v31, v31\n"
    "v_pk_add_f32 v[24:25], v[24:25], 1.0 op_sel_hi:[1,0]\n"
    "v_pk_add_f32 v[26:27], v[26:27], 1.0 op_sel_hi:[1,0]\n"
    "v_pk_add_f32 v[28:29], v[28:29], 1.0 op_sel_hi:[1,0]\n"
    "v_pk_add_f32 v[30:31], v[30:31], 1.0 op_sel_hi:[1,0]\n"
    "v_rcp_f32 v24, v24\n"
    "v_rcp_f32 v25, v25\n"
    "v_rcp_f32 v26, v26\n"
    "v_rcp_f32 v27, v27\n"
    "v_rcp_f32 v28, v28\n"
    "v_rcp_f32 v29, v29\n"
    "v_rcp_f32 v30, v30\n"
    "v_rcp_f32 v31, v31\n"
    "v_pk_mul_f32 v[248:249], v[248:249], v[24:25]\n"
    "v_pk_mul_f32 v[250:251], v[250:251], v[26:27]\n"
    "v_pk_mul_f32 v[252:253], v[252:253], v[28:29]\n"
    "v_pk_mul_f32 v[254:255], v[254:255], v[30:31]\n"
    "v_cvt_pk_bf16_f32 v248, v248, v249\n"
    "v_cvt_pk_bf16_f32 v249, v250, v251\n"
    "v_cvt_pk_bf16_f32 v250, v252, v253\n"
    "v_cvt_pk_bf16_f32 v251, v254, v255\n"
    "buffer_store_dwordx4 v[240:243], v23, s[44:47], s30 offen offset:2048 sc1\n"
    "buffer_store_dwordx4 v[248:251], v23, s[44:47], s30 offen offset:3072 sc1\n"
    "s_cmp_eq_u32 s88, s67\n"
    "s_cbranch_scc0 .Lk2_aslow\n"
    "s_waitcnt vmcnt(8)\n"
    ".Lk2_aok:\n"
    "s_branch .Lk2_join\n"
    ".Lk2_qkv:\n"
    "v_accvgpr_write_b32 a222, v27\n"
    "s_lshr_b32 s22, s7, 1\n"
    "v_lshrrev_b32 v195, 2, v194\n"
    "s_lshl_b32 s0, s22, 11\n"
    "v_lshl_add_u32 v200, v194, 7, s0\n"
    "v_lshrrev_b32 v201, 1, v194\n"
    "v_xor_b32 v201, v193, v201\n"
    "v_lshl_add_u32 v8, v201, 4, v200\n"
    "v_xor_b32 v9, 0x4000, v8\n"
    "v_xor_b32 v10, 64, v8\n"
    "v_xor_b32 v11, 0x4040, v8\n"
    "v_and_b32 v208, 1, v193\n"
    "v_and_b32 v209, 1, v196\n"
    "v_lshrrev_b32 v210, 1, v196\n"
    "v_mul_u32_u24 v200, 0xc00, v193\n"
    "v_add_u32 v200, 0x12000, v200\n"
    "v_lshl_add_u32 v200, v195, 7, v200\n"
    "s_lshl_b32 s0, s23, 2\n"
    "v_and_b32 v201, 2, v195\n"
    "v_xor_b32 v201, v210, v201\n"
    "v_lshlrev_b32 v202, 2, v208\n"
    "v_xor_b32 v202, v201, v202\n"
    "v_xor_b32 v202, s0, v202\n"
    "v_lshl_add_u32 v200, v202, 4, v200\n"
    "v_lshl_add_u32 v14, v209, 3, v200\n"
    "v_xor_b32 v15, 32, v14\n"
    "v_lshlrev_b32 v200, 5, v209\n"
    "v_xor_b32 v12, v14, v200\n"
    "v_xor_b32 v13, 32, v12\n"
    "v_mov_b32 v200, 0x3f80\n"
    "v_lshlrev_b32 v202, 4, v209\n"
    "v_lshlrev_b32 v211, v202, v200\n"
    "v_lshrrev_b32 v212, 1, v198\n"
    "v_cmp_eq_u32 vcc, 0, v212\n"
    "s_nop 1\n"
    "v_cndmask_b32 v202, 0, v211, vcc\n"
    "v_accvgpr_write_b32 a132, v202\n"
    "v_cmp_eq_u32 vcc, 1, v212\n"
    "s_nop 1\n"
    "v_cndmask_b32 v202, 0, v211, vcc\n"
    "v_accvgpr_write_b32 a133, v202\n"
    "v_cmp_eq_u32 vcc, 2, v212\n"
    "s_nop 1\n"
    "v_cndmask_b32 v202, 0, v211, vcc\n"
    "v_accvgpr_write_b32 a134, v202\n"
    "v_cmp_eq_u32 vcc, 3, v212\n"
    "s_nop 1\n"
    "v_cndmask_b32 v202, 0, v211, vcc\n"
    "v_accvgpr_write_b32 a135, v202\n"
    "s_and_b32 s17, s6, 1\n"
    "s_and_b32 s0, s6, 2\n"
    "s_lshl2_add_u32 s18, s0, s57\n"
    "s_and_b32 s0, s6, 4\n"
    "s_lshr_b32 s1, s6, 6\n"
    "s_add_u32 s19, s0, s1\n"
    "s_lshr_b32 s1, s6, 3\n"
    "s_and_b32 s0, s6, 2\n"
    "s_lshl_b32 s0, s0, 6\n"
    "s_lshl2_add_u32 s0, s1, s0\n"
    "s_add_u32 s99, s0, s7\n"
    "s_mul_i32 s0, s99, 0x3800\n"
    "s_add_u32 s0, 0x400, s0\n"
    "s_and_b32 s1, s6, 4\n"
    "s_lshl_b32 s1, s1, 8\n"
    "s_add_u32 s1, s0, s1\n"
    "v_lshrrev_b32 v202, 5, v192\n"
    "v_lshl_add_u32 v202, v202, 11, s1\n"
    "v_and_b32 v200, 31, v192\n"
    "v_lshl_add_u32 v28, v200, 5, v202\n"
    "s_lshl2_add_u32 s35, s6, s7\n"
    "s_and_b32 s45, s45, 0xffff\n"
    "s_mov_b32 s46, 41943040\n"
    "s_mov_b32 s47, 0x20000\n"
    "s_mul_i32 s0, s17, 3670016\n"
    "s_add_u32 s48, s80, s0\n"
    "s_addc_u32 s49, s81, 0\n"
    "s_sub_u32 s48, s48, 1024\n"
    "s_subb_u32 s49, s49, 0\n"
    "s_and_b32 s49, s49, 0xffff\n"
    "s_mov_b32 s50, 3671040\n"
    "s_mov_b32 s51, 0x20000\n"
    "s_sub_u32 s58, s82, 1024\n"
    "s_subb_u32 s59, s83, 0\n"
    "s_and_b32 s59, s59, 0xffff\n"
    "s_cmp_eq_u32 s23, 0\n"
    "s_cselect_b64 s[96:97], s[80:81], s[82:83]\n"
    "s_cselect_b64 s[28:29], s[70:71], s[68:69]\n"
    "s_cselect_b64 s[0:1], s[72:73], s[74:75]\n"
    "s_cmp_lt_u32 s7, 2\n"
    "s_cselect_b64 s[96:97], s[96:97], s[44:45]\n"
    "s_cselect_b64 s[28:29], s[28:29], s[0:1]\n"
    "s_lshl_b32 s1, s57, 1\n"
    "s_mul_i32 s1, s1, 0x120000\n"
    "s_lshl_b32 s0, s22, 12\n"
    "s_add_u32 s60, s1, s0\n"
    "s_lshl_b32 s0, s57, 1\n"
    "s_mul_i32 s0, s0, 0x120000\n"
    "s_add_u32 s0, s0, 0x120000\n"
    "s_lshl_b32 s1, s22, 12\n"
    "s_add_u32 s61, s0, s1\n"
    "s_lshr_b32 s1, s8, 3\n"
    "s_mul_i32 s1, s1, 56\n"
    "s_lshl1_add_u32 s1, s8, s1\n"
    "s_add_u32 s1, s1, s23\n"
    "s_add_u32 s1, s1, 56\n"
    "s_lshl_b32 s62, s1, 13\n"
    "s_lshl3_add_u32 s65, s8, s57\n"
    "s_lshl_b32 s1, s65, 16\n"
    "s_add_u32 s63, s1, 0x1400000\n"
    "s_lshl_b32 s1, s65, 7\n"
    "s_add_u32 s65, s1, 0x2003f80\n"
    "s_lshl2_add_u32 s1, s7, s65\n"
    "s_add_u32 s64, s1, 0x80\n"
    "s_lshl_b32 s1, s57, 12\n"
    "s_lshl_b32 s0, s8, 7\n"
    "s_add_u32 s0, s1, s0\n"
    "s_lshl2_add_u32 s0, s7, s0\n"
    "s_add_u32 s66, s0, 0x20a0800\n"
    "s_xor_b32 s83, s66, 0x100\n"
    "s_mul_i32 s0, s18, 0x120000\n"
    "s_lshl_b32 s1, s23, 11\n"
    "s_add_u32 s67, s0, s1\n"
    "s_lshl_b32 s1, s8, 14\n"
    "s_lshl_b32 s0, s7, 9\n"
    "s_add_u32 s0, s1, s0\n"
    "s_add_u32 s77, s0, 0x2020000\n"
    "s_lshl_b32 s0, s57, 11\n"
    "s_add_u32 s76, s77, s0\n"
    "s_lshl_b32 s0, s57, 12\n"
    "s_lshl_b32 s1, s8, 8\n"
    "s_add_u32 s1, s0, s1\n"
    "s_lshl_b32 s0, s7, 6\n"
    "s_add_u32 s0, s1, s0\n"
    "s_add_u32 s78, s0, 0x2010000\n"
    "s_lshl_b32 s0, s57, 12\n"
    "s_lshl_b32 s1, s7, 10\n"
    "s_add_u32 s1, s0, s1\n"
    "s_add_u32 s79, s1, 0x2010000\n"
    "s_lshl_b32 s1, s35, 12\n"
    "s_add_u32 s81, s1, 0x1c00000\n"
    "s_xor_b32 s80, s81, 0x4000\n"
    "s_lshr_b32 s1, s18, 1\n"
    "s_lshl_b32 s1, s1, 12\n"
    "s_lshl_b32 s0, s17, 10\n"
    "s_add_u32 s0, s1, s0\n"
    "s_add_u32 s82, s0, 0x20a0000\n"
    "s_cmp_eq_u32 s7, 0\n"
    "s_cselect_b32 s0, 0x380000, 0\n"
    "s_mul_i32 s0, s17, s0\n"
    "s_mul_i32 s1, s57, 0x240000\n"
    "s_cmp_eq_u32 s7, 2\n"
    "s_cselect_b32 s1, s1, s67\n"
    "s_cmp_lt_u32 s7, 2\n"
    "s_cselect_b32 s93, s0, s1\n"
    "s_mov_b32 s1, 0x37fffc\n"
    "s_cmp_eq_u32 s7, 0\n"
    "s_cselect_b32 s1, s1, 0x1ffffc\n"
    "s_mov_b32 s0, 0x23fffc\n"
    "s_cmp_eq_u32 s7, 2\n"
    "s_cselect_b32 s0, s0, 0x11f7fc\n"
    "s_cmp_lt_u32 s7, 2\n"
    "s_cselect_b32 s94, s1, s0\n"
    "v_lshlrev_b32 v202, 19, v197\n"
    "v_min_u32 v202, s94, v202\n"
    "v_add_u32 v29, s93, v202\n"
    "s_add_u32 s0, s63, 0xfffc\n"
    "v_and_b32 v202, 1, v197\n"
    "v_mul_u32_u24 v202, 0x1fffc, v202\n"
    "v_sub_u32 v213, s0, v202\n"
    "s_cmp_eq_u32 s7, 3\n"
    "s_cselect_b32 s0, 6, 8\n"
    "v_cmp_le_u32 vcc, s0, v197\n"
    "s_nop 1\n"
    "v_cndmask_b32 v29, v29, v213, vcc\n"
    "s_cmp_eq_u32 s7, 3\n"
    "s_cselect_b32 s0, 4, 8\n"
    "v_cmp_eq_u32 vcc, s0, v197\n"
    "v_mov_b32 v202, s66\n"
    "s_nop 0\n"
    "v_cndmask_b32 v29, v29, v202, vcc\n"
    "s_cmp_eq_u32 s7, 3\n"
    "s_cselect_b32 s0, 5, 8\n"
    "v_cmp_eq_u32 vcc, s0, v197\n"
    "v_mov_b32 v202, s64\n"
    "s_nop 0\n"
    "v_cndmask_b32 v29, v29, v202, vcc\n"
    "v_and_b32 v202, 1, v197\n"
    "v_lshlrev_b32 v202, 7, v202\n"
    "v_sub_u32 v213, s78, v202\n"
    "s_cmp_eq_u32 s7, 2\n"
    "s_cselect_b32 s0, 6, 8\n"
    "v_cmp_le_u32 vcc, s0, v197\n"
    "s_nop 1\n"
    "v_cndmask_b32 v29, v29, v213, vcc\n"
    "s_lshl_b32 s0, s18, 18\n"
    "s_cmp_lt_u32 s7, 2\n"
    "s_cselect_b32 s0, s0, 0\n"
    "s_mov_b32 s1, 0x1bfc\n"
    "s_cmp_eq_u32 s7, 2\n"
    "s_cselect_b32 s1, s1, 0x7fc\n"
    "s_cmp_lt_u32 s7, 2\n"
    "s_cselect_b32 s1, 0x3fffc, s1\n"
    "v_lshrrev_b32 v202, 5, v192\n"
    "v_mul_u32_u24 v202, s1, v202\n"
    "v_add_u32 v31, s0, v202\n"
    "s_mov_b32 s85, 0x10000\n"
    "s_mov_b32 s87, 0\n"
    "global_load_dword a220, v29, s[96:97] sc1\n"
    "global_load_dword a220, v31, s[28:29] sc1\n"
    "s_getpc_b64 s[0:1]\n"
    ".Lk2_qi0:\n"
    "s_add_u32 s0, s0, .Lk2_qi1-.Lk2_qi0\n"
    "s_addc_u32 s1, s1, 0\n"
    "s_mov_b64 exec, 0\n"
    "s_cmp_eq_u32 s7, 0\n"
    "s_cbranch_scc1 .Lk2_hq0\n"
    "s_cmp_eq_u32 s7, 1\n"
    "s_cbranch_scc1 .Lk2_hq1\n"
    "s_cmp_eq_u32 s7, 2\n"
    "s_cbranch_scc1 .Lk2_hq2\n"
    "s_branch .Lk2_hq3\n"
    ".Lk2_qi1:\n"
    "s_mov_b64 exec, -1\n"
    "s_add_u32 m0, s52, 0\n"
    "s_nop 0\n"
    "buffer_load_dwordx4 v16, s[36:39], s84 offen lds\n"
    "v_lshl_add_u32 v212, v208, 2, v195\n"
    "v_lshrrev_b32 v213, 1, v193\n"
    "s_lshl_b32 s0, s23, 11\n"
    "v_lshl_add_u32 v202, v213, 13, s0\n"
    "v_lshl_add_u32 v202, v212, 7, v202\n"
    "v_and_b32 v200, 6, v212\n"
    "v_xor_b32 v200, v210, v200\n"
    "v_lshl_add_u32 v202, v200, 4, v202\n"
    "v_lshl_add_u32 v214, v209, 3, v202\n"
    "v_accvgpr_write_b32 a160, v214\n"
    "v_xor_b32 v202, 32, v214\n"
    "v_accvgpr_write_b32 a161, v202\n"
    "v_xor_b32 v202, 64, v214\n"
    "v_accvgpr_write_b32 a162, v202\n"
    "v_xor_b32 v202, 0x60, v214\n"
    "v_accvgpr_write_b32 a163, v202\n"
    "s_lshl_b32 s0, s7, 14\n"
    "s_lshl_b32 s1, s19, 8\n"
    "s_add_u32 s1, s0, s1\n"
    "v_lshl_add_u32 v202, v197, 11, s1\n"
    "v_lshlrev_b32 v200, 1, v193\n"
    "v_xor_b32 v200, v198, v200\n"
    "v_lshl_add_u32 v214, v200, 4, v202\n"
    "buffer_load_dwordx4 v17, s[36:39], s84 offen offset:1024 lds\n"
    "s_lshl_b32 s1, s17, 7\n"
    "s_add_u32 s1, 0x400, s1\n"
    "v_add_u32 v202, s1, v214\n"
    "v_accvgpr_write_b32 a164, v202\n"
    "s_xor_b32 s1, s17, 1\n"
    "s_lshl_b32 s1, s1, 7\n"
    "v_add_u32 v202, s1, v214\n"
    "v_accvgpr_write_b32 a165, v202\n"
    "s_lshl_b32 s1, s22, 12\n"
    "s_lshl_b32 s0, s23, 13\n"
    "s_add_u32 s0, s1, s0\n"
    "v_add_u32 v202, s0, v25\n"
    "v_accvgpr_write_b32 a166, v202\n"
    "s_lshr_b32 s0, s19, 1\n"
    "s_add_u32 s0, s18, s0\n"
    "s_and_b32 s0, s0, 1\n"
    "s_lshl_b32 s0, s0, 1\n"
    "v_xor_b32 v214, s0, v192\n"
    "buffer_load_dwordx4 v18, s[36:39], s84 offen offset:2048 lds\n"
    "v_cmp_gt_u32 vcc, 24, v192\n"
    "v_bfe_u32 v202, v214, 1, 2\n"
    "v_mul_u32_u24 v202, 7, v202\n"
    "v_lshrrev_b32 v200, 3, v214\n"
    "v_add_u32 v202, v202, v200\n"
    "v_and_b32 v200, 1, v214\n"
    "v_lshl_add_u32 v202, v200, 2, v202\n"
    "v_mul_u32_u24 v200, 7, v214\n"
    "v_add_u32 v200, 0xffffff5b, v200\n"
    "v_cndmask_b32 v215, v200, v202, vcc\n"
    "s_mul_i32 s0, s17, 9\n"
    "s_add_u32 s0, 0xffffffe4, s0\n"
    "v_cmp_gt_u32 vcc, 32, v192\n"
    "s_nop 1\n"
    "v_cndmask_b32 v200, v192, v214, vcc\n"
    "v_add_u32 v214, s0, v200\n"
    "v_cmp_gt_u32 vcc, 28, v192\n"
    "s_nop 1\n"
    "v_cndmask_b32 v200, v214, v215, vcc\n"
    "v_lshlrev_b32 v200, 17, v200\n"
    "v_accvgpr_write_b32 a198, v200\n"
    "buffer_load_dwordx4 v19, s[36:39], s84 offen offset:3072 lds\n"
    "v_cmp_gt_u32 vcc, 28, v192\n"
    "s_mul_i32 s0, s17, 36\n"
    "v_add_u32 v200, s0, v215\n"
    "v_lshrrev_b32 v202, 3, v214\n"
    "v_mul_u32_u24 v202, 28, v202\n"
    "v_add3_u32 v202, v202, v214, 28\n"
    "v_cndmask_b32 v202, v202, v200, vcc\n"
    "v_lshlrev_b32 v202, 14, v202\n"
    "v_accvgpr_write_b32 a167, v202\n"
    "s_lshl_b32 s0, s23, 2\n"
    "s_lshl3_add_u32 s0, s18, s0\n"
    "s_add_u32 s0, s0, s22\n"
    "s_lshl_b32 s0, s0, 15\n"
    "s_lshl_b32 s1, s19, 8\n"
    "s_add_u32 s93, s0, s1\n"
    "s_lshl_b32 s1, s17, 7\n"
    "s_add_u32 s1, s93, s1\n"
    "v_lshl_add_u32 v202, v197, 11, s1\n"
    "v_xor_b32 v200, v198, v193\n"
    "v_lshl_add_u32 v202, v200, 4, v202\n"
    "v_accvgpr_write_b32 a168, v202\n"
    "s_add_u32 s84, s84, 128\n"
    "s_add_u32 m0, s52, 4096\n"
    "s_nop 0\n"
    "buffer_load_dwordx4 v16, s[36:39], s85 offen lds\n"
    "s_lshl_b32 s1, s19, 8\n"
    "s_lshl_b32 s0, s17, 7\n"
    "s_add_u32 s0, s1, s0\n"
    "v_and_b32 v202, 31, v192\n"
    "v_lshl_add_u32 v202, v202, 2, s0\n"
    "v_accvgpr_write_b32 a169, v202\n"
    "s_lshl_b32 s0, s17, 7\n"
    "s_add_u32 s0, s93, s0\n"
    "v_lshl_add_u32 v202, v197, 11, s0\n"
    "v_lshl_add_u32 v202, v198, 4, v202\n"
    "v_accvgpr_write_b32 a170, v202\n"
    "s_lshl_b32 s0, s22, 7\n"
    "s_add_u32 s0, 0x20000, s0\n"
    "v_lshl_add_u32 v214, v194, 3, s0\n"
    "v_accvgpr_write_b32 a171, v214\n"
    "s_lshl_b32 s0, s23, 7\n"
    "s_add_u32 s0, 0x20800, s0\n"
    "v_lshl_add_u32 v214, v193, 4, s0\n"
    "buffer_load_dwordx4 v17, s[36:39], s85 offen offset:1024 lds\n"
    "v_accvgpr_write_b32 a173, v214\n"
    "v_add_u32 v202, 0x200, v214\n"
    "v_lshl_add_u32 v214, v208, 6, v202\n"
    "v_accvgpr_write_b32 a174, v214\n"
    "v_xor_b32 v202, 64, v214\n"
    "v_accvgpr_write_b32 a175, v202\n"
    "s_lshl_b32 s0, s99, 12\n"
    "s_add_u32 s0, 0x400, s0\n"
    "s_and_b32 s1, s6, 4\n"
    "s_lshl_b32 s1, s1, 8\n"
    "s_add_u32 s1, s0, s1\n"
    "s_lshl_b32 s0, s17, 20\n"
    "s_add_u32 s0, s1, s0\n"
    "v_lshrrev_b32 v202, 5, v192\n"
    "v_lshl_add_u32 v202, v202, 11, s0\n"
    "v_and_b32 v200, 31, v192\n"
    "v_lshl_add_u32 v202, v200, 5, v202\n"
    "v_accvgpr_write_b32 a178, v202\n"
    "v_lshlrev_b32 v202, 6, v198\n"
    "v_lshl_add_u32 v202, v208, 5, v202\n"
    "v_lshl_add_u32 v202, v213, 4, v202\n"
    "v_and_b32 v200, 1, v197\n"
    "v_lshl_add_u32 v202, v200, 3, v202\n"
    "v_accvgpr_write_b32 a192, v202\n"
    "buffer_load_dwordx4 v18, s[36:39], s85 offen offset:2048 lds\n"
    "v_cmp_eq_u32 vcc, 0, v194\n"
    "v_lshlrev_b32 v202, 4, v193\n"
    "v_mov_b32 v200, 0x40000000\n"
    "v_cndmask_b32 v200, v200, v202, vcc\n"
    "v_accvgpr_write_b32 a193, v200\n"
    "s_lshl_b32 s0, s23, 14\n"
    "v_add_u32 v214, s0, v25\n"
    "v_accvgpr_write_b32 a181, v214\n"
    "s_lshl_b32 s0, s22, 10\n"
    "v_add_u32 v214, s0, v214\n"
    "v_accvgpr_write_b32 a179, v214\n"
    "v_lshlrev_b32 v200, 7, v213\n"
    "v_xor_b32 v200, v214, v200\n"
    "v_add_u32 v200, 0x8000, v200\n"
    "v_accvgpr_write_b32 a180, v200\n"
    "buffer_load_dwordx4 v19, s[36:39], s85 offen offset:3072 lds\n"
    "s_lshl_b32 s0, s23, 14\n"
    "s_add_u32 s0, 0x8000, s0\n"
    "v_lshl_add_u32 v200, v196, 8, s0\n"
    "v_lshrrev_b32 v202, 2, v192\n"
    "v_lshlrev_b32 v201, 3, v210\n"
    "v_xor_b32 v201, v202, v201\n"
    "v_lshl_add_u32 v200, v201, 4, v200\n"
    "v_lshl_add_u32 v214, v209, 3, v200\n"
    "v_accvgpr_write_b32 a182, v214\n"
    "v_xor_b32 v200, 8, v214\n"
    "v_accvgpr_write_b32 a183, v200\n"
    "s_lshl4_add_u32 s0, s22, 12\n"
    "v_add_u32 v200, s0, v194\n"
    "v_lshlrev_b32 v201, 2, v193\n"
    "v_sub_u32 v214, v200, v201\n"
    "v_mov_b32 v215, 0xf149f2ca\n"
    "v_cmp_gt_u32 vcc, 12, v214\n"
    "s_nop 1\n"
    "v_cndmask_b32 v201, 0, v215, vcc\n"
    "v_accvgpr_write_b32 a184, v201\n"
    "v_cmp_gt_u32 vcc, 13, v214\n"
    "s_nop 1\n"
    "v_cndmask_b32 v201, 0, v215, vcc\n"
    "v_accvgpr_write_b32 a185, v201\n"
    "s_add_u32 s85, s85, 128\n"
    "s_add_u32 m0, s53, 12288\n"
    "s_nop 0\n"
    "buffer_load_dwordx4 v20, s[40:43], s86 offen lds\n"
    "v_cmp_gt_u32 vcc, 14, v214\n"
    "s_nop 1\n"
    "v_cndmask_b32 v201, 0, v215, vcc\n"
    "v_accvgpr_write_b32 a186, v201\n"
    "v_cmp_gt_u32 vcc, 15, v214\n"
    "s_nop 1\n"
    "v_cndmask_b32 v201, 0, v215, vcc\n"
    "v_accvgpr_write_b32 a187, v201\n"
    "v_cmp_gt_u32 vcc, 28, v214\n"
    "s_nop 1\n"
    "v_cndmask_b32 v201, 0, v215, vcc\n"
    "v_accvgpr_write_b32 a188, v201\n"
    "v_cmp_gt_u32 vcc, 29, v214\n"
    "s_nop 1\n"
    "v_cndmask_b32 v201, 0, v215, vcc\n"
    "v_accvgpr_write_b32 a189, v201\n"
    "v_cmp_gt_u32 vcc, 30, v214\n"
    "s_nop 1\n"
    "v_cndmask_b32 v201, 0, v215, vcc\n"
    "v_accvgpr_write_b32 a190, v201\n"
    "v_cmp_gt_u32 vcc, 31, v214\n"
    "s_nop 1\n"
    "v_cndmask_b32 v201, 0, v215, vcc\n"
    "v_accvgpr_write_b32 a191, v201\n"
    "buffer_load_dwordx4 v21, s[40:43], s86 offen offset:1024 lds\n"
    "s_lshl_b32 s0, s7, 12\n"
    "s_add_u32 s0, 0x21800, s0\n"
    "v_lshl_add_u32 v201, v194, 7, s0\n"
    "v_lshrrev_b32 v200, 1, v194\n"
    "v_xor_b32 v200, v213, v200\n"
    "v_lshl_add_u32 v201, v200, 4, v201\n"
    "v_lshl_add_u32 v201, v208, 3, v201\n"
    "v_accvgpr_write_b32 a194, v201\n"
    "s_lshl_b32 s0, s7, 8\n"
    "s_add_u32 s0, 0x21400, s0\n"
    "v_lshl_add_u32 v201, v193, 3, s0\n"
    "v_accvgpr_write_b32 a195, v201\n"
    "s_xor_b32 s0, s7, 1\n"
    "s_lshl_b32 s0, s0, 13\n"
    "s_add_u32 s25, s0, 0x20000\n"
    "v_mul_u32_u24 v201, 0x90, v194\n"
    "v_add_u32 v201, s25, v201\n"
    "v_lshl_add_u32 v201, v193, 3, v201\n"
    "v_accvgpr_write_b32 a196, v201\n"
    "buffer_load_dwordx4 v22, s[40:43], s86 offen offset:2048 lds\n"
    "v_mul_u32_u24 v201, 0x90, v197\n"
    "v_add_u32 v201, s25, v201\n"
    "v_lshl_add_u32 v201, v198, 4, v201\n"
    "v_accvgpr_write_b32 a197, v201\n"
    "s_mov_b32 s98, 0\n"
    "s_mov_b32 s88, 0\n"
    "s_lshl_b32 s89, s55, 3\n"
    "s_lshr_b32 s27, s55, 2\n"
    "s_mov_b32 s90, 1073741824\n"
    "s_mov_b32 s91, 512\n"
    "s_mov_b32 s34, 1073741824\n"
    "v_accvgpr_write_b32 a216, -1\n"
    "v_accvgpr_write_b32 a217, -1\n"
    "v_accvgpr_write_b32 a218, -1\n"
    "v_accvgpr_write_b32 a219, -1\n"
    "v_mov_b32 v224, 0\n"
    "v_mov_b32 v225, 0\n"
    "v_mov_b32 v226, 0\n"
    "v_mov_b32 v227, 0\n"
    "s_add_u32 s86, s86, s55\n"
    "s_add_u32 m0, s53, 24576\n"
    "s_nop 0\n"
    "buffer_load_dwordx4 v20, s[40:43], s86 offen lds\n"
    "buffer_load_dwordx4 v21, s[40:43], s86 offen offset:1024 lds\n"
    "buffer_load_dwordx4 v22, s[40:43], s86 offen offset:2048 lds\n"
    "s_add_u32 s86, s86, s55\n"
    "s_mov_b32 s94, 0x2000000\n"
    "s_mov_b64 exec, 1\n"
    "buffer_load_dword v27, off, s[44:47], s94 sc1\n"
    "s_mov_b64 exec, -1\n"
    "s_waitcnt vmcnt(17)\n"
    "v_mfma_f32_16x16x32_bf16 a[128:131], a[132:135], a[136:139], 0\n"
    "v_mfma_f32_16x16x32_bf16 a[128:131], a[132:135], a[140:143], a[128:131]\n"
    "v_mfma_f32_16x16x32_bf16 a[128:131], a[132:135], a[144:147], a[128:131]\n"
    "v_mfma_f32_16x16x32_bf16 a[128:131], a[132:135], a[148:151], a[128:131]\n"
    "s_waitcnt vmcnt(4)\n"
    "s_barrier\n"
    "ds_read_b128 a[64:67], v8 offset:0\n"
    "ds_read_b128 a[68:71], v10 offset:0\n"
    "ds_read_b128 a[72:75], v8 offset:4096\n"
    "ds_read_b128 a[76:79], v10 offset:4096\n"
    "ds_read_b64_tr_b16 a[0:1], v14 offset:0\n"
    "ds_read_b64_tr_b16 a[2:3], v14 offset:512\n"
    "ds_read_b64_tr_b16 a[4:5], v14 offset:12288\n"
    "ds_read_b64_tr_b16 a[6:7], v14 offset:12800\n"
    "ds_read_b64_tr_b16 a[8:9], v15 offset:0\n"
    "ds_read_b64_tr_b16 a[10:11], v15 offset:512\n"
    "ds_read_b64_tr_b16 a[12:13], v15 offset:12288\n"
    "ds_read_b64_tr_b16 a[14:15], v15 offset:12800\n"
    "ds_read_b64_tr_b16 a[16:17], v14 offset:1024\n"
    "ds_read_b64_tr_b16 a[18:19], v14 offset:1536\n"
    "ds_read_b64_tr_b16 a[20:21], v14 offset:13312\n"
    "ds_read_b64_tr_b16 a[22:23], v14 offset:13824\n"
    "ds_read_b64_tr_b16 a[24:25], v15 offset:1024\n"
    "ds_read_b64_tr_b16 a[26:27], v15 offset:1536\n"
    "ds_read_b64_tr_b16 a[28:29], v15 offset:13312\n"
    "ds_read_b64_tr_b16 a[30:31], v15 offset:13824\n"
    "ds_read_b64_tr_b16 a[32:33], v12 offset:2048\n"
    "ds_read_b64_tr_b16 a[34:35], v12 offset:2560\n"
    "ds_read_b64_tr_b16 a[36:37], v12 offset:14336\n"
    "ds_read_b64_tr_b16 a[38:39], v12 offset:14848\n"
    "s_waitcnt lgkmcnt(6)\n"
    "v_mfma_f32_16x16x32_bf16 v[32:35], a[0:3], a[64:67], 0\n"
    "s_add_u32 m0, s53, 36864\n"
    "ds_read_b64_tr_b16 a[40:41], v13 offset:2048\n"
    "v_mfma_f32_16x16x32_bf16 v[32:35], a[4:7], a[68:71], v[32:35]\n"
    "buffer_load_dwordx4 v20, s[40:43], s86 offen lds\n"
    "v_mfma_f32_16x16x32_bf16 v[56:59], a[4:7], a[76:79], 0\n"
    "ds_read_b64_tr_b16 a[42:43], v13 offset:2560\n"
    "v_cmp_eq_u32 vcc, v193, v195\n"
    "v_mfma_f32_16x16x32_bf16 v[56:59], a[0:3], a[72:75], v[56:59]\n"
    "buffer_load_dwordx4 v21, s[40:43], s86 offen offset:1024 lds\n"
    "v_mfma_f32_16x16x32_bf16 v[60:63], a[8:11], a[72:75], 0\n"
    "ds_read_b64_tr_b16 a[44:45], v13 offset:14336\n"
    "v_mfma_f32_16x16x32_bf16 v[60:63], a[12:15], a[76:79], v[60:63]\n"
    "ds_read_b64_tr_b16 a[46:47], v13 offset:14848\n"
    "v_cndmask_b32 v214, 0, v211, vcc\n"
    "v_mfma_f32_16x16x32_bf16 v[36:39], a[12:15], a[68:71], 0\n"
    "buffer_load_dwordx4 v22, s[40:43], s86 offen offset:2048 lds\n"
    "v_mfma_f32_16x16x32_bf16 v[36:39], a[8:11], a[64:67], v[36:39]\n"
    "s_add_u32 s86, s86, s55\n"
    "s_add_u32 m0, s52, 36864\n"
    "v_mfma_f32_16x16x32_bf16 v[40:43], a[16:19], a[64:67], 0\n"
    "buffer_load_dwordx4 v16, s[36:39], s84 offen lds\n"
    "v_mfma_f32_16x16x32_bf16 v[40:43], a[20:23], a[68:71], v[40:43]\n"
    "ds_read_b128 a[80:83], v8 offset:8192\n"
    "v_mfma_f32_16x16x32_bf16 v[64:67], a[20:23], a[76:79], 0\n"
    "ds_read_b128 a[84:87], v10 offset:8192\n"
    "v_cmp_gt_u32 vcc, 2, v196\n"
    "v_mfma_f32_16x16x32_bf16 v[64:67], a[16:19], a[72:75], v[64:67]\n"
    "buffer_load_dwordx4 v17, s[36:39], s84 offen offset:1024 lds\n"
    ".Lk2_m3744:\n"
    ".Lk2_hq0:\n"
    "s_cbranch_execz .Lk2_m6104\n"
    "v_mfma_f32_16x16x32_bf16 v[68:71], a[24:27], a[72:75], 0\n"
    "ds_read_b128 a[88:91], v8 offset:12288\n"
    "s_waitcnt lgkmcnt(6)\n"
    "v_mfma_f32_16x16x32_bf16 v[68:71], a[28:31], a[76:79], v[68:71]\n"
    "ds_read_b128 a[92:95], v10 offset:12288\n"
    "v_mfma_f32_16x16x32_bf16 v[44:47], a[28:31], a[68:71], 0\n"
    "buffer_load_dwordx4 v18, s[36:39], s84 offen offset:2048 lds\n"
    "v_mfma_f32_16x16x32_bf16 v[44:47], a[24:27], a[64:67], v[44:47]\n"
    "ds_read_b128 a[96:99], v9 offset:0\n"
    "v_cndmask_b32 v215, 0, v214, vcc\n"
    "v_mfma_f32_16x16x32_bf16 v[48:51], a[32:35], a[64:67], 0\n"
    "buffer_load_dwordx4 v19, s[36:39], s84 offen offset:3072 lds\n"
    "v_mfma_f32_16x16x32_bf16 v[48:51], a[36:39], a[68:71], v[48:51]\n"
    "ds_read_b128 a[100:103], v11 offset:0\n"
    "v_mfma_f32_16x16x32_bf16 v[72:75], a[36:39], a[76:79], 0\n"
    "s_add_u32 s84, s84, 128\n"
    "s_add_u32 m0, s52, 40960\n"
    "v_xor_b32 v214, v214, v215\n"
    "v_mfma_f32_16x16x32_bf16 v[72:75], a[32:35], a[72:75], v[72:75]\n"
    "buffer_load_dwordx4 v16, s[36:39], s85 offen lds\n"
    "s_waitcnt lgkmcnt(6)\n"
    "v_mfma_f32_16x16x32_bf16 v[76:79], a[40:43], a[72:75], 0\n"
    "ds_read_b128 a[104:107], v9 offset:4096\n"
    "v_mfma_f32_16x16x32_bf16 v[76:79], a[44:47], a[76:79], v[76:79]\n"
    "ds_read_b128 a[108:111], v11 offset:4096\n"
    "v_mfma_f32_16x16x32_bf16 v[52:55], a[44:47], a[68:71], 0\n"
    "buffer_load_dwordx4 v17, s[36:39], s85 offen offset:1024 lds\n"
    "v_mfma_f32_16x16x32_bf16 v[52:55], a[40:43], a[64:67], v[52:55]\n"
    "ds_read_b128 a[112:115], v9 offset:8192\n"
    "v_accvgpr_write_b32 a200, v215\n"
    "s_waitcnt lgkmcnt(6)\n"
    "v_mfma_f32_16x16x32_bf16 v[100:103], a[40:43], a[80:83], 0\n"
    "buffer_load_dwordx4 v18, s[36:39], s85 offen offset:2048 lds\n"
    "v_mfma_f32_16x16x32_bf16 v[100:103], a[44:47], a[84:87], v[100:103]\n"
    "ds_read_b128 a[116:119], v11 offset:8192\n"
    "s_waitcnt lgkmcnt(6)\n"
    "v_mfma_f32_16x16x32_bf16 v[124:127], a[44:47], a[92:95], 0\n"
    "v_mfma_f32_16x16x32_bf16 v[124:127], a[40:43], a[88:91], v[124:127]\n"
    "buffer_load_dwordx4 v19, s[36:39], s85 offen offset:3072 lds\n"
    "v_mfma_f32_16x16x32_bf16 v[120:123], a[32:35], a[88:91], 0\n"
    "s_add_u32 s85, s85, 128\n"
    "ds_read_b128 a[120:123], v9 offset:12288\n"
    "v_mfma_f32_16x16x32_bf16 v[120:123], a[36:39], a[92:95], v[120:123]\n"
    "ds_read_b128 a[124:127], v11 offset:12288\n"
    "v_accvgpr_write_b32 a201, v214\n"
    "v_mfma_f32_16x16x32_bf16 v[96:99], a[36:39], a[84:87], 0\n"
    "v_mfma_f32_16x16x32_bf16 v[96:99], a[32:35], a[80:83], v[96:99]\n"
    "v_accvgpr_write_b32 a202, 0\n"
    "v_mfma_f32_16x16x32_bf16 v[92:95], a[24:27], a[80:83], 0\n"
    "v_mfma_f32_16x16x32_bf16 v[92:95], a[28:31], a[84:87], v[92:95]\n"
    "v_mfma_f32_16x16x32_bf16 v[116:119], a[28:31], a[92:95], 0\n"
    "v_accvgpr_write_b32 a203, 0\n"
    "v_mfma_f32_16x16x32_bf16 v[116:119], a[24:27], a[88:91], v[116:119]\n"
    "v_mfma_f32_16x16x32_bf16 v[112:115], a[16:19], a[88:91], 0\n"
    "v_mfma_f32_16x16x32_bf16 v[112:115], a[20:23], a[92:95], v[112:115]\n"
    "s_waitcnt lgkmcnt(0)\n"
    "s_barrier\n"
    "v_accvgpr_write_b32 a204, 0\n"
    "v_mfma_f32_16x16x32_bf16 v[88:91], a[20:23], a[84:87], 0\n"
    "s_add_u32 m0, s53, 0\n"
    "v_mfma_f32_16x16x32_bf16 v[88:91], a[16:19], a[80:83], v[88:91]\n"
    "buffer_load_dwordx4 v20, s[40:43], s86 offen lds\n"
    "v_mfma_f32_16x16x32_bf16 v[84:87], a[8:11], a[80:83], 0\n"
    "buffer_load_dwordx4 v21, s[40:43], s86 offen offset:1024 lds\n"
    "v_mfma_f32_16x16x32_bf16 v[84:87], a[12:15], a[84:87], v[84:87]\n"
    "v_mfma_f32_16x16x32_bf16 v[108:111], a[12:15], a[92:95], 0\n"
    "buffer_load_dwordx4 v22, s[40:43], s86 offen offset:2048 lds\n"
    "v_mfma_f32_16x16x32_bf16 v[108:111], a[8:11], a[88:91], v[108:111]\n"
    "s_add_u32 s86, s86, s55\n"
    "s_add_u32 m0, s53, 12288\n"
    "v_accvgpr_write_b32 a205, 0\n"
    "v_mfma_f32_16x16x32_bf16 v[104:107], a[0:3], a[88:91], 0\n"
    "buffer_load_dwordx4 v20, s[40:43], s86 offen lds\n"
    "v_mfma_f32_16x16x32_bf16 v[104:107], a[4:7], a[92:95], v[104:107]\n"
    "v_mfma_f32_16x16x32_bf16 v[80:83], a[4:7], a[84:87], 0\n"
    "buffer_load_dwordx4 v21, s[40:43], s86 offen offset:1024 lds\n"
    "v_mfma_f32_16x16x32_bf16 v[80:83], a[0:3], a[80:83], v[80:83]\n"
    "v_accvgpr_write_b32 a206, v215\n"
    "v_mfma_f32_16x16x32_bf16 v[148:151], a[40:43], a[96:99], 0\n"
    "buffer_load_dwordx4 v22, s[40:43], s86 offen offset:2048 lds\n"
    "v_mfma_f32_16x16x32_bf16 v[148:151], a[44:47], a[100:103], v[148:151]\n"
    "s_add_u32 s86, s86, s55\n"
    "v_mfma_f32_16x16x32_bf16 v[172:175], a[44:47], a[108:111], 0\n"
    "v_mfma_f32_16x16x32_bf16 v[172:175], a[40:43], a[104:107], v[172:175]\n"
    "buffer_load_dwordx4 a[136:139], v24, s[40:43], s89 offen\n"
    "v_mfma_f32_16x16x32_bf16 v[168:171], a[32:35], a[104:107], 0\n"
    "v_accvgpr_write_b32 a207, v214\n"
    "v_mfma_f32_16x16x32_bf16 v[168:171], a[36:39], a[108:111], v[168:171]\n"
    "s_add_u32 s94, s89, s27\n"
    "v_mfma_f32_16x16x32_bf16 v[144:147], a[36:39], a[100:103], 0\n"
    "buffer_load_dwordx4 a[140:143], v24, s[40:43], s94 offen\n"
    "v_mfma_f32_16x16x32_bf16 v[144:147], a[32:35], a[96:99], v[144:147]\n"
    "v_mfma_f32_16x16x32_bf16 v[140:143], a[24:27], a[96:99], 0\n"
    "v_accvgpr_write_b32 a208, v215\n"
    "v_mfma_f32_16x16x32_bf16 v[140:143], a[28:31], a[100:103], v[140:143]\n"
    "s_lshl_b32 s94, s55, 2\n"
    "s_add_u32 s89, s89, s94\n"
    "v_mfma_f32_16x16x32_bf16 v[164:167], a[28:31], a[108:111], 0\n"
    "buffer_load_dwordx4 v[228:231], v26, s[36:39], s91 offen\n"
    "v_mfma_f32_16x16x32_bf16 v[164:167], a[24:27], a[104:107], v[164:167]\n"
    "v_accvgpr_write_b32 a209, v214\n"
    "v_mfma_f32_16x16x32_bf16 v[160:163], a[16:19], a[104:107], 0\n"
    "v_mfma_f32_16x16x32_bf16 v[160:163], a[20:23], a[108:111], v[160:163]\n"
    "buffer_load_dwordx4 v[232:235], v26, s[36:39], s91 offen offset:256\n"
    "v_mfma_f32_16x16x32_bf16 v[136:139], a[20:23], a[100:103], 0\n"
    "v_mfma_f32_16x16x32_bf16 v[136:139], a[16:19], a[96:99], v[136:139]\n"
    "v_accvgpr_write_b32 a210, v215\n"
    "v_mfma_f32_16x16x32_bf16 v[132:135], a[8:11], a[96:99], 0\n"
    "s_add_u32 s91, s91, 512\n"
    "v_mfma_f32_16x16x32_bf16 v[132:135], a[12:15], a[100:103], v[132:135]\n"
    "buffer_store_dwordx4 a[216:219], v25, s[44:47], s90 offen sc1\n"
    "v_mfma_f32_16x16x32_bf16 v[156:159], a[12:15], a[108:111], 0\n"
    "s_add_u32 s90, s90, 1024\n"
    "v_mfma_f32_16x16x32_bf16 v[156:159], a[8:11], a[104:107], v[156:159]\n"
    "v_accvgpr_write_b32 a211, v214\n"
    "v_mfma_f32_16x16x32_bf16 v[152:155], a[0:3], a[104:107], 0\n"
    "v_mfma_f32_16x16x32_bf16 v[152:155], a[4:7], a[108:111], v[152:155]\n"
    "v_mfma_f32_16x16x32_bf16 v[128:131], a[4:7], a[100:103], 0\n"
    "v_mfma_f32_16x16x32_bf16 v[128:131], a[0:3], a[96:99], v[128:131]\n"
    "s_waitcnt vmcnt(11)\n"
    "s_barrier\n"
    "v_mfma_f32_16x16x32_bf16 v[176:179], a[0:3], a[112:115], 0\n"
    "ds_read_b128 a[64:67], v8 offset:36864\n"
    "v_lshlrev_b32 v236, 16, v240\n"
    "v_and_b32 v237, 0xffff0000, v240\n"
    "v_mfma_f32_16x16x32_bf16 v[176:179], a[4:7], a[116:119], v[176:179]\n"
    "ds_read_b128 a[68:71], v10 offset:36864\n"
    "v_add_f32 v224, v224, v236\n"
    "v_add_f32 v225, v225, v237\n"
    "v_mfma_f32_16x16x32_bf16 v[200:203], a[4:7], a[124:127], 0\n"
    "ds_read_b128 a[72:75], v8 offset:40960\n"
    "v_fmac_f32 v226, v236, v236\n"
    "v_fmac_f32 v227, v237, v237\n"
    "v_mfma_f32_16x16x32_bf16 v[200:203], a[0:3], a[120:123], v[200:203]\n"
    "ds_read_b128 a[76:79], v10 offset:40960\n"
    "v_lshlrev_b32 v238, 16, v241\n"
    "v_and_b32 v239, 0xffff0000, v241\n"
    "v_mfma_f32_16x16x32_bf16 v[204:207], a[8:11], a[120:123], 0\n"
    "ds_read_b64_tr_b16 a[0:1], v14 offset:24576\n"
    "v_add_f32 v224, v224, v238\n"
    "v_add_f32 v225, v225, v239\n"
    "v_mfma_f32_16x16x32_bf16 v[204:207], a[12:15], a[124:127], v[204:207]\n"
    "ds_read_b64_tr_b16 a[2:3], v14 offset:25088\n"
    "v_fmac_f32 v226, v238, v238\n"
    "v_fmac_f32 v227, v239, v239\n"
    "v_mfma_f32_16x16x32_bf16 v[180:183], a[12:15], a[116:119], 0\n"
    "ds_read_b64_tr_b16 a[4:5], v14 offset:36864\n"
    "v_lshlrev_b32 v236, 16, v242\n"
    "v_and_b32 v237, 0xffff0000, v242\n"
    "v_mfma_f32_16x16x32_bf16 v[180:183], a[8:11], a[112:115], v[180:183]\n"
    "ds_read_b64_tr_b16 a[6:7], v14 offset:37376\n"
    "v_add_f32 v224, v224, v236\n"
    "v_add_f32 v225, v225, v237\n"
    "v_mfma_f32_16x16x32_bf16 v[184:187], a[16:19], a[112:115], 0\n"
    "ds_read_b64_tr_b16 a[8:9], v15 offset:24576\n"
    "v_fmac_f32 v226, v236, v236\n"
    "v_fmac_f32 v227, v237, v237\n"
    "v_mfma_f32_16x16x32_bf16 v[184:187], a[20:23], a[116:119], v[184:187]\n"
    "ds_read_b64_tr_b16 a[10:11], v15 offset:25088\n"
    "v_lshlrev_b32 v238, 16, v243\n"
    "v_and_b32 v239, 0xffff0000, v243\n"
    "v_mfma_f32_16x16x32_bf16 v[208:211], a[20:23], a[124:127], 0\n"
    "ds_read_b64_tr_b16 a[12:13], v15 offset:36864\n"
    "v_add_f32 v224, v224, v238\n"
    "v_add_f32 v225, v225, v239\n"
    "v_mfma_f32_16x16x32_bf16 v[208:211], a[16:19], a[120:123], v[208:211]\n"
    "ds_read_b64_tr_b16 a[14:15], v15 offset:37376\n"
    "v_fmac_f32 v226, v238, v238\n"
    "v_fmac_f32 v227, v239, v239\n"
    "v_mfma_f32_16x16x32_bf16 v[212:215], a[24:27], a[120:123], 0\n"
    "ds_read_b64_tr_b16 a[16:17], v14 offset:25600\n"
    "v_lshlrev_b32 v236, 16, v244\n"
    "v_and_b32 v237, 0xffff0000, v244\n"
    "v_mfma_f32_16x16x32_bf16 v[212:215], a[28:31], a[124:127], v[212:215]\n"
    "ds_read_b64_tr_b16 a[18:19], v14 offset:26112\n"
    "v_add_f32 v224, v224, v236\n"
    "v_add_f32 v225, v225, v237\n"
    "v_mfma_f32_16x16x32_bf16 v[188:191], a[28:31], a[116:119], 0\n"
    "ds_read_b64_tr_b16 a[20:21], v14 offset:37888\n"
    "v_fmac_f32 v226, v236, v236\n"
    "v_fmac_f32 v227, v237, v237\n"
    "v_mfma_f32_16x16x32_bf16 v[188:191], a[24:27], a[112:115], v[188:191]\n"
    "ds_read_b64_tr_b16 a[22:23], v14 offset:38400\n"
    "v_lshlrev_b32 v238, 16, v245\n"
    "v_and_b32 v239, 0xffff0000, v245\n"
    "v_mfma_f32_16x16x32_bf16 v[192:195], a[32:35], a[112:115], 0\n"
    "ds_read_b64_tr_b16 a[24:25], v15 offset:25600\n"
    "v_add_f32 v224, v224, v238\n"
    "v_add_f32 v225, v225, v239\n"
    "v_mfma_f32_16x16x32_bf16 v[192:195], a[36:39], a[116:119], v[192:195]\n"
    "ds_read_b64_tr_b16 a[26:27], v15 offset:26112\n"
    "v_fmac_f32 v226, v238, v238\n"
    "v_fmac_f32 v227, v239, v239\n"
    "v_mfma_f32_16x16x32_bf16 v[216:219], a[36:39], a[124:127], 0\n"
    "ds_read_b64_tr_b16 a[28:29], v15 offset:37888\n"
    "v_lshlrev_b32 v236, 16, v246\n"
    "v_and_b32 v237, 0xffff0000, v246\n"
    "v_mfma_f32_16x16x32_bf16 v[216:219], a[32:35], a[120:123], v[216:219]\n"
    "ds_read_b64_tr_b16 a[30:31], v15 offset:38400\n"
    "v_add_f32 v224, v224, v236\n"
    "v_add_f32 v225, v225, v237\n"
    "v_mfma_f32_16x16x32_bf16 v[220:223], a[40:43], a[120:123], 0\n"
    "ds_read_b64_tr_b16 a[32:33], v12 offset:26624\n"
    "v_fmac_f32 v226, v236, v236\n"
    "v_fmac_f32 v227, v237, v237\n"
    "v_mfma_f32_16x16x32_bf16 v[220:223], a[44:47], a[124:127], v[220:223]\n"
    "ds_read_b64_tr_b16 a[34:35], v12 offset:27136\n"
    "v_lshlrev_b32 v238, 16, v247\n"
    "v_and_b32 v239, 0xffff0000, v247\n"
    "v_mfma_f32_16x16x32_bf16 v[196:199], a[44:47], a[116:119], 0\n"
    "ds_read_b64_tr_b16 a[36:37], v12 offset:38912\n"
    "v_add_f32 v224, v224, v238\n"
    "v_add_f32 v225, v225, v239\n"
    "v_mfma_f32_16x16x32_bf16 v[196:199], a[40:43], a[112:115], v[196:199]\n"
    "ds_read_b64_tr_b16 a[38:39], v12 offset:39424\n"
    "v_fmac_f32 v226, v238, v238\n"
    "v_fmac_f32 v227, v239, v239\n"
    "s_branch .Lk2_qu1\n"
    ".Lk2_qloop:\n"
    "s_waitcnt lgkmcnt(6)\n"
    "v_mfma_f32_16x16x32_bf16 v[32:35], a[0:3], a[64:67], v[32:35]\n"
    "s_add_u32 m0, s52, 36864\n"
    "ds_read_b64_tr_b16 a[40:41], v13 offset:2048\n"
    "v_mfma_f32_16x16x32_bf16 v[32:35], a[4:7], a[68:71], v[32:35]\n"
    "buffer_load_dwordx4 v16, s[36:39], s84 offen lds\n"
    "v_mfma_f32_16x16x32_bf16 v[56:59], a[4:7], a[76:79], v[56:59]\n"
    "ds_read_b64_tr_b16 a[42:43], v13 offset:2560\n"
    "v_mfma_f32_16x16x32_bf16 v[56:59], a[0:3], a[72:75], v[56:59]\n"
    "ds_read_b64_tr_b16 a[44:45], v13 offset:14336\n"
    "v_mfma_f32_16x16x32_bf16 v[60:63], a[8:11], a[72:75], v[60:63]\n"
    "buffer_load_dwordx4 v17, s[36:39], s84 offen offset:1024 lds\n"
    "v_mfma_f32_16x16x32_bf16 v[60:63], a[12:15], a[76:79], v[60:63]\n"
    "ds_read_b64_tr_b16 a[46:47], v13 offset:14848\n"
    "v_mfma_f32_16x16x32_bf16 v[36:39], a[12:15], a[68:71], v[36:39]\n"
    "v_mfma_f32_16x16x32_bf16 v[36:39], a[8:11], a[64:67], v[36:39]\n"
    "ds_read_b128 a[80:83], v8 offset:8192\n"
    "v_mfma_f32_16x16x32_bf16 v[40:43], a[16:19], a[64:67], v[40:43]\n"
    "buffer_load_dwordx4 v18, s[36:39], s84 offen offset:2048 lds\n"
    "v_mfma_f32_16x16x32_bf16 v[40:43], a[20:23], a[68:71], v[40:43]\n"
    "ds_read_b128 a[84:87], v10 offset:8192\n"
    "v_mfma_f32_16x16x32_bf16 v[64:67], a[20:23], a[76:79], v[64:67]\n"
    "v_mfma_f32_16x16x32_bf16 v[64:67], a[16:19], a[72:75], v[64:67]\n"
    "buffer_load_dwordx4 v19, s[36:39], s84 offen offset:3072 lds\n"
    "v_mfma_f32_16x16x32_bf16 v[68:71], a[24:27], a[72:75], v[68:71]\n"
    "ds_read_b128 a[88:91], v8 offset:12288\n"
    "s_waitcnt lgkmcnt(6)\n"
    "v_mfma_f32_16x16x32_bf16 v[68:71], a[28:31], a[76:79], v[68:71]\n"
    "ds_read_b128 a[92:95], v10 offset:12288\n"
    "v_mfma_f32_16x16x32_bf16 v[44:47], a[28:31], a[68:71], v[44:47]\n"
    "s_add_u32 s84, s84, 128\n"
    "s_add_u32 m0, s52, 40960\n"
    "v_mfma_f32_16x16x32_bf16 v[44:47], a[24:27], a[64:67], v[44:47]\n"
    "buffer_load_dwordx4 v16, s[36:39], s85 offen lds\n"
    "v_mfma_f32_16x16x32_bf16 v[48:51], a[32:35], a[64:67], v[48:51]\n"
    "ds_read_b128 a[96:99], v9 offset:0\n"
    "v_mfma_f32_16x16x32_bf16 v[48:51], a[36:39], a[68:71], v[48:51]\n"
    "ds_read_b128 a[100:103], v11 offset:0\n"
    "v_mfma_f32_16x16x32_bf16 v[72:75], a[36:39], a[76:79], v[72:75]\n"
    "ds_read_b128 a[104:107], v9 offset:4096\n"
    "v_mfma_f32_16x16x32_bf16 v[72:75], a[32:35], a[72:75], v[72:75]\n"
    "buffer_load_dwordx4 v17, s[36:39], s85 offen offset:1024 lds\n"
    "s_waitcnt lgkmcnt(6)\n"
    "v_mfma_f32_16x16x32_bf16 v[76:79], a[40:43], a[72:75], v[76:79]\n"
    "ds_read_b128 a[108:111], v11 offset:4096\n"
    "v_mfma_f32_16x16x32_bf16 v[76:79], a[44:47], a[76:79], v[76:79]\n"
    "v_mfma_f32_16x16x32_bf16 v[52:55], a[44:47], a[68:71], v[52:55]\n"
    "buffer_load_dwordx4 v18, s[36:39], s85 offen offset:2048 lds\n"
    "v_mfma_f32_16x16x32_bf16 v[52:55], a[40:43], a[64:67], v[52:55]\n"
    "ds_read_b128 a[112:115], v9 offset:8192\n"
    "v_mfma_f32_16x16x32_bf16 v[100:103], a[40:43], a[80:83], v[100:103]\n"
    "s_waitcnt lgkmcnt(6)\n"
    "v_mfma_f32_16x16x32_bf16 v[100:103], a[44:47], a[84:87], v[100:103]\n"
    "ds_read_b128 a[116:119], v11 offset:8192\n"
    "s_waitcnt lgkmcnt(6)\n"
    "v_mfma_f32_16x16x32_bf16 v[124:127], a[44:47], a[92:95], v[124:127]\n"
    "buffer_load_dwordx4 v19, s[36:39], s85 offen offset:3072 lds\n"
    "v_mfma_f32_16x16x32_bf16 v[124:127], a[40:43], a[88:91], v[124:127]\n"
    "s_add_u32 s85, s85, 128\n"
    "ds_read_b128 a[120:123], v9 offset:12288\n"
    "v_mfma_f32_16x16x32_bf16 v[120:123], a[32:35], a[88:91], v[120:123]\n"
    "v_mfma_f32_16x16x32_bf16 v[120:123], a[36:39], a[92:95], v[120:123]\n"
    "ds_read_b128 a[124:127], v11 offset:12288\n"
    "v_mfma_f32_16x16x32_bf16 v[96:99], a[36:39], a[84:87], v[96:99]\n"
    "v_mfma_f32_16x16x32_bf16 v[96:99], a[32:35], a[80:83], v[96:99]\n"
    "v_mfma_f32_16x16x32_bf16 v[92:95], a[24:27], a[80:83], v[92:95]\n"
    "v_mfma_f32_16x16x32_bf16 v[92:95], a[28:31], a[84:87], v[92:95]\n"
    "v_mfma_f32_16x16x32_bf16 v[116:119], a[28:31], a[92:95], v[116:119]\n"
    "v_mfma_f32_16x16x32_bf16 v[116:119], a[24:27], a[88:91], v[116:119]\n"
    "v_mfma_f32_16x16x32_bf16 v[112:115], a[16:19], a[88:91], v[112:115]\n"
    "v_mfma_f32_16x16x32_bf16 v[112:115], a[20:23], a[92:95], v[112:115]\n"
    "s_waitcnt lgkmcnt(0)\n"
    "s_barrier\n"
    "v_mfma_f32_16x16x32_bf16 v[88:91], a[20:23], a[84:87], v[88:91]\n"
    "s_add_u32 m0, s53, 0\n"
    "v_mfma_f32_16x16x32_bf16 v[88:91], a[16:19], a[80:83], v[88:91]\n"
    "buffer_load_dwordx4 v20, s[40:43], s86 offen lds\n"
    "v_mfma_f32_16x16x32_bf16 v[84:87], a[8:11], a[80:83], v[84:87]\n"
    "buffer_load_dwordx4 v21, s[40:43], s86 offen offset:1024 lds\n"
    "v_mfma_f32_16x16x32_bf16 v[84:87], a[12:15], a[84:87], v[84:87]\n"
    "v_mfma_f32_16x16x32_bf16 v[108:111], a[12:15], a[92:95], v[108:111]\n"
    "buffer_load_dwordx4 v22, s[40:43], s86 offen offset:2048 lds\n"
    "v_mfma_f32_16x16x32_bf16 v[108:111], a[8:11], a[88:91], v[108:111]\n"
    "s_add_u32 s86, s86, s55\n"
    "s_add_u32 m0, s53, 12288\n"
    "v_mfma_f32_16x16x32_bf16 v[104:107], a[0:3], a[88:91], v[104:107]\n"
    "buffer_load_dwordx4 v20, s[40:43], s86 offen lds\n"
    "v_mfma_f32_16x16x32_bf16 v[104:107], a[4:7], a[92:95], v[104:107]\n"
    "v_mfma_f32_16x16x32_bf16 v[80:83], a[4:7], a[84:87], v[80:83]\n"
    "buffer_load_dwordx4 v21, s[40:43], s86 offen offset:1024 lds\n"
    "v_mfma_f32_16x16x32_bf16 v[80:83], a[0:3], a[80:83], v[80:83]\n"
    ".Lk2_qback:\n"
    "v_mfma_f32_16x16x32_bf16 v[148:151], a[40:43], a[96:99], v[148:151]\n"
    "buffer_load_dwordx4 v22, s[40:43], s86 offen offset:2048 lds\n"
    "v_mfma_f32_16x16x32_bf16 v[148:151], a[44:47], a[100:103], v[148:151]\n"
    "s_add_u32 s86, s86, s55\n"
    "v_mfma_f32_16x16x32_bf16 v[172:175], a[44:47], a[108:111], v[172:175]\n"
    "v_mfma_f32_16x16x32_bf16 v[172:175], a[40:43], a[104:107], v[172:175]\n"
    "buffer_load_dwordx4 a[136:139], v24, s[40:43], s89 offen\n"
    "v_mfma_f32_16x16x32_bf16 v[168:171], a[32:35], a[104:107], v[168:171]\n"
    "v_mfma_f32_16x16x32_bf16 v[168:171], a[36:39], a[108:111], v[168:171]\n"
    "s_add_u32 s94, s89, s27\n"
    "v_mfma_f32_16x16x32_bf16 v[144:147], a[36:39], a[100:103], v[144:147]\n"
    "buffer_load_dwordx4 a[140:143], v24, s[40:43], s94 offen\n"
    "v_mfma_f32_16x16x32_bf16 v[144:147], a[32:35], a[96:99], v[144:147]\n"
    "v_mfma_f32_16x16x32_bf16 v[140:143], a[24:27], a[96:99], v[140:143]\n"
    "v_mfma_f32_16x16x32_bf16 v[140:143], a[28:31], a[100:103], v[140:143]\n"
    "s_lshl_b32 s94, s55, 2\n"
    "s_add_u32 s89, s89, s94\n"
    "v_mfma_f32_16x16x32_bf16 v[164:167], a[28:31], a[108:111], v[164:167]\n"
    "buffer_load_dwordx4 v[228:231], v26, s[36:39], s91 offen\n"
    "v_mfma_f32_16x16x32_bf16 v[164:167], a[24:27], a[104:107], v[164:167]\n"
    "v_mfma_f32_16x16x32_bf16 v[160:163], a[16:19], a[104:107], v[160:163]\n"
    "v_mfma_f32_16x16x32_bf16 v[160:163], a[20:23], a[108:111], v[160:163]\n"
    "buffer_load_dwordx4 v[232:235], v26, s[36:39], s91 offen offset:256\n"
    "v_mfma_f32_16x16x32_bf16 v[136:139], a[20:23], a[100:103], v[136:139]\n"
    "v_mfma_f32_16x16x32_bf16 v[136:139], a[16:19], a[96:99], v[136:139]\n"
    "v_mfma_f32_16x16x32_bf16 v[132:135], a[8:11], a[96:99], v[132:135]\n"
    "s_add_u32 s91, s91, 512\n"
    "v_mfma_f32_16x16x32_bf16 v[132:135], a[12:15], a[100:103], v[132:135]\n"
    "buffer_store_dwordx4 a[216:219], v25, s[44:47], s90 offen sc1\n"
    "v_mfma_f32_16x16x32_bf16 v[156:159], a[12:15], a[108:111], v[156:159]\n"
    "s_add_u32 s90, s90, 1024\n"
    "v_mfma_f32_16x16x32_bf16 v[156:159], a[8:11], a[104:107], v[156:159]\n"
    "v_mfma_f32_16x16x32_bf16 v[152:155], a[0:3], a[104:107], v[152:155]\n"
    "v_mfma_f32_16x16x32_bf16 v[152:155], a[4:7], a[108:111], v[152:155]\n"
    "v_mfma_f32_16x16x32_bf16 v[128:131], a[4:7], a[100:103], v[128:131]\n"
    "v_mfma_f32_16x16x32_bf16 v[128:131], a[0:3], a[96:99], v[128:131]\n"
    "s_waitcnt vmcnt(11)\n"
    "s_barrier\n"
    "v_mfma_f32_16x16x32_bf16 v[176:179], a[0:3], a[112:115], v[176:179]\n"
    "ds_read_b128 a[64:67], v8 offset:36864\n"
    "v_mfma_f32_16x16x32_bf16 v[176:179], a[4:7], a[116:119], v[176:179]\n"
    "ds_read_b128 a[68:71], v10 offset:36864\n"
    "v_mfma_f32_16x16x32_bf16 v[200:203], a[4:7], a[124:127], v[200:203]\n"
    "ds_read_b128 a[72:75], v8 offset:40960\n"
    "v_mfma_f32_16x16x32_bf16 v[200:203], a[0:3], a[120:123], v[200:203]\n"
    "ds_read_b128 a[76:79], v10 offset:40960\n"
    "v_mfma_f32_16x16x32_bf16 v[204:207], a[8:11], a[120:123], v[204:207]\n"
    "ds_read_b64_tr_b16 a[0:1], v14 offset:24576\n"
    "v_mfma_f32_16x16x32_bf16 v[204:207], a[12:15], a[124:127], v[204:207]\n"
    "ds_read_b64_tr_b16 a[2:3], v14 offset:25088\n"
    "v_mfma_f32_16x16x32_bf16 v[180:183], a[12:15], a[116:119], v[180:183]\n"
    "ds_read_b64_tr_b16 a[4:5], v14 offset:36864\n"
    "v_mfma_f32_16x16x32_bf16 v[180:183], a[8:11], a[112:115], v[180:183]\n"
    "ds_read_b64_tr_b16 a[6:7], v14 offset:37376\n"
    "v_mfma_f32_16x16x32_bf16 v[184:187], a[16:19], a[112:115], v[184:187]\n"
    "ds_read_b64_tr_b16 a[8:9], v15 offset:24576\n"
    "v_mfma_f32_16x16x32_bf16 v[184:187], a[20:23], a[116:119], v[184:187]\n"
    "ds_read_b64_tr_b16 a[10:11], v15 offset:25088\n"
    "v_mfma_f32_16x16x32_bf16 v[208:211], a[20:23], a[124:127], v[208:211]\n"
    "ds_read_b64_tr_b16 a[12:13], v15 offset:36864\n"
    "v_mfma_f32_16x16x32_bf16 v[208:211], a[16:19], a[120:123], v[208:211]\n"
    "ds_read_b64_tr_b16 a[14:15], v15 offset:37376\n"
    "v_mfma_f32_16x16x32_bf16 v[212:215], a[24:27], a[120:123], v[212:215]\n"
    "ds_read_b64_tr_b16 a[16:17], v14 offset:25600\n"
    "v_mfma_f32_16x16x32_bf16 v[212:215], a[28:31], a[124:127], v[212:215]\n"
    "ds_read_b64_tr_b16 a[18:19], v14 offset:26112\n"
    "v_mfma_f32_16x16x32_bf16 v[188:191], a[28:31], a[116:119], v[188:191]\n"
    "ds_read_b64_tr_b16 a[20:21], v14 offset:37888\n"
    "v_mfma_f32_16x16x32_bf16 v[188:191], a[24:27], a[112:115], v[188:191]\n"
    "ds_read_b64_tr_b16 a[22:23], v14 offset:38400\n"
    "v_mfma_f32_16x16x32_bf16 v[192:195], a[32:35], a[112:115], v[192:195]\n"
    "ds_read_b64_tr_b16 a[24:25], v15 offset:25600\n"
    "v_mfma_f32_16x16x32_bf16 v[192:195], a[36:39], a[116:119], v[192:195]\n"
    "ds_read_b64_tr_b16 a[26:27], v15 offset:26112\n"
    "v_mfma_f32_16x16x32_bf16 v[216:219], a[36:39], a[124:127], v[216:219]\n"
    "ds_read_b64_tr_b16 a[28:29], v15 offset:37888\n"
    "v_mfma_f32_16x16x32_bf16 v[216:219], a[32:35], a[120:123], v[216:219]\n"
    "ds_read_b64_tr_b16 a[30:31], v15 offset:38400\n"
    "v_mfma_f32_16x16x32_bf16 v[220:223], a[40:43], a[120:123], v[220:223]\n"
    "ds_read_b64_tr_b16 a[32:33], v12 offset:26624\n"
    "v_mfma_f32_16x16x32_bf16 v[220:223], a[44:47], a[124:127], v[220:223]\n"
    "ds_read_b64_tr_b16 a[34:35], v12 offset:27136\n"
    "v_mfma_f32_16x16x32_bf16 v[196:199], a[44:47], a[116:119], v[196:199]\n"
    "ds_read_b64_tr_b16 a[36:37], v12 offset:38912\n"
    "v_mfma_f32_16x16x32_bf16 v[196:199], a[40:43], a[112:115], v[196:199]\n"
    "ds_read_b64_tr_b16 a[38:39], v12 offset:39424\n"
    ".Lk2_qu1:\n"
    "s_waitcnt lgkmcnt(6)\n"
    "v_mfma_f32_16x16x32_bf16 v[32:35], a[0:3], a[64:67], v[32:35]\n"
    "s_add_u32 m0, s52, 0\n"
    "ds_read_b64_tr_b16 a[40:41], v13 offset:26624\n"
    "v_mfma_f32_16x16x32_bf16 v[32:35], a[4:7], a[68:71], v[32:35]\n"
    "buffer_load_dwordx4 v16, s[36:39], s84 offen lds\n"
    "v_mfma_f32_16x16x32_bf16 v[56:59], a[4:7], a[76:79], v[56:59]\n"
    "ds_read_b64_tr_b16 a[42:43], v13 offset:27136\n"
    "v_mfma_f32_16x16x32_bf16 v[56:59], a[0:3], a[72:75], v[56:59]\n"
    "ds_read_b64_tr_b16 a[44:45], v13 offset:38912\n"
    "v_mfma_f32_16x16x32_bf16 v[60:63], a[8:11], a[72:75], v[60:63]\n"
    "buffer_load_dwordx4 v17, s[36:39], s84 offen offset:1024 lds\n"
    "v_mfma_f32_16x16x32_bf16 v[60:63], a[12:15], a[76:79], v[60:63]\n"
    "ds_read_b64_tr_b16 a[46:47], v13 offset:39424\n"
    "v_mfma_f32_16x16x32_bf16 v[36:39], a[12:15], a[68:71], v[36:39]\n"
    "s_cmp_eq_u32 s88, 2\n"
    "s_cselect_b32 s34, 0, s34\n"
    "v_mfma_f32_16x16x32_bf16 v[36:39], a[8:11], a[64:67], v[36:39]\n"
    "ds_read_b128 a[80:83], v8 offset:45056\n"
    "v_mfma_f32_16x16x32_bf16 v[40:43], a[16:19], a[64:67], v[40:43]\n"
    "buffer_load_dwordx4 v18, s[36:39], s84 offen offset:2048 lds\n"
    "v_mfma_f32_16x16x32_bf16 v[40:43], a[20:23], a[68:71], v[40:43]\n"
    "ds_read_b128 a[84:87], v10 offset:45056\n"
    "v_mfma_f32_16x16x32_bf16 v[64:67], a[20:23], a[76:79], v[64:67]\n"
    "s_cmp_eq_u32 s88, 1\n"
    "s_cselect_b32 s90, s81, s90\n"
    "v_mfma_f32_16x16x32_bf16 v[64:67], a[16:19], a[72:75], v[64:67]\n"
    "buffer_load_dwordx4 v19, s[36:39], s84 offen offset:3072 lds\n"
    "v_mfma_f32_16x16x32_bf16 v[68:71], a[24:27], a[72:75], v[68:71]\n"
    "ds_read_b128 a[88:91], v8 offset:49152\n"
    "s_waitcnt lgkmcnt(6)\n"
    "v_mfma_f32_16x16x32_bf16 v[68:71], a[28:31], a[76:79], v[68:71]\n"
    "ds_read_b128 a[92:95], v10 offset:49152\n"
    "v_mfma_f32_16x16x32_bf16 v[44:47], a[28:31], a[68:71], v[44:47]\n"
    "s_add_u32 s84, s84, 128\n"
    "s_add_u32 m0, s52, 4096\n"
    "s_cmp_eq_u32 s88, 5\n"
    "s_cselect_b32 s90, 1073741824, s90\n"
    "v_mfma_f32_16x16x32_bf16 v[44:47], a[24:27], a[64:67], v[44:47]\n"
    "buffer_load_dwordx4 v16, s[36:39], s85 offen lds\n"
    "v_mfma_f32_16x16x32_bf16 v[48:51], a[32:35], a[64:67], v[48:51]\n"
    "ds_read_b128 a[96:99], v9 offset:36864\n"
    "v_mfma_f32_16x16x32_bf16 v[48:51], a[36:39], a[68:71], v[48:51]\n"
    "ds_read_b128 a[100:103], v11 offset:36864\n"
    "v_mfma_f32_16x16x32_bf16 v[72:75], a[36:39], a[76:79], v[72:75]\n"
    "ds_read_b128 a[104:107], v9 offset:40960\n"
    "v_mfma_f32_16x16x32_bf16 v[72:75], a[32:35], a[72:75], v[72:75]\n"
    "buffer_load_dwordx4 v17, s[36:39], s85 offen offset:1024 lds\n"
    "s_waitcnt lgkmcnt(6)\n"
    "v_mfma_f32_16x16x32_bf16 v[76:79], a[40:43], a[72:75], v[76:79]\n"
    "ds_read_b128 a[108:111], v11 offset:40960\n"
    "v_mfma_f32_16x16x32_bf16 v[76:79], a[44:47], a[76:79], v[76:79]\n"
    "s_cmp_ge_u32 s88, 2\n"
    "s_cselect_b32 s91, 1073741824, s91\n"
    "v_mfma_f32_16x16x32_bf16 v[52:55], a[44:47], a[68:71], v[52:55]\n"
    "buffer_load_dwordx4 v18, s[36:39], s85 offen offset:2048 lds\n"
    "v_mfma_f32_16x16x32_bf16 v[52:55], a[40:43], a[64:67], v[52:55]\n"
    "ds_read_b128 a[112:115], v9 offset:45056\n"
    "v_mfma_f32_16x16x32_bf16 v[100:103], a[40:43], a[80:83], v[100:103]\n"
    "s_waitcnt lgkmcnt(6)\n"
    "v_mfma_f32_16x16x32_bf16 v[100:103], a[44:47], a[84:87], v[100:103]\n"
    "ds_read_b128 a[116:119], v11 offset:45056\n"
    "s_waitcnt lgkmcnt(6)\n"
    "v_mfma_f32_16x16x32_bf16 v[124:127], a[44:47], a[92:95], v[124:127]\n"
    "buffer_load_dwordx4 v19, s[36:39], s85 offen offset:3072 lds\n"
    "v_mfma_f32_16x16x32_bf16 v[124:127], a[40:43], a[88:91], v[124:127]\n"
    "s_add_u32 s85, s85, 128\n"
    "ds_read_b128 a[120:123], v9 offset:49152\n"
    "v_mfma_f32_16x16x32_bf16 v[120:123], a[32:35], a[88:91], v[120:123]\n"
    "v_mfma_f32_16x16x32_bf16 v[120:123], a[36:39], a[92:95], v[120:123]\n"
    "ds_read_b128 a[124:127], v11 offset:49152\n"
    "v_mfma_f32_16x16x32_bf16 v[96:99], a[36:39], a[84:87], v[96:99]\n"
    "v_mfma_f32_16x16x32_bf16 v[96:99], a[32:35], a[80:83], v[96:99]\n"
    "v_mfma_f32_16x16x32_bf16 v[92:95], a[24:27], a[80:83], v[92:95]\n"
    "v_mfma_f32_16x16x32_bf16 v[92:95], a[28:31], a[84:87], v[92:95]\n"
    "v_mfma_f32_16x16x32_bf16 v[116:119], a[28:31], a[92:95], v[116:119]\n"
    "v_mfma_f32_16x16x32_bf16 v[116:119], a[24:27], a[88:91], v[116:119]\n"
    "v_mfma_f32_16x16x32_bf16 v[112:115], a[16:19], a[88:91], v[112:115]\n"
    "v_mfma_f32_16x16x32_bf16 v[112:115], a[20:23], a[92:95], v[112:115]\n"
    "s_waitcnt vmcnt(8) lgkmcnt(0)\n"
    "s_barrier\n"
    "v_mfma_f32_16x16x32_bf16 v[88:91], a[20:23], a[84:87], v[88:91]\n"
    "s_add_u32 m0, s53, 24576\n"
    "v_lshlrev_b32 v236, 16, v228\n"
    "v_mfma_f32_16x16x32_bf16 v[88:91], a[16:19], a[80:83], v[88:91]\n"
    "buffer_load_dwordx4 v20, s[40:43], s86 offen lds\n"
    "v_mfma_f32_16x16x32_bf16 v[84:87], a[8:11], a[80:83], v[84:87]\n"
    "buffer_load_dwordx4 v21, s[40:43], s86 offen offset:1024 lds\n"
    "v_mfma_f32_16x16x32_bf16 v[84:87], a[12:15], a[84:87], v[84:87]\n"
    "v_and_b32 v237, 0xffff0000, v228\n"
    "v_add_f32 v224, v224, v236\n"
    "v_mfma_f32_16x16x32_bf16 a[128:131], a[132:135], a[136:139], a[128:131]\n"
    "v_mfma_f32_16x16x32_bf16 v[108:111], a[12:15], a[92:95], v[108:111]\n"
    "buffer_load_dwordx4 v22, s[40:43], s86 offen offset:2048 lds\n"
    "v_mfma_f32_16x16x32_bf16 v[108:111], a[8:11], a[88:91], v[108:111]\n"
    "s_add_u32 s86, s86, s55\n"
    "s_add_u32 m0, s53, 36864\n"
    "v_add_f32 v225, v225, v237\n"
    "v_mfma_f32_16x16x32_bf16 v[104:107], a[0:3], a[88:91], v[104:107]\n"
    "buffer_load_dwordx4 v20, s[40:43], s86 offen lds\n"
    "v_mfma_f32_16x16x32_bf16 v[104:107], a[4:7], a[92:95], v[104:107]\n"
    "v_fmac_f32 v226, v236, v236\n"
    "v_fmac_f32 v227, v237, v237\n"
    "v_mfma_f32_16x16x32_bf16 v[80:83], a[4:7], a[84:87], v[80:83]\n"
    "buffer_load_dwordx4 v21, s[40:43], s86 offen offset:1024 lds\n"
    "v_mfma_f32_16x16x32_bf16 v[80:83], a[0:3], a[80:83], v[80:83]\n"
    "v_lshlrev_b32 v238, 16, v229\n"
    "v_and_b32 v239, 0xffff0000, v229\n"
    "v_mfma_f32_16x16x32_bf16 v[148:151], a[40:43], a[96:99], v[148:151]\n"
    "buffer_load_dwordx4 v22, s[40:43], s86 offen offset:2048 lds\n"
    "v_mfma_f32_16x16x32_bf16 v[148:151], a[44:47], a[100:103], v[148:151]\n"
    "s_add_u32 s86, s86, s55\n"
    "v_add_f32 v224, v224, v238\n"
    "v_add_f32 v225, v225, v239\n"
    "v_mfma_f32_16x16x32_bf16 a[128:131], a[132:135], a[140:143], a[128:131]\n"
    "v_mfma_f32_16x16x32_bf16 v[172:175], a[44:47], a[108:111], v[172:175]\n"
    "v_fmac_f32 v226, v238, v238\n"
    "v_fmac_f32 v227, v239, v239\n"
    "v_mfma_f32_16x16x32_bf16 v[172:175], a[40:43], a[104:107], v[172:175]\n"
    "v_lshlrev_b32 v236, 16, v230\n"
    "v_mfma_f32_16x16x32_bf16 v[168:171], a[32:35], a[104:107], v[168:171]\n"
    "buffer_load_dword a220, v28, s[48:51], s34 offen\n"
    "v_mfma_f32_16x16x32_bf16 v[168:171], a[36:39], a[108:111], v[168:171]\n"
    "s_add_u32 s34, s34, 4096\n"
    "v_and_b32 v237, 0xffff0000, v230\n"
    "v_add_f32 v224, v224, v236\n"
    "v_mfma_f32_16x16x32_bf16 v[144:147], a[36:39], a[100:103], v[144:147]\n"
    "v_add_f32 v225, v225, v237\n"
    "v_mfma_f32_16x16x32_bf16 v[144:147], a[32:35], a[96:99], v[144:147]\n"
    "v_fmac_f32 v226, v236, v236\n"
    "v_mfma_f32_16x16x32_bf16 v[140:143], a[24:27], a[96:99], v[140:143]\n"
    "v_fmac_f32 v227, v237, v237\n"
    "v_mfma_f32_16x16x32_bf16 v[140:143], a[28:31], a[100:103], v[140:143]\n"
    "v_lshlrev_b32 v238, 16, v231\n"
    "v_mfma_f32_16x16x32_bf16 v[164:167], a[28:31], a[108:111], v[164:167]\n"
    "v_and_b32 v239, 0xffff0000, v231\n"
    "v_mfma_f32_16x16x32_bf16 v[164:167], a[24:27], a[104:107], v[164:167]\n"
    "v_add_f32 v224, v224, v238\n"
    "v_mfma_f32_16x16x32_bf16 v[160:163], a[16:19], a[104:107], v[160:163]\n"
    "v_add_f32 v225, v225, v239\n"
    "v_mfma_f32_16x16x32_bf16 v[160:163], a[20:23], a[108:111], v[160:163]\n"
    "v_fmac_f32 v226, v238, v238\n"
    "v_mfma_f32_16x16x32_bf16 v[136:139], a[20:23], a[100:103], v[136:139]\n"
    "v_fmac_f32 v227, v239, v239\n"
    "v_mfma_f32_16x16x32_bf16 v[136:139], a[16:19], a[96:99], v[136:139]\n"
    "v_mfma_f32_16x16x32_bf16 v[132:135], a[8:11], a[96:99], v[132:135]\n"
    "v_lshlrev_b32 v236, 16, v232\n"
    "v_mfma_f32_16x16x32_bf16 v[132:135], a[12:15], a[100:103], v[132:135]\n"
    "v_and_b32 v237, 0xffff0000, v232\n"
    "v_mfma_f32_16x16x32_bf16 v[156:159], a[12:15], a[108:111], v[156:159]\n"
    "v_add_f32 v224, v224, v236\n"
    "v_mfma_f32_16x16x32_bf16 v[156:159], a[8:11], a[104:107], v[156:159]\n"
    "v_add_f32 v225, v225, v237\n"
    "v_mfma_f32_16x16x32_bf16 v[152:155], a[0:3], a[104:107], v[152:155]\n"
    "v_fmac_f32 v226, v236, v236\n"
    "v_mfma_f32_16x16x32_bf16 v[152:155], a[4:7], a[108:111], v[152:155]\n"
    "v_fmac_f32 v227, v237, v237\n"
    "v_mfma_f32_16x16x32_bf16 v[128:131], a[4:7], a[100:103], v[128:131]\n"
    "v_lshlrev_b32 v238, 16, v233\n"
    "v_mfma_f32_16x16x32_bf16 v[128:131], a[0:3], a[96:99], v[128:131]\n"
    "s_waitcnt vmcnt(7)\n"
    "s_barrier\n"
    "v_and_b32 v239, 0xffff0000, v233\n"
    "v_mfma_f32_16x16x32_bf16 v[176:179], a[0:3], a[112:115], v[176:179]\n"
    "ds_read_b128 a[64:67], v8 offset:0\n"
    "v_add_f32 v224, v224, v238\n"
    "v_mfma_f32_16x16x32_bf16 v[176:179], a[4:7], a[116:119], v[176:179]\n"
    "ds_read_b128 a[68:71], v10 offset:0\n"
    "v_add_f32 v225, v225, v239\n"
    "v_mfma_f32_16x16x32_bf16 v[200:203], a[4:7], a[124:127], v[200:203]\n"
    "ds_read_b128 a[72:75], v8 offset:4096\n"
    "v_fmac_f32 v226, v238, v238\n"
    "v_mfma_f32_16x16x32_bf16 v[200:203], a[0:3], a[120:123], v[200:203]\n"
    "ds_read_b128 a[76:79], v10 offset:4096\n"
    "v_fmac_f32 v227, v239, v239\n"
    "v_mfma_f32_16x16x32_bf16 v[204:207], a[8:11], a[120:123], v[204:207]\n"
    "ds_read_b64_tr_b16 a[0:1], v14 offset:0\n"
    "v_mfma_f32_16x16x32_bf16 v[204:207], a[12:15], a[124:127], v[204:207]\n"
    ".Lk2_m4321:\n"
    ".Lk2_hq1:\n"
    "s_cbranch_execz .Lk2_m6749\n"
    "ds_read_b64_tr_b16 a[2:3], v14 offset:512\n"
    "v_lshlrev_b32 v236, 16, v234\n"
    "v_mfma_f32_16x16x32_bf16 v[180:183], a[12:15], a[116:119], v[180:183]\n"
    "ds_read_b64_tr_b16 a[4:5], v14 offset:12288\n"
    "v_and_b32 v237, 0xffff0000, v234\n"
    "v_mfma_f32_16x16x32_bf16 v[180:183], a[8:11], a[112:115], v[180:183]\n"
    "ds_read_b64_tr_b16 a[6:7], v14 offset:12800\n"
    "v_add_f32 v224, v224, v236\n"
    "v_mfma_f32_16x16x32_bf16 v[184:187], a[16:19], a[112:115], v[184:187]\n"
    "ds_read_b64_tr_b16 a[8:9], v15 offset:0\n"
    "v_add_f32 v225, v225, v237\n"
    "v_mfma_f32_16x16x32_bf16 v[184:187], a[20:23], a[116:119], v[184:187]\n"
    "ds_read_b64_tr_b16 a[10:11], v15 offset:512\n"
    "v_fmac_f32 v226, v236, v236\n"
    "v_mfma_f32_16x16x32_bf16 v[208:211], a[20:23], a[124:127], v[208:211]\n"
    "ds_read_b64_tr_b16 a[12:13], v15 offset:12288\n"
    "v_fmac_f32 v227, v237, v237\n"
    "v_mfma_f32_16x16x32_bf16 v[208:211], a[16:19], a[120:123], v[208:211]\n"
    "ds_read_b64_tr_b16 a[14:15], v15 offset:12800\n"
    "v_lshlrev_b32 v238, 16, v235\n"
    "v_mfma_f32_16x16x32_bf16 v[212:215], a[24:27], a[120:123], v[212:215]\n"
    "ds_read_b64_tr_b16 a[16:17], v14 offset:1024\n"
    "v_and_b32 v239, 0xffff0000, v235\n"
    "v_mfma_f32_16x16x32_bf16 v[212:215], a[28:31], a[124:127], v[212:215]\n"
    "ds_read_b64_tr_b16 a[18:19], v14 offset:1536\n"
    "v_add_f32 v224, v224, v238\n"
    "v_mfma_f32_16x16x32_bf16 v[188:191], a[28:31], a[116:119], v[188:191]\n"
    "ds_read_b64_tr_b16 a[20:21], v14 offset:13312\n"
    "v_add_f32 v225, v225, v239\n"
    "v_mfma_f32_16x16x32_bf16 v[188:191], a[24:27], a[112:115], v[188:191]\n"
    "ds_read_b64_tr_b16 a[22:23], v14 offset:13824\n"
    "v_fmac_f32 v226, v238, v238\n"
    "v_mfma_f32_16x16x32_bf16 v[192:195], a[32:35], a[112:115], v[192:195]\n"
    "ds_read_b64_tr_b16 a[24:25], v15 offset:1024\n"
    "v_fmac_f32 v227, v239, v239\n"
    "v_mfma_f32_16x16x32_bf16 v[192:195], a[36:39], a[116:119], v[192:195]\n"
    "ds_read_b64_tr_b16 a[26:27], v15 offset:1536\n"
    "v_mfma_f32_16x16x32_bf16 v[216:219], a[36:39], a[124:127], v[216:219]\n"
    "ds_read_b64_tr_b16 a[28:29], v15 offset:13312\n"
    "v_mfma_f32_16x16x32_bf16 v[216:219], a[32:35], a[120:123], v[216:219]\n"
    "ds_read_b64_tr_b16 a[30:31], v15 offset:13824\n"
    "v_mfma_f32_16x16x32_bf16 v[220:223], a[40:43], a[120:123], v[220:223]\n"
    "ds_read_b64_tr_b16 a[32:33], v12 offset:2048\n"
    "v_mfma_f32_16x16x32_bf16 v[220:223], a[44:47], a[124:127], v[220:223]\n"
    "ds_read_b64_tr_b16 a[34:35], v12 offset:2560\n"
    "s_add_u32 s88, s88, 1\n"
    "v_mfma_f32_16x16x32_bf16 v[196:199], a[44:47], a[116:119], v[196:199]\n"
    "ds_read_b64_tr_b16 a[36:37], v12 offset:14336\n"
    "s_cmp_lt_u32 s88, 6\n"
    "v_mfma_f32_16x16x32_bf16 v[196:199], a[40:43], a[112:115], v[196:199]\n"
    "ds_read_b64_tr_b16 a[38:39], v12 offset:14848\n"
    "s_cbranch_scc1 .Lk2_qloop\n"
    "s_cmp_eq_u32 s88, 6\n"
    "s_cbranch_scc1 .Lk2_qpub\n"
    "s_waitcnt lgkmcnt(6)\n"
    "v_mfma_f32_16x16x32_bf16 v[32:35], a[0:3], a[64:67], v[32:35]\n"
    "s_add_u32 m0, s52, 36864\n"
    "ds_read_b64_tr_b16 a[40:41], v13 offset:2048\n"
    "v_mfma_f32_16x16x32_bf16 v[32:35], a[4:7], a[68:71], v[32:35]\n"
    "buffer_load_dwordx4 v16, s[36:39], s84 offen lds\n"
    "v_mfma_f32_16x16x32_bf16 v[56:59], a[4:7], a[76:79], v[56:59]\n"
    "ds_read_b64_tr_b16 a[42:43], v13 offset:2560\n"
    "v_mfma_f32_16x16x32_bf16 v[56:59], a[0:3], a[72:75], v[56:59]\n"
    "buffer_load_dwordx4 v17, s[36:39], s84 offen offset:1024 lds\n"
    "v_mfma_f32_16x16x32_bf16 v[60:63], a[8:11], a[72:75], v[60:63]\n"
    "ds_read_b64_tr_b16 a[44:45], v13 offset:14336\n"
    "v_mfma_f32_16x16x32_bf16 v[60:63], a[12:15], a[76:79], v[60:63]\n"
    "ds_read_b64_tr_b16 a[46:47], v13 offset:14848\n"
    "v_mfma_f32_16x16x32_bf16 v[36:39], a[12:15], a[68:71], v[36:39]\n"
    "buffer_load_dwordx4 v18, s[36:39], s84 offen offset:2048 lds\n"
    "v_mfma_f32_16x16x32_bf16 v[36:39], a[8:11], a[64:67], v[36:39]\n"
    "ds_read_b128 a[80:83], v8 offset:8192\n"
    "v_mfma_f32_16x16x32_bf16 v[40:43], a[16:19], a[64:67], v[40:43]\n"
    "v_mfma_f32_16x16x32_bf16 v[40:43], a[20:23], a[68:71], v[40:43]\n"
    "buffer_load_dwordx4 v19, s[36:39], s84 offen offset:3072 lds\n"
    "v_mfma_f32_16x16x32_bf16 v[64:67], a[20:23], a[76:79], v[64:67]\n"
    "ds_read_b128 a[84:87], v10 offset:8192\n"
    "v_mfma_f32_16x16x32_bf16 v[64:67], a[16:19], a[72:75], v[64:67]\n"
    "s_add_u32 s84, s84, 128\n"
    "s_add_u32 m0, s52, 40960\n"
    "v_mfma_f32_16x16x32_bf16 v[68:71], a[24:27], a[72:75], v[68:71]\n"
    "buffer_load_dwordx4 v16, s[36:39], s85 offen lds\n"
    "s_waitcnt lgkmcnt(6)\n"
    "v_mfma_f32_16x16x32_bf16 v[68:71], a[28:31], a[76:79], v[68:71]\n"
    "ds_read_b128 a[88:91], v8 offset:12288\n"
    "v_mfma_f32_16x16x32_bf16 v[44:47], a[28:31], a[68:71], v[44:47]\n"
    "ds_read_b128 a[92:95], v10 offset:12288\n"
    "v_mfma_f32_16x16x32_bf16 v[44:47], a[24:27], a[64:67], v[44:47]\n"
    "buffer_load_dwordx4 v17, s[36:39], s85 offen offset:1024 lds\n"
    "v_mfma_f32_16x16x32_bf16 v[48:51], a[32:35], a[64:67], v[48:51]\n"
    "ds_read_b128 a[96:99], v9 offset:0\n"
    "v_mfma_f32_16x16x32_bf16 v[48:51], a[36:39], a[68:71], v[48:51]\n"
    "ds_read_b128 a[100:103], v11 offset:0\n"
    "v_mfma_f32_16x16x32_bf16 v[72:75], a[36:39], a[76:79], v[72:75]\n"
    "buffer_load_dwordx4 v18, s[36:39], s85 offen offset:2048 lds\n"
    "v_mfma_f32_16x16x32_bf16 v[72:75], a[32:35], a[72:75], v[72:75]\n"
    "ds_read_b128 a[104:107], v9 offset:4096\n"
    "s_waitcnt lgkmcnt(6)\n"
    "v_mfma_f32_16x16x32_bf16 v[76:79], a[40:43], a[72:75], v[76:79]\n"
    "v_mfma_f32_16x16x32_bf16 v[76:79], a[44:47], a[76:79], v[76:79]\n"
    "buffer_load_dwordx4 v19, s[36:39], s85 offen offset:3072 lds\n"
    "v_mfma_f32_16x16x32_bf16 v[52:55], a[44:47], a[68:71], v[52:55]\n"
    "s_add_u32 s85, s85, 128\n"
    "ds_read_b128 a[108:111], v11 offset:4096\n"
    "v_mfma_f32_16x16x32_bf16 v[52:55], a[40:43], a[64:67], v[52:55]\n"
    "v_lshrrev_b32 v26, 1, v25\n"
    "s_add_u32 s93, s77, 4096\n"
    "v_mfma_f32_16x16x32_bf16 v[100:103], a[40:43], a[80:83], v[100:103]\n"
    "buffer_load_dwordx2 v[0:1], v26, s[44:47], s77 offen sc1\n"
    "s_waitcnt lgkmcnt(6)\n"
    "v_mfma_f32_16x16x32_bf16 v[100:103], a[44:47], a[84:87], v[100:103]\n"
    "ds_read_b128 a[112:115], v9 offset:8192\n"
    "s_waitcnt lgkmcnt(5)\n"
    "v_mfma_f32_16x16x32_bf16 v[124:127], a[44:47], a[92:95], v[124:127]\n"
    "buffer_load_dwordx2 v[2:3], v26, s[44:47], s77 offen offset:2048 sc1\n"
    "v_mfma_f32_16x16x32_bf16 v[124:127], a[40:43], a[88:91], v[124:127]\n"
    "s_add_u32 s94, s77, 8192\n"
    "ds_read_b128 a[116:119], v11 offset:8192\n"
    "v_mfma_f32_16x16x32_bf16 v[120:123], a[32:35], a[88:91], v[120:123]\n"
    "buffer_load_dwordx2 v[4:5], v26, s[44:47], s93 offen sc1\n"
    "v_mfma_f32_16x16x32_bf16 v[120:123], a[36:39], a[92:95], v[120:123]\n"
    "ds_read_b128 a[120:123], v9 offset:12288\n"
    "v_mfma_f32_16x16x32_bf16 v[96:99], a[36:39], a[84:87], v[96:99]\n"
    "ds_read_b128 a[124:127], v11 offset:12288\n"
    "v_mfma_f32_16x16x32_bf16 v[96:99], a[32:35], a[80:83], v[96:99]\n"
    "buffer_load_dwordx2 v[6:7], v26, s[44:47], s93 offen offset:2048 sc1\n"
    "v_mfma_f32_16x16x32_bf16 v[92:95], a[24:27], a[80:83], v[92:95]\n"
    "s_add_u32 s93, s77, 12288\n"
    "v_mfma_f32_16x16x32_bf16 v[92:95], a[28:31], a[84:87], v[92:95]\n"
    "buffer_load_dwordx2 v[16:17], v26, s[44:47], s94 offen sc1\n"
    "v_mfma_f32_16x16x32_bf16 v[116:119], a[28:31], a[92:95], v[116:119]\n"
    "v_mfma_f32_16x16x32_bf16 v[116:119], a[24:27], a[88:91], v[116:119]\n"
    "v_mfma_f32_16x16x32_bf16 v[112:115], a[16:19], a[88:91], v[112:115]\n"
    "buffer_load_dwordx2 v[18:19], v26, s[44:47], s94 offen offset:2048 sc1\n"
    "v_mfma_f32_16x16x32_bf16 v[112:115], a[20:23], a[92:95], v[112:115]\n"
    "s_waitcnt lgkmcnt(0)\n"
    "s_barrier\n"
    "v_mfma_f32_16x16x32_bf16 v[88:91], a[20:23], a[84:87], v[88:91]\n"
    "buffer_load_dwordx2 v[28:29], v26, s[44:47], s93 offen sc1\n"
    "v_mfma_f32_16x16x32_bf16 v[88:91], a[16:19], a[80:83], v[88:91]\n"
    "v_mfma_f32_16x16x32_bf16 v[84:87], a[8:11], a[80:83], v[84:87]\n"
    "buffer_load_dwordx2 v[30:31], v26, s[44:47], s93 offen offset:2048 sc1\n"
    "v_mfma_f32_16x16x32_bf16 v[84:87], a[12:15], a[84:87], v[84:87]\n"
    "v_mfma_f32_16x16x32_bf16 v[108:111], a[12:15], a[92:95], v[108:111]\n"
    "v_mfma_f32_16x16x32_bf16 v[108:111], a[8:11], a[88:91], v[108:111]\n"
    "buffer_load_dwordx4 v[20:23], v25, s[44:47], s79 offen sc1\n"
    "v_mfma_f32_16x16x32_bf16 v[104:107], a[0:3], a[88:91], v[104:107]\n"
    "s_lshr_b32 s94, s52, 5\n"
    "v_accvgpr_read_b32 v27, a169\n"
    "s_add_u32 m0, s94, 136192\n"
    "v_mfma_f32_16x16x32_bf16 v[104:107], a[4:7], a[92:95], v[104:107]\n"
    "global_load_lds_dword v27, s[74:75]\n"
    "v_mfma_f32_16x16x32_bf16 v[80:83], a[4:7], a[84:87], v[80:83]\n"
    "s_mov_b64 s[48:49], s[28:29]\n"
    "v_accvgpr_read_b32 v27, a198\n"
    "v_mfma_f32_16x16x32_bf16 v[80:83], a[0:3], a[80:83], v[80:83]\n"
    "s_and_b32 s91, s35, 4\n"
    "v_mfma_f32_16x16x32_bf16 v[148:151], a[40:43], a[96:99], v[148:151]\n"
    "v_readlane_b32 s84, v27, 0\n"
    "v_readlane_b32 s85, v27, 1\n"
    "v_mfma_f32_16x16x32_bf16 v[148:151], a[44:47], a[100:103], v[148:151]\n"
    "v_readlane_b32 s87, v27, 2\n"
    "v_mfma_f32_16x16x32_bf16 v[172:175], a[44:47], a[108:111], v[172:175]\n"
    "v_readlane_b32 s89, v27, 3\n"
    "s_lshl_b32 s91, s91, 6\n"
    "v_mfma_f32_16x16x32_bf16 v[172:175], a[40:43], a[104:107], v[172:175]\n"
    "s_add_u32 s91, s91, 896\n"
    "v_mfma_f32_16x16x32_bf16 v[168:171], a[32:35], a[104:107], v[168:171]\n"
    "v_accvgpr_read_b32 v27, a165\n"
    "v_mfma_f32_16x16x32_bf16 v[168:171], a[36:39], a[108:111], v[168:171]\n"
    "v_mfma_f32_16x16x32_bf16 v[144:147], a[36:39], a[100:103], v[144:147]\n"
    "v_mfma_f32_16x16x32_bf16 v[144:147], a[32:35], a[96:99], v[144:147]\n"
    "v_mfma_f32_16x16x32_bf16 v[140:143], a[24:27], a[96:99], v[140:143]\n"
    "v_mfma_f32_16x16x32_bf16 v[140:143], a[28:31], a[100:103], v[140:143]\n"
    "v_mfma_f32_16x16x32_bf16 v[164:167], a[28:31], a[108:111], v[164:167]\n"
    "v_mfma_f32_16x16x32_bf16 v[164:167], a[24:27], a[104:107], v[164:167]\n"
    "v_mfma_f32_16x16x32_bf16 v[160:163], a[16:19], a[104:107], v[160:163]\n"
    "v_mfma_f32_16x16x32_bf16 v[160:163], a[20:23], a[108:111], v[160:163]\n"
    "v_mfma_f32_16x16x32_bf16 v[136:139], a[20:23], a[100:103], v[136:139]\n"
    "v_mfma_f32_16x16x32_bf16 v[136:139], a[16:19], a[96:99], v[136:139]\n"
    "v_mfma_f32_16x16x32_bf16 v[132:135], a[8:11], a[96:99], v[132:135]\n"
    "v_mfma_f32_16x16x32_bf16 v[132:135], a[12:15], a[100:103], v[132:135]\n"
    "v_mfma_f32_16x16x32_bf16 v[156:159], a[12:15], a[108:111], v[156:159]\n"
    "v_mfma_f32_16x16x32_bf16 v[156:159], a[8:11], a[104:107], v[156:159]\n"
    "v_mfma_f32_16x16x32_bf16 v[152:155], a[0:3], a[104:107], v[152:155]\n"
    "v_mfma_f32_16x16x32_bf16 v[152:155], a[4:7], a[108:111], v[152:155]\n"
    "v_mfma_f32_16x16x32_bf16 v[128:131], a[4:7], a[100:103], v[128:131]\n"
    "v_mfma_f32_16x16x32_bf16 v[128:131], a[0:3], a[96:99], v[128:131]\n"
    "s_waitcnt vmcnt(10)\n"
    "s_barrier\n"
    "v_mfma_f32_16x16x32_bf16 v[176:179], a[0:3], a[112:115], v[176:179]\n"
    "ds_read_b128 a[64:67], v8 offset:36864\n"
    "v_mfma_f32_16x16x32_bf16 v[176:179], a[4:7], a[116:119], v[176:179]\n"
    "ds_read_b128 a[68:71], v10 offset:36864\n"
    "v_mfma_f32_16x16x32_bf16 v[200:203], a[4:7], a[124:127], v[200:203]\n"
    "ds_read_b128 a[72:75], v8 offset:40960\n"
    "v_mfma_f32_16x16x32_bf16 v[200:203], a[0:3], a[120:123], v[200:203]\n"
    "ds_read_b128 a[76:79], v10 offset:40960\n"
    "v_mfma_f32_16x16x32_bf16 v[204:207], a[8:11], a[120:123], v[204:207]\n"
    "ds_read_b64_tr_b16 a[0:1], v14 offset:24576\n"
    "v_mfma_f32_16x16x32_bf16 v[204:207], a[12:15], a[124:127], v[204:207]\n"
    "ds_read_b64_tr_b16 a[2:3], v14 offset:25088\n"
    "v_mfma_f32_16x16x32_bf16 v[180:183], a[12:15], a[116:119], v[180:183]\n"
    "ds_read_b64_tr_b16 a[4:5], v14 offset:36864\n"
    "v_mfma_f32_16x16x32_bf16 v[180:183], a[8:11], a[112:115], v[180:183]\n"
    "ds_read_b64_tr_b16 a[6:7], v14 offset:37376\n"
    "v_mfma_f32_16x16x32_bf16 v[184:187], a[16:19], a[112:115], v[184:187]\n"
    "ds_read_b64_tr_b16 a[8:9], v15 offset:24576\n"
    "v_mfma_f32_16x16x32_bf16 v[184:187], a[20:23], a[116:119], v[184:187]\n"
    "ds_read_b64_tr_b16 a[10:11], v15 offset:25088\n"
    "v_mfma_f32_16x16x32_bf16 v[208:211], a[20:23], a[124:127], v[208:211]\n"
    "ds_read_b64_tr_b16 a[12:13], v15 offset:36864\n"
    "v_mfma_f32_16x16x32_bf16 v[208:211], a[16:19], a[120:123], v[208:211]\n"
    "ds_read_b64_tr_b16 a[14:15], v15 offset:37376\n"
    "v_mfma_f32_16x16x32_bf16 v[212:215], a[24:27], a[120:123], v[212:215]\n"
    "ds_read_b64_tr_b16 a[16:17], v14 offset:25600\n"
    "v_mfma_f32_16x16x32_bf16 v[212:215], a[28:31], a[124:127], v[212:215]\n"
    "ds_read_b64_tr_b16 a[18:19], v14 offset:26112\n"
    "v_mfma_f32_16x16x32_bf16 v[188:191], a[28:31], a[116:119], v[188:191]\n"
    "ds_read_b64_tr_b16 a[20:21], v14 offset:37888\n"
    "v_mfma_f32_16x16x32_bf16 v[188:191], a[24:27], a[112:115], v[188:191]\n"
    "ds_read_b64_tr_b16 a[22:23], v14 offset:38400\n"
    "v_mfma_f32_16x16x32_bf16 v[192:195], a[32:35], a[112:115], v[192:195]\n"
    "ds_read_b64_tr_b16 a[24:25], v15 offset:25600\n"
    "v_mfma_f32_16x16x32_bf16 v[192:195], a[36:39], a[116:119], v[192:195]\n"
    "ds_read_b64_tr_b16 a[26:27], v15 offset:26112\n"
    "v_mfma_f32_16x16x32_bf16 v[216:219], a[36:39], a[124:127], v[216:219]\n"
    "ds_read_b64_tr_b16 a[28:29], v15 offset:37888\n"
    "v_mfma_f32_16x16x32_bf16 v[216:219], a[32:35], a[120:123], v[216:219]\n"
    "ds_read_b64_tr_b16 a[30:31], v15 offset:38400\n"
    "v_mfma_f32_16x16x32_bf16 v[220:223], a[40:43], a[120:123], v[220:223]\n"
    "ds_read_b64_tr_b16 a[32:33], v12 offset:26624\n"
    "v_mfma_f32_16x16x32_bf16 v[220:223], a[44:47], a[124:127], v[220:223]\n"
    "ds_read_b64_tr_b16 a[34:35], v12 offset:27136\n"
    "v_mfma_f32_16x16x32_bf16 v[196:199], a[44:47], a[116:119], v[196:199]\n"
    "ds_read_b64_tr_b16 a[36:37], v12 offset:38912\n"
    "v_mfma_f32_16x16x32_bf16 v[196:199], a[40:43], a[112:115], v[196:199]\n"
    "ds_read_b64_tr_b16 a[38:39], v12 offset:39424\n"
    "s_waitcnt lgkmcnt(6)\n"
    "v_mfma_f32_16x16x32_bf16 v[32:35], a[0:3], a[64:67], v[32:35]\n"
    "ds_read_b64_tr_b16 a[40:41], v13 offset:26624\n"
    "v_mfma_f32_16x16x32_bf16 v[32:35], a[4:7], a[68:71], v[32:35]\n"
    "ds_read_b64_tr_b16 a[42:43], v13 offset:27136\n"
    "s_waitcnt vmcnt(1)\n"
    "v_mfma_f32_16x16x32_bf16 v[56:59], a[4:7], a[76:79], v[56:59]\n"
    "s_add_u32 s93, s92, 1\n"
    "v_xor_b32 v21, s93, v21\n"
    "v_mfma_f32_16x16x32_bf16 v[56:59], a[0:3], a[72:75], v[56:59]\n"
    "ds_read_b64_tr_b16 a[44:45], v13 offset:38912\n"
    "v_xor_b32 v23, s93, v23\n"
    "v_mfma_f32_16x16x32_bf16 v[60:63], a[8:11], a[72:75], v[60:63]\n"
    "v_xor_b32 v1, s93, v1\n"
    "v_mfma_f32_16x16x32_bf16 v[60:63], a[12:15], a[76:79], v[60:63]\n"
    "ds_read_b64_tr_b16 a[46:47], v13 offset:39424\n"
    "v_xor_b32 v3, s93, v3\n"
    "v_mfma_f32_16x16x32_bf16 v[36:39], a[12:15], a[68:71], v[36:39]\n"
    "v_xor_b32 v5, s93, v5\n"
    "v_xor_b32 v7, s93, v7\n"
    "v_mfma_f32_16x16x32_bf16 v[36:39], a[8:11], a[64:67], v[36:39]\n"
    "ds_read_b128 a[80:83], v8 offset:45056\n"
    "v_xor_b32 v17, s93, v17\n"
    "v_mfma_f32_16x16x32_bf16 v[40:43], a[16:19], a[64:67], v[40:43]\n"
    "v_xor_b32 v19, s93, v19\n"
    "v_mfma_f32_16x16x32_bf16 v[40:43], a[20:23], a[68:71], v[40:43]\n"
    "ds_read_b128 a[84:87], v10 offset:45056\n"
    "v_xor_b32 v29, s93, v29\n"
    "v_mfma_f32_16x16x32_bf16 v[64:67], a[20:23], a[76:79], v[64:67]\n"
    "v_xor_b32 v31, s93, v31\n"
    "v_or3_b32 v21, v21, v23, v1\n"
    "v_mfma_f32_16x16x32_bf16 v[64:67], a[16:19], a[72:75], v[64:67]\n"
    "ds_read_b128 a[88:91], v8 offset:49152\n"
    "v_or3_b32 v3, v3, v5, v7\n"
    "v_mfma_f32_16x16x32_bf16 v[68:71], a[24:27], a[72:75], v[68:71]\n"
    "v_or3_b32 v17, v17, v19, v29\n"
    "s_waitcnt lgkmcnt(6)\n"
    "v_mfma_f32_16x16x32_bf16 v[68:71], a[28:31], a[76:79], v[68:71]\n"
    "ds_read_b128 a[92:95], v10 offset:49152\n"
    "v_or3_b32 v21, v21, v3, v17\n"
    "v_mfma_f32_16x16x32_bf16 v[44:47], a[28:31], a[68:71], v[44:47]\n"
    "ds_read_b128 a[96:99], v9 offset:36864\n"
    "v_or_b32 v21, v21, v31\n"
    "v_mfma_f32_16x16x32_bf16 v[44:47], a[24:27], a[64:67], v[44:47]\n"
    "v_cmp_ne_u32 vcc, 0, v21\n"
    "s_nop 0\n"
    "s_cmp_eq_u64 vcc, 0\n"
    "s_cbranch_scc0 .Lk2_qslow\n"
    ".Lk2_qok:\n"
    "v_mfma_f32_16x16x32_bf16 v[48:51], a[32:35], a[64:67], v[48:51]\n"
    "ds_read_b128 a[100:103], v11 offset:36864\n"
    "v_add_f32 v0, v0, v2\n"
    "v_mfma_f32_16x16x32_bf16 v[48:51], a[36:39], a[68:71], v[48:51]\n"
    "v_add_f32 v4, v4, v6\n"
    "v_mfma_f32_16x16x32_bf16 v[72:75], a[36:39], a[76:79], v[72:75]\n"
    "ds_read_b128 a[104:107], v9 offset:40960\n"
    "v_add_f32 v16, v16, v18\n"
    "v_mfma_f32_16x16x32_bf16 v[72:75], a[32:35], a[72:75], v[72:75]\n"
    "v_add_f32 v28, v28, v30\n"
    "v_add_f32 v0, v0, v4\n"
    "s_waitcnt lgkmcnt(6)\n"
    "v_mfma_f32_16x16x32_bf16 v[76:79], a[40:43], a[72:75], v[76:79]\n"
    "ds_read_b128 a[108:111], v11 offset:40960\n"
    "v_add_f32 v16, v16, v28\n"
    "v_mfma_f32_16x16x32_bf16 v[76:79], a[44:47], a[76:79], v[76:79]\n"
    "v_add_f32 v0, v0, v16\n"
    "v_mfma_f32_16x16x32_bf16 v[52:55], a[44:47], a[68:71], v[52:55]\n"
    "v_add_f32 v23, 0x3727c5ac, v22\n"
    "v_rsq_f32 v2, v23\n"
    "v_mfma_f32_16x16x32_bf16 v[52:55], a[40:43], a[64:67], v[52:55]\n"
    "ds_read_b128 a[112:115], v9 offset:45056\n"
    "s_lshr_b32 s93, s52, 4\n"
    "v_mfma_f32_16x16x32_bf16 v[100:103], a[40:43], a[80:83], v[100:103]\n"
    "v_mul_f32 v3, v2, v20\n"
    "s_waitcnt lgkmcnt(6)\n"
    "v_mfma_f32_16x16x32_bf16 v[100:103], a[44:47], a[84:87], v[100:103]\n"
    "ds_read_b128 a[116:119], v11 offset:45056\n"
    "v_add_u32 v26, s93, v26\n"
    "s_waitcnt lgkmcnt(6)\n"
    "v_mfma_f32_16x16x32_bf16 v[124:127], a[44:47], a[92:95], v[124:127]\n"
    "v_xor_b32 v3, 0x80000000, v3\n"
    "v_lshrrev_b32 v4, 1, v26\n"
    "v_mfma_f32_16x16x32_bf16 v[124:127], a[40:43], a[88:91], v[124:127]\n"
    "ds_read_b128 a[120:123], v9 offset:49152\n"
    "v_add_u32 v26, 131072, v26\n"
    "v_mfma_f32_16x16x32_bf16 v[120:123], a[32:35], a[88:91], v[120:123]\n"
    "v_add_u32 v4, 133120, v4\n"
    "v_mfma_f32_16x16x32_bf16 v[120:123], a[36:39], a[92:95], v[120:123]\n"
    "ds_read_b128 a[124:127], v11 offset:49152\n"
    "v_mfma_f32_16x16x32_bf16 v[96:99], a[36:39], a[84:87], v[96:99]\n"
    "ds_write_b64 v26, v[2:3]\n"
    "v_mfma_f32_16x16x32_bf16 v[96:99], a[32:35], a[80:83], v[96:99]\n"
    "ds_write_b32 v4, v0\n"
    "v_mfma_f32_16x16x32_bf16 v[92:95], a[24:27], a[80:83], v[92:95]\n"
    "v_mfma_f32_16x16x32_bf16 v[92:95], a[28:31], a[84:87], v[92:95]\n"
    "v_mfma_f32_16x16x32_bf16 v[116:119], a[28:31], a[92:95], v[116:119]\n"
    "v_mfma_f32_16x16x32_bf16 v[116:119], a[24:27], a[88:91], v[116:119]\n"
    "v_mfma_f32_16x16x32_bf16 v[112:115], a[16:19], a[88:91], v[112:115]\n"
    "v_mfma_f32_16x16x32_bf16 v[112:115], a[20:23], a[92:95], v[112:115]\n"
    "s_waitcnt lgkmcnt(0)\n"
    "s_barrier\n"
    "v_mfma_f32_16x16x32_bf16 v[88:91], a[20:23], a[84:87], v[88:91]\n"
    "v_accvgpr_read_b32 v30, a171\n"
    "v_accvgpr_read_b32 v31, a173\n"
    "v_mfma_f32_16x16x32_bf16 v[88:91], a[16:19], a[80:83], v[88:91]\n"
    "v_accvgpr_read_b32 v28, a174\n"
    "v_mfma_f32_16x16x32_bf16 v[84:87], a[8:11], a[80:83], v[84:87]\n"
    "v_accvgpr_read_b32 v29, a175\n"
    "v_mfma_f32_16x16x32_bf16 v[84:87], a[12:15], a[84:87], v[84:87]\n"
    "ds_read_b64 v[224:225], v30 offset:0\n"
    "v_mfma_f32_16x16x32_bf16 v[108:111], a[12:15], a[92:95], v[108:111]\n"
    "ds_read_b64 v[226:227], v30 offset:256\n"
    "v_mfma_f32_16x16x32_bf16 v[108:111], a[8:11], a[88:91], v[108:111]\n"
    "ds_read_b64 v[228:229], v30 offset:512\n"
    "v_mfma_f32_16x16x32_bf16 v[104:107], a[0:3], a[88:91], v[104:107]\n"
    "ds_read_b64 v[230:231], v30 offset:768\n"
    "v_mfma_f32_16x16x32_bf16 v[104:107], a[4:7], a[92:95], v[104:107]\n"
    "ds_read_b64 v[232:233], v30 offset:1024\n"
    "v_mfma_f32_16x16x32_bf16 v[80:83], a[4:7], a[84:87], v[80:83]\n"
    "ds_read_b64 v[234:235], v30 offset:1280\n"
    "v_mfma_f32_16x16x32_bf16 v[80:83], a[0:3], a[80:83], v[80:83]\n"
    "ds_read_b64 v[236:237], v30 offset:1536\n"
    "v_mfma_f32_16x16x32_bf16 v[148:151], a[40:43], a[96:99], v[148:151]\n"
    "ds_read_b64 v[238:239], v30 offset:1792\n"
    "v_mfma_f32_16x16x32_bf16 v[148:151], a[44:47], a[100:103], v[148:151]\n"
    "ds_read_b128 v[0:3], v31 offset:256\n"
    "v_mfma_f32_16x16x32_bf16 v[172:175], a[44:47], a[108:111], v[172:175]\n"
    "ds_read_b128 v[4:7], v31 offset:320\n"
    "v_mfma_f32_16x16x32_bf16 v[172:175], a[40:43], a[104:107], v[172:175]\n"
    "ds_read_b128 v[8:11], v28\n"
    "v_mfma_f32_16x16x32_bf16 v[168:171], a[32:35], a[104:107], v[168:171]\n"
    "ds_read_b128 v[12:15], v29\n"
    "v_mfma_f32_16x16x32_bf16 v[168:171], a[36:39], a[108:111], v[168:171]\n"
    "ds_read_b128 v[16:19], v31\n"
    "v_mfma_f32_16x16x32_bf16 v[144:147], a[36:39], a[100:103], v[144:147]\n"
    "ds_read_b128 v[20:23], v31 offset:64\n"
    "v_mfma_f32_16x16x32_bf16 v[144:147], a[32:35], a[96:99], v[144:147]\n"
    "v_accvgpr_read_b32 v28, a179\n"
    "v_mfma_f32_16x16x32_bf16 v[140:143], a[24:27], a[96:99], v[140:143]\n"
    "v_accvgpr_read_b32 v29, a180\n"
    "v_accvgpr_read_b32 v244, a181\n"
    "v_mfma_f32_16x16x32_bf16 v[140:143], a[28:31], a[100:103], v[140:143]\n"
    "v_accvgpr_read_b32 v245, a182\n"
    "v_mfma_f32_16x16x32_bf16 v[164:167], a[28:31], a[108:111], v[164:167]\n"
    "v_accvgpr_read_b32 v246, a183\n"
    "v_mfma_f32_16x16x32_bf16 v[164:167], a[24:27], a[104:107], v[164:167]\n"
    "v_accvgpr_read_b32 v247, a167\n"
    "v_mfma_f32_16x16x32_bf16 v[160:163], a[16:19], a[104:107], v[160:163]\n"
    "v_accvgpr_read_b32 v249, a165\n"
    "v_mfma_f32_16x16x32_bf16 v[160:163], a[20:23], a[108:111], v[160:163]\n"
    "v_accvgpr_read_b32 v253, a166\n"
    "v_mfma_f32_16x16x32_bf16 v[136:139], a[20:23], a[100:103], v[136:139]\n"
    "v_mov_b32 v250, v25\n"
    "v_mfma_f32_16x16x32_bf16 v[136:139], a[16:19], a[96:99], v[136:139]\n"
    "v_mov_b32 v30, 0x3f803f80\n"
    "v_mfma_f32_16x16x32_bf16 v[132:135], a[8:11], a[96:99], v[132:135]\n"
    "v_accvgpr_write_b32 a128, v30\n"
    "v_mfma_f32_16x16x32_bf16 v[132:135], a[12:15], a[100:103], v[132:135]\n"
    "v_accvgpr_write_b32 a129, v30\n"
    "v_mfma_f32_16x16x32_bf16 v[156:159], a[12:15], a[108:111], v[156:159]\n"
    "v_accvgpr_write_b32 a130, v30\n"
    "v_mfma_f32_16x16x32_bf16 v[156:159], a[8:11], a[104:107], v[156:159]\n"
    "v_accvgpr_write_b32 a131, v30\n"
    "v_mfma_f32_16x16x32_bf16 v[152:155], a[0:3], a[104:107], v[152:155]\n"
    "v_mov_b32 v30, 0xbf80bf80\n"
    "v_mfma_f32_16x16x32_bf16 v[152:155], a[4:7], a[108:111], v[152:155]\n"
    "v_accvgpr_write_b32 a132, v30\n"
    "v_mfma_f32_16x16x32_bf16 v[128:131], a[4:7], a[100:103], v[128:131]\n"
    "v_accvgpr_write_b32 a133, v30\n"
    "v_mfma_f32_16x16x32_bf16 v[128:131], a[0:3], a[96:99], v[128:131]\n"
    "v_accvgpr_write_b32 a134, v30\n"
    "v_mfma_f32_16x16x32_bf16 v[176:179], a[0:3], a[112:115], v[176:179]\n"
    "v_accvgpr_write_b32 a135, v30\n"
    "v_mfma_f32_16x16x32_bf16 v[176:179], a[4:7], a[116:119], v[176:179]\n"
    "v_mov_b32 v241, 0\n"
    "v_mfma_f32_16x16x32_bf16 v[200:203], a[4:7], a[124:127], v[200:203]\n"
    "v_mov_b32 v242, 0\n"
    "v_mfma_f32_16x16x32_bf16 v[200:203], a[0:3], a[120:123], v[200:203]\n"
    "v_mov_b32 v243, 0\n"
    "s_lshl_b32 s90, s52, 1\n"
    "v_mfma_f32_16x16x32_bf16 v[204:207], a[8:11], a[120:123], v[204:207]\n"
    "s_add_u32 s34, s63, s90\n"
    "v_mfma_f32_16x16x32_bf16 v[204:207], a[12:15], a[124:127], v[204:207]\n"
    "s_sub_u32 s34, s34, 0x10000\n"
    "v_mfma_f32_16x16x32_bf16 v[180:183], a[12:15], a[116:119], v[180:183]\n"
    "s_add_u32 s90, s90, 0x10000\n"
    "v_mfma_f32_16x16x32_bf16 v[180:183], a[8:11], a[112:115], v[180:183]\n"
    "s_cmp_eq_u32 s57, 0\n"
    "s_cselect_b32 s34, 1073741824, s34\n"
    "v_mfma_f32_16x16x32_bf16 v[184:187], a[16:19], a[112:115], v[184:187]\n"
    "s_mov_b32 s96, 0x3e8293ee\n"
    "v_mfma_f32_16x16x32_bf16 v[184:187], a[20:23], a[116:119], v[184:187]\n"
    "s_add_u32 s28, s60, s62\n"
    "v_mfma_f32_16x16x32_bf16 v[208:211], a[20:23], a[124:127], v[208:211]\n"
    "s_add_u32 s29, s61, s62\n"
    "v_mfma_f32_16x16x32_bf16 v[208:211], a[16:19], a[120:123], v[208:211]\n"
    "s_mov_b32 s30, 1073741824\n"
    "v_mfma_f32_16x16x32_bf16 v[212:215], a[24:27], a[120:123], v[212:215]\n"
    "s_cmp_eq_u32 s57, 7\n"
    "s_cselect_b32 s86, 1073741824, s63\n"
    "v_mfma_f32_16x16x32_bf16 v[212:215], a[28:31], a[124:127], v[212:215]\n"
    "s_waitcnt lgkmcnt(0)\n"
    "v_mfma_f32_16x16x32_bf16 v[188:191], a[28:31], a[116:119], v[188:191]\n"
    "v_mul_f32 v16, s96, v16\n"
    "v_mfma_f32_16x16x32_bf16 v[188:191], a[24:27], a[112:115], v[188:191]\n"
    "v_mul_f32 v17, s96, v17\n"
    "v_mfma_f32_16x16x32_bf16 v[192:195], a[32:35], a[112:115], v[192:195]\n"
    "v_mul_f32 v18, s96, v18\n"
    "v_mfma_f32_16x16x32_bf16 v[192:195], a[36:39], a[116:119], v[192:195]\n"
    "v_mul_f32 v19, s96, v19\n"
    "v_mfma_f32_16x16x32_bf16 v[216:219], a[36:39], a[124:127], v[216:219]\n"
    "v_mul_f32 v20, s96, v20\n"
    "v_mfma_f32_16x16x32_bf16 v[216:219], a[32:35], a[120:123], v[216:219]\n"
    "v_mul_f32 v21, s96, v21\n"
    "v_mfma_f32_16x16x32_bf16 v[220:223], a[40:43], a[120:123], v[220:223]\n"
    "v_mul_f32 v22, s96, v22\n"
    "v_mfma_f32_16x16x32_bf16 v[220:223], a[44:47], a[124:127], v[220:223]\n"
    "v_mul_f32 v23, s96, v23\n"
    "v_mfma_f32_16x16x32_bf16 v[196:199], a[44:47], a[116:119], v[196:199]\n"
    "v_mfma_f32_16x16x32_bf16 v[196:199], a[40:43], a[112:115], v[196:199]\n"
    "v_pk_mul_f32 v[24:25], v[8:9], v[224:225] op_sel:[0,1] op_sel_hi:[1,1]\n"
    "v_pk_mul_f32 v[26:27], v[10:11], v[224:225] op_sel:[0,1] op_sel_hi:[1,1]\n"
    "v_pk_fma_f32 v[48:49], v[48:49], v[224:225], v[24:25] op_sel_hi:[1,0,1]\n"
    "v_pk_fma_f32 v[50:51], v[50:51], v[224:225], v[26:27] op_sel_hi:[1,0,1]\n"
    "v_pk_mul_f32 v[24:25], v[12:13], v[224:225] op_sel:[0,1] op_sel_hi:[1,1]\n"
    "v_pk_mul_f32 v[26:27], v[14:15], v[224:225] op_sel:[0,1] op_sel_hi:[1,1]\n"
    "v_pk_fma_f32 v[52:53], v[52:53], v[224:225], v[24:25] op_sel_hi:[1,0,1]\n"
    "v_pk_fma_f32 v[54:55], v[54:55], v[224:225], v[26:27] op_sel_hi:[1,0,1]\n"
    "v_cvt_pk_bf16_f32 v48, v48, v49\n"
    "v_cvt_pk_bf16_f32 v49, v50, v51\n"
    "v_cvt_pk_bf16_f32 v50, v52, v53\n"
    "v_cvt_pk_bf16_f32 v51, v54, v55\n"
    "ds_write_b128 v29, v[48:51] offset:0\n"
    "s_add_u32 s99, s86, 0\n"
    "buffer_store_dwordx4 v[48:51], v29, s[44:47], s99 offen sc1\n"
    "v_pk_mul_f32 v[24:25], v[0:1], v[224:225] op_sel:[0,1] op_sel_hi:[1,1]\n"
    "v_pk_mul_f32 v[26:27], v[2:3], v[224:225] op_sel:[0,1] op_sel_hi:[1,1]\n"
    "v_pk_fma_f32 v[40:41], v[40:41], v[224:225], v[24:25] op_sel_hi:[1,0,1]\n"
    "v_pk_fma_f32 v[42:43], v[42:43], v[224:225], v[26:27] op_sel_hi:[1,0,1]\n"
    "v_pk_mul_f32 v[24:25], v[4:5], v[224:225] op_sel:[0,1] op_sel_hi:[1,1]\n"
    "v_pk_mul_f32 v[26:27], v[6:7], v[224:225] op_sel:[0,1] op_sel_hi:[1,1]\n"
    "v_pk_fma_f32 v[44:45], v[44:45], v[224:225], v[24:25] op_sel_hi:[1,0,1]\n"
    "v_pk_fma_f32 v[46:47], v[46:47], v[224:225], v[26:27] op_sel_hi:[1,0,1]\n"
    "v_pk_mul_f32 v[24:25], v[16:17], v[224:225] op_sel:[0,1] op_sel_hi:[1,1]\n"
    "v_pk_mul_f32 v[26:27], v[18:19], v[224:225] op_sel:[0,1] op_sel_hi:[1,1]\n"
    "v_mul_f32 v224, s96, v224\n"
    "v_pk_fma_f32 v[32:33], v[32:33], v[224:225], v[24:25] op_sel_hi:[1,0,1]\n"
    "v_pk_fma_f32 v[34:35], v[34:35], v[224:225], v[26:27] op_sel_hi:[1,0,1]\n"
    "v_pk_mul_f32 v[24:25], v[20:21], v[224:225] op_sel:[0,1] op_sel_hi:[1,1]\n"
    "v_pk_mul_f32 v[26:27], v[22:23], v[224:225] op_sel:[0,1] op_sel_hi:[1,1]\n"
    "v_pk_fma_f32 v[36:37], v[36:37], v[224:225], v[24:25] op_sel_hi:[1,0,1]\n"
    "v_pk_fma_f32 v[38:39], v[38:39], v[224:225], v[26:27] op_sel_hi:[1,0,1]\n"
    "v_pk_mul_f32 v[24:25], v[32:33], v[40:41]\n"
    "v_pk_fma_f32 v[24:25], v[34:35], v[42:43], v[24:25]\n"
    "v_pk_fma_f32 v[24:25], v[36:37], v[44:45], v[24:25]\n"
    "v_pk_fma_f32 v[24:25], v[38:39], v[46:47], v[24:25]\n"
    "v_cvt_pk_bf16_f32 v240, v24, v25\n"
    "v_cvt_pk_bf16_f32 v40, v40, v41\n"
    "v_cvt_pk_bf16_f32 v41, v42, v43\n"
    "v_cvt_pk_bf16_f32 v42, v44, v45\n"
    "v_cvt_pk_bf16_f32 v43, v46, v47\n"
    "v_mfma_f32_16x16x32_bf16 v[52:55], a[132:135], v[240:243], 0\n"
    "ds_write_b128 v28, v[40:43] offset:0\n"
    "buffer_store_dwordx4 v[40:43], v28, s[44:47], s99 offen sc1\n"
    "v_cvt_pk_bf16_f32 v32, v32, v33\n"
    "v_cvt_pk_bf16_f32 v33, v34, v35\n"
    "v_cvt_pk_bf16_f32 v34, v36, v37\n"
    "v_cvt_pk_bf16_f32 v35, v38, v39\n"
    "v_pk_mul_f32 v[24:25], v[8:9], v[226:227] op_sel:[0,1] op_sel_hi:[1,1]\n"
    "v_pk_mul_f32 v[26:27], v[10:11], v[226:227] op_sel:[0,1] op_sel_hi:[1,1]\n"
    "v_pk_fma_f32 v[72:73], v[72:73], v[226:227], v[24:25] op_sel_hi:[1,0,1]\n"
    "v_pk_fma_f32 v[74:75], v[74:75], v[226:227], v[26:27] op_sel_hi:[1,0,1]\n"
    "v_pk_mul_f32 v[24:25], v[12:13], v[226:227] op_sel:[0,1] op_sel_hi:[1,1]\n"
    "v_pk_mul_f32 v[26:27], v[14:15], v[226:227] op_sel:[0,1] op_sel_hi:[1,1]\n"
    "v_pk_fma_f32 v[76:77], v[76:77], v[226:227], v[24:25] op_sel_hi:[1,0,1]\n"
    "v_pk_fma_f32 v[78:79], v[78:79], v[226:227], v[26:27] op_sel_hi:[1,0,1]\n"
    "v_cvt_pk_bf16_f32 v72, v72, v73\n"
    "v_cvt_pk_bf16_f32 v73, v74, v75\n"
    "v_cvt_pk_bf16_f32 v74, v76, v77\n"
    "v_cvt_pk_bf16_f32 v75, v78, v79\n"
    "ds_write_b128 v29, v[72:75] offset:2048\n"
    "buffer_store_dwordx4 v[72:75], v29, s[44:47], s99 offen offset:2048 sc1\n"
    "v_pk_mul_f32 v[24:25], v[0:1], v[226:227] op_sel:[0,1] op_sel_hi:[1,1]\n"
    "v_pk_mul_f32 v[26:27], v[2:3], v[226:227] op_sel:[0,1] op_sel_hi:[1,1]\n"
    "v_pk_fma_f32 v[64:65], v[64:65], v[226:227], v[24:25] op_sel_hi:[1,0,1]\n"
    "v_pk_fma_f32 v[66:67], v[66:67], v[226:227], v[26:27] op_sel_hi:[1,0,1]\n"
    "v_pk_mul_f32 v[24:25], v[4:5], v[226:227] op_sel:[0,1] op_sel_hi:[1,1]\n"
    "v_pk_mul_f32 v[26:27], v[6:7], v[226:227] op_sel:[0,1] op_sel_hi:[1,1]\n"
    "v_pk_fma_f32 v[68:69], v[68:69], v[226:227], v[24:25] op_sel_hi:[1,0,1]\n"
    "v_pk_fma_f32 v[70:71], v[70:71], v[226:227], v[26:27] op_sel_hi:[1,0,1]\n"
    "v_pk_mul_f32 v[24:25], v[16:17], v[226:227] op_sel:[0,1] op_sel_hi:[1,1]\n"
    "v_pk_mul_f32 v[26:27], v[18:19], v[226:227] op_sel:[0,1] op_sel_hi:[1,1]\n"
    "v_mul_f32 v226, s96, v226\n"
    "v_pk_fma_f32 v[56:57], v[56:57], v[226:227], v[24:25] op_sel_hi:[1,0,1]\n"
    "v_pk_fma_f32 v[58:59], v[58:59], v[226:227], v[26:27] op_sel_hi:[1,0,1]\n"
    "v_pk_mul_f32 v[24:25], v[20:21], v[226:227] op_sel:[0,1] op_sel_hi:[1,1]\n"
    "v_pk_mul_f32 v[26:27], v[22:23], v[226:227] op_sel:[0,1] op_sel_hi:[1,1]\n"
    "v_pk_fma_f32 v[60:61], v[60:61], v[226:227], v[24:25] op_sel_hi:[1,0,1]\n"
    "v_pk_fma_f32 v[62:63], v[62:63], v[226:227], v[26:27] op_sel_hi:[1,0,1]\n"
    "v_pk_mul_f32 v[24:25], v[56:57], v[64:65]\n"
    "v_pk_fma_f32 v[24:25], v[58:59], v[66:67], v[24:25]\n"
    "v_pk_fma_f32 v[24:25], v[60:61], v[68:69], v[24:25]\n"
    "v_pk_fma_f32 v[24:25], v[62:63], v[70:71], v[24:25]\n"
    "v_cvt_pk_bf16_f32 v240, v24, v25\n"
    "v_cvt_pk_bf16_f32 v64, v64, v65\n"
    "v_cvt_pk_bf16_f32 v65, v66, v67\n"
    "v_cvt_pk_bf16_f32 v66, v68, v69\n"
    "v_cvt_pk_bf16_f32 v67, v70, v71\n"
    "v_mfma_f32_16x16x32_bf16 v[76:79], a[132:135], v[240:243], 0\n"
    "ds_write_b128 v28, v[64:67] offset:2048\n"
    "buffer_store_dwordx4 v[64:67], v28, s[44:47], s99 offen offset:2048 sc1\n"
    "v_cvt_pk_bf16_f32 v56, v56, v57\n"
    "v_cvt_pk_bf16_f32 v57, v58, v59\n"
    "v_cvt_pk_bf16_f32 v58, v60, v61\n"
    "v_cvt_pk_bf16_f32 v59, v62, v63\n"
    "v_pk_mul_f32 v[24:25], v[8:9], v[228:229] op_sel:[0,1] op_sel_hi:[1,1]\n"
    "v_pk_mul_f32 v[26:27], v[10:11], v[228:229] op_sel:[0,1] op_sel_hi:[1,1]\n"
    "v_pk_fma_f32 v[96:97], v[96:97], v[228:229], v[24:25] op_sel_hi:[1,0,1]\n"
    "v_pk_fma_f32 v[98:99], v[98:99], v[228:229], v[26:27] op_sel_hi:[1,0,1]\n"
    ".Lk2_m4879:\n"
    ".Lk2_hq2:\n"
    "s_cbranch_execz .Lk2_m8951\n"
    "v_pk_mul_f32 v[24:25], v[12:13], v[228:229] op_sel:[0,1] op_sel_hi:[1,1]\n"
    "v_pk_mul_f32 v[26:27], v[14:15], v[228:229] op_sel:[0,1] op_sel_hi:[1,1]\n"
    "v_pk_fma_f32 v[100:101], v[100:101], v[228:229], v[24:25] op_sel_hi:[1,0,1]\n"
    "v_pk_fma_f32 v[102:103], v[102:103], v[228:229], v[26:27] op_sel_hi:[1,0,1]\n"
    "v_cvt_pk_bf16_f32 v96, v96, v97\n"
    "v_cvt_pk_bf16_f32 v97, v98, v99\n"
    "v_cvt_pk_bf16_f32 v98, v100, v101\n"
    "v_cvt_pk_bf16_f32 v99, v102, v103\n"
    "ds_write_b128 v29, v[96:99] offset:4096\n"
    "s_add_u32 s99, s86, 4096\n"
    "buffer_store_dwordx4 v[96:99], v29, s[44:47], s99 offen sc1\n"
    "v_pk_mul_f32 v[24:25], v[0:1], v[228:229] op_sel:[0,1] op_sel_hi:[1,1]\n"
    "v_pk_mul_f32 v[26:27], v[2:3], v[228:229] op_sel:[0,1] op_sel_hi:[1,1]\n"
    "v_pk_fma_f32 v[88:89], v[88:89], v[228:229], v[24:25] op_sel_hi:[1,0,1]\n"
    "v_pk_fma_f32 v[90:91], v[90:91], v[228:229], v[26:27] op_sel_hi:[1,0,1]\n"
    "v_pk_mul_f32 v[24:25], v[4:5], v[228:229] op_sel:[0,1] op_sel_hi:[1,1]\n"
    "v_pk_mul_f32 v[26:27], v[6:7], v[228:229] op_sel:[0,1] op_sel_hi:[1,1]\n"
    "v_pk_fma_f32 v[92:93], v[92:93], v[228:229], v[24:25] op_sel_hi:[1,0,1]\n"
    "v_pk_fma_f32 v[94:95], v[94:95], v[228:229], v[26:27] op_sel_hi:[1,0,1]\n"
    "v_pk_mul_f32 v[24:25], v[16:17], v[228:229] op_sel:[0,1] op_sel_hi:[1,1]\n"
    "v_pk_mul_f32 v[26:27], v[18:19], v[228:229] op_sel:[0,1] op_sel_hi:[1,1]\n"
    "v_mul_f32 v228, s96, v228\n"
    "v_pk_fma_f32 v[80:81], v[80:81], v[228:229], v[24:25] op_sel_hi:[1,0,1]\n"
    "v_pk_fma_f32 v[82:83], v[82:83], v[228:229], v[26:27] op_sel_hi:[1,0,1]\n"
    "v_pk_mul_f32 v[24:25], v[20:21], v[228:229] op_sel:[0,1] op_sel_hi:[1,1]\n"
    "v_pk_mul_f32 v[26:27], v[22:23], v[228:229] op_sel:[0,1] op_sel_hi:[1,1]\n"
    "v_pk_fma_f32 v[84:85], v[84:85], v[228:229], v[24:25] op_sel_hi:[1,0,1]\n"
    "v_pk_fma_f32 v[86:87], v[86:87], v[228:229], v[26:27] op_sel_hi:[1,0,1]\n"
    "v_pk_mul_f32 v[24:25], v[80:81], v[88:89]\n"
    "v_pk_fma_f32 v[24:25], v[82:83], v[90:91], v[24:25]\n"
    "v_pk_fma_f32 v[24:25], v[84:85], v[92:93], v[24:25]\n"
    "v_pk_fma_f32 v[24:25], v[86:87], v[94:95], v[24:25]\n"
    "v_cvt_pk_bf16_f32 v240, v24, v25\n"
    "v_cvt_pk_bf16_f32 v88, v88, v89\n"
    "v_cvt_pk_bf16_f32 v89, v90, v91\n"
    "v_cvt_pk_bf16_f32 v90, v92, v93\n"
    "v_cvt_pk_bf16_f32 v91, v94, v95\n"
    "v_mfma_f32_16x16x32_bf16 v[100:103], a[132:135], v[240:243], 0\n"
    "ds_write_b128 v28, v[88:91] offset:4096\n"
    "buffer_store_dwordx4 v[88:91], v28, s[44:47], s99 offen sc1\n"
    "v_cvt_pk_bf16_f32 v80, v80, v81\n"
    "v_cvt_pk_bf16_f32 v81, v82, v83\n"
    "v_cvt_pk_bf16_f32 v82, v84, v85\n"
    "v_cvt_pk_bf16_f32 v83, v86, v87\n"
    "v_pk_mul_f32 v[24:25], v[8:9], v[230:231] op_sel:[0,1] op_sel_hi:[1,1]\n"
    "v_pk_mul_f32 v[26:27], v[10:11], v[230:231] op_sel:[0,1] op_sel_hi:[1,1]\n"
    "v_pk_fma_f32 v[120:121], v[120:121], v[230:231], v[24:25] op_sel_hi:[1,0,1]\n"
    "v_pk_fma_f32 v[122:123], v[122:123], v[230:231], v[26:27] op_sel_hi:[1,0,1]\n"
    "v_pk_mul_f32 v[24:25], v[12:13], v[230:231] op_sel:[0,1] op_sel_hi:[1,1]\n"
    "v_pk_mul_f32 v[26:27], v[14:15], v[230:231] op_sel:[0,1] op_sel_hi:[1,1]\n"
    "v_pk_fma_f32 v[124:125], v[124:125], v[230:231], v[24:25] op_sel_hi:[1,0,1]\n"
    "v_pk_fma_f32 v[126:127], v[126:127], v[230:231], v[26:27] op_sel_hi:[1,0,1]\n"
    "v_cvt_pk_bf16_f32 v120, v120, v121\n"
    "v_cvt_pk_bf16_f32 v121, v122, v123\n"
    "v_cvt_pk_bf16_f32 v122, v124, v125\n"
    "v_cvt_pk_bf16_f32 v123, v126, v127\n"
    "ds_write_b128 v29, v[120:123] offset:6144\n"
    "buffer_store_dwordx4 v[120:123], v29, s[44:47], s99 offen offset:2048 sc1\n"
    "v_pk_mul_f32 v[24:25], v[0:1], v[230:231] op_sel:[0,1] op_sel_hi:[1,1]\n"
    "v_pk_mul_f32 v[26:27], v[2:3], v[230:231] op_sel:[0,1] op_sel_hi:[1,1]\n"
    "v_pk_fma_f32 v[112:113], v[112:113], v[230:231], v[24:25] op_sel_hi:[1,0,1]\n"
    "v_pk_fma_f32 v[114:115], v[114:115], v[230:231], v[26:27] op_sel_hi:[1,0,1]\n"
    "v_pk_mul_f32 v[24:25], v[4:5], v[230:231] op_sel:[0,1] op_sel_hi:[1,1]\n"
    "v_pk_mul_f32 v[26:27], v[6:7], v[230:231] op_sel:[0,1] op_sel_hi:[1,1]\n"
    "v_pk_fma_f32 v[116:117], v[116:117], v[230:231], v[24:25] op_sel_hi:[1,0,1]\n"
    "v_pk_fma_f32 v[118:119], v[118:119], v[230:231], v[26:27] op_sel_hi:[1,0,1]\n"
    "v_pk_mul_f32 v[24:25], v[16:17], v[230:231] op_sel:[0,1] op_sel_hi:[1,1]\n"
    "v_pk_mul_f32 v[26:27], v[18:19], v[230:231] op_sel:[0,1] op_sel_hi:[1,1]\n"
    "v_mul_f32 v230, s96, v230\n"
    "v_pk_fma_f32 v[104:105], v[104:105], v[230:231], v[24:25] op_sel_hi:[1,0,1]\n"
    "v_pk_fma_f32 v[106:107], v[106:107], v[230:231], v[26:27] op_sel_hi:[1,0,1]\n"
    "v_pk_mul_f32 v[24:25], v[20:21], v[230:231] op_sel:[0,1] op_sel_hi:[1,1]\n"
    "v_pk_mul_f32 v[26:27], v[22:23], v[230:231] op_sel:[0,1] op_sel_hi:[1,1]\n"
    "v_pk_fma_f32 v[108:109], v[108:109], v[230:231], v[24:25] op_sel_hi:[1,0,1]\n"
    "v_pk_fma_f32 v[110:111], v[110:111], v[230:231], v[26:27] op_sel_hi:[1,0,1]\n"
    "v_pk_mul_f32 v[24:25], v[104:105], v[112:113]\n"
    "v_pk_fma_f32 v[24:25], v[106:107], v[114:115], v[24:25]\n"
    "v_pk_fma_f32 v[24:25], v[108:109], v[116:117], v[24:25]\n"
    "v_pk_fma_f32 v[24:25], v[110:111], v[118:119], v[24:25]\n"
    "v_cvt_pk_bf16_f32 v240, v24, v25\n"
    "v_cvt_pk_bf16_f32 v112, v112, v113\n"
    "v_cvt_pk_bf16_f32 v113, v114, v115\n"
    "v_cvt_pk_bf16_f32 v114, v116, v117\n"
    "v_cvt_pk_bf16_f32 v115, v118, v119\n"
    "v_mfma_f32_16x16x32_bf16 v[124:127], a[132:135], v[240:243], 0\n"
    "ds_write_b128 v28, v[112:115] offset:6144\n"
    "buffer_store_dwordx4 v[112:115], v28, s[44:47], s99 offen offset:2048 sc1\n"
    "v_cvt_pk_bf16_f32 v104, v104, v105\n"
    "v_cvt_pk_bf16_f32 v105, v106, v107\n"
    "v_cvt_pk_bf16_f32 v106, v108, v109\n"
    "v_cvt_pk_bf16_f32 v107, v110, v111\n"
    "v_pk_mul_f32 v[24:25], v[8:9], v[232:233] op_sel:[0,1] op_sel_hi:[1,1]\n"
    "v_pk_mul_f32 v[26:27], v[10:11], v[232:233] op_sel:[0,1] op_sel_hi:[1,1]\n"
    "v_pk_fma_f32 v[144:145], v[144:145], v[232:233], v[24:25] op_sel_hi:[1,0,1]\n"
    "v_pk_fma_f32 v[146:147], v[146:147], v[232:233], v[26:27] op_sel_hi:[1,0,1]\n"
    "v_pk_mul_f32 v[24:25], v[12:13], v[232:233] op_sel:[0,1] op_sel_hi:[1,1]\n"
    "v_pk_mul_f32 v[26:27], v[14:15], v[232:233] op_sel:[0,1] op_sel_hi:[1,1]\n"
    "v_pk_fma_f32 v[148:149], v[148:149], v[232:233], v[24:25] op_sel_hi:[1,0,1]\n"
    "v_pk_fma_f32 v[150:151], v[150:151], v[232:233], v[26:27] op_sel_hi:[1,0,1]\n"
    "v_cvt_pk_bf16_f32 v144, v144, v145\n"
    "v_cvt_pk_bf16_f32 v145, v146, v147\n"
    "v_cvt_pk_bf16_f32 v146, v148, v149\n"
    "v_cvt_pk_bf16_f32 v147, v150, v151\n"
    "ds_write_b128 v29, v[144:147] offset:8192\n"
    "s_add_u32 s99, s86, 8192\n"
    "buffer_store_dwordx4 v[144:147], v29, s[44:47], s99 offen sc1\n"
    "v_pk_mul_f32 v[24:25], v[0:1], v[232:233] op_sel:[0,1] op_sel_hi:[1,1]\n"
    "v_pk_mul_f32 v[26:27], v[2:3], v[232:233] op_sel:[0,1] op_sel_hi:[1,1]\n"
    "v_pk_fma_f32 v[136:137], v[136:137], v[232:233], v[24:25] op_sel_hi:[1,0,1]\n"
    "v_pk_fma_f32 v[138:139], v[138:139], v[232:233], v[26:27] op_sel_hi:[1,0,1]\n"
    "v_pk_mul_f32 v[24:25], v[4:5], v[232:233] op_sel:[0,1] op_sel_hi:[1,1]\n"
    "v_pk_mul_f32 v[26:27], v[6:7], v[232:233] op_sel:[0,1] op_sel_hi:[1,1]\n"
    "v_pk_fma_f32 v[140:141], v[140:141], v[232:233], v[24:25] op_sel_hi:[1,0,1]\n"
    "v_pk_fma_f32 v[142:143], v[142:143], v[232:233], v[26:27] op_sel_hi:[1,0,1]\n"
    "v_pk_mul_f32 v[24:25], v[16:17], v[232:233] op_sel:[0,1] op_sel_hi:[1,1]\n"
    "v_pk_mul_f32 v[26:27], v[18:19], v[232:233] op_sel:[0,1] op_sel_hi:[1,1]\n"
    "v_mul_f32 v232, s96, v232\n"
    "v_pk_fma_f32 v[128:129], v[128:129], v[232:233], v[24:25] op_sel_hi:[1,0,1]\n"
    "v_pk_fma_f32 v[130:131], v[130:131], v[232:233], v[26:27] op_sel_hi:[1,0,1]\n"
    "v_pk_mul_f32 v[24:25], v[20:21], v[232:233] op_sel:[0,1] op_sel_hi:[1,1]\n"
    "v_pk_mul_f32 v[26:27], v[22:23], v[232:233] op_sel:[0,1] op_sel_hi:[1,1]\n"
    "v_pk_fma_f32 v[132:133], v[132:133], v[232:233], v[24:25] op_sel_hi:[1,0,1]\n"
    "v_pk_fma_f32 v[134:135], v[134:135], v[232:233], v[26:27] op_sel_hi:[1,0,1]\n"
    "v_pk_mul_f32 v[24:25], v[128:129], v[136:137]\n"
    "v_pk_fma_f32 v[24:25], v[130:131], v[138:139], v[24:25]\n"
    "v_pk_fma_f32 v[24:25], v[132:133], v[140:141], v[24:25]\n"
    "v_pk_fma_f32 v[24:25], v[134:135], v[142:143], v[24:25]\n"
    "v_cvt_pk_bf16_f32 v240, v24, v25\n"
    "v_cvt_pk_bf16_f32 v136, v136, v137\n"
    "v_cvt_pk_bf16_f32 v137, v138, v139\n"
    "v_cvt_pk_bf16_f32 v138, v140, v141\n"
    "v_cvt_pk_bf16_f32 v139, v142, v143\n"
    "v_mfma_f32_16x16x32_bf16 v[148:151], a[132:135], v[240:243], 0\n"
    "ds_write_b128 v28, v[136:139] offset:8192\n"
    "buffer_store_dwordx4 v[136:139], v28, s[44:47], s99 offen sc1\n"
    "v_cvt_pk_bf16_f32 v128, v128, v129\n"
    "v_cvt_pk_bf16_f32 v129, v130, v131\n"
    "v_cvt_pk_bf16_f32 v130, v132, v133\n"
    "v_cvt_pk_bf16_f32 v131, v134, v135\n"
    "v_pk_mul_f32 v[24:25], v[8:9], v[234:235] op_sel:[0,1] op_sel_hi:[1,1]\n"
    "v_pk_mul_f32 v[26:27], v[10:11], v[234:235] op_sel:[0,1] op_sel_hi:[1,1]\n"
    "v_pk_fma_f32 v[168:169], v[168:169], v[234:235], v[24:25] op_sel_hi:[1,0,1]\n"
    "v_pk_fma_f32 v[170:171], v[170:171], v[234:235], v[26:27] op_sel_hi:[1,0,1]\n"
    "v_pk_mul_f32 v[24:25], v[12:13], v[234:235] op_sel:[0,1] op_sel_hi:[1,1]\n"
    "v_pk_mul_f32 v[26:27], v[14:15], v[234:235] op_sel:[0,1] op_sel_hi:[1,1]\n"
    "v_pk_fma_f32 v[172:173], v[172:173], v[234:235], v[24:25] op_sel_hi:[1,0,1]\n"
    "v_pk_fma_f32 v[174:175], v[174:175], v[234:235], v[26:27] op_sel_hi:[1,0,1]\n"
    "v_cvt_pk_bf16_f32 v168, v168, v169\n"
    "v_cvt_pk_bf16_f32 v169, v170, v171\n"
    "v_cvt_pk_bf16_f32 v170, v172, v173\n"
    "v_cvt_pk_bf16_f32 v171, v174, v175\n"
    "ds_write_b128 v29, v[168:171] offset:10240\n"
    "buffer_store_dwordx4 v[168:171], v29, s[44:47], s99 offen offset:2048 sc1\n"
    "v_pk_mul_f32 v[24:25], v[0:1], v[234:235] op_sel:[0,1] op_sel_hi:[1,1]\n"
    "v_pk_mul_f32 v[26:27], v[2:3], v[234:235] op_sel:[0,1] op_sel_hi:[1,1]\n"
    "v_pk_fma_f32 v[160:161], v[160:161], v[234:235], v[24:25] op_sel_hi:[1,0,1]\n"
    "v_pk_fma_f32 v[162:163], v[162:163], v[234:235], v[26:27] op_sel_hi:[1,0,1]\n"
    "v_pk_mul_f32 v[24:25], v[4:5], v[234:235] op_sel:[0,1] op_sel_hi:[1,1]\n"
    "v_pk_mul_f32 v[26:27], v[6:7], v[234:235] op_sel:[0,1] op_sel_hi:[1,1]\n"
    "v_pk_fma_f32 v[164:165], v[164:165], v[234:235], v[24:25] op_sel_hi:[1,0,1]\n"
    "v_pk_fma_f32 v[166:167], v[166:167], v[234:235], v[26:27] op_sel_hi:[1,0,1]\n"
    "v_pk_mul_f32 v[24:25], v[16:17], v[234:235] op_sel:[0,1] op_sel_hi:[1,1]\n"
    "v_pk_mul_f32 v[26:27], v[18:19], v[234:235] op_sel:[0,1] op_sel_hi:[1,1]\n"
    "v_mul_f32 v234, s96, v234\n"
    "v_pk_fma_f32 v[152:153], v[152:153], v[234:235], v[24:25] op_sel_hi:[1,0,1]\n"
    "v_pk_fma_f32 v[154:155], v[154:155], v[234:235], v[26:27] op_sel_hi:[1,0,1]\n"
    "v_pk_mul_f32 v[24:25], v[20:21], v[234:235] op_sel:[0,1] op_sel_hi:[1,1]\n"
    "v_pk_mul_f32 v[26:27], v[22:23], v[234:235] op_sel:[0,1] op_sel_hi:[1,1]\n"
    "v_pk_fma_f32 v[156:157], v[156:157], v[234:235], v[24:25] op_sel_hi:[1,0,1]\n"
    "v_pk_fma_f32 v[158:159], v[158:159], v[234:235], v[26:27] op_sel_hi:[1,0,1]\n"
    "v_pk_mul_f32 v[24:25], v[152:153], v[160:161]\n"
    "v_pk_fma_f32 v[24:25], v[154:155], v[162:163], v[24:25]\n"
    "v_pk_fma_f32 v[24:25], v[156:157], v[164:165], v[24:25]\n"
    "v_pk_fma_f32 v[24:25], v[158:159], v[166:167], v[24:25]\n"
    "v_cvt_pk_bf16_f32 v240, v24, v25\n"
    "v_cvt_pk_bf16_f32 v160, v160, v161\n"
    "v_cvt_pk_bf16_f32 v161, v162, v163\n"
    "v_cvt_pk_bf16_f32 v162, v164, v165\n"
    "v_cvt_pk_bf16_f32 v163, v166, v167\n"
    "v_mfma_f32_16x16x32_bf16 v[172:175], a[132:135], v[240:243], 0\n"
    "ds_write_b128 v28, v[160:163] offset:10240\n"
    "buffer_store_dwordx4 v[160:163], v28, s[44:47], s99 offen offset:2048 sc1\n"
    "v_cvt_pk_bf16_f32 v152, v152, v153\n"
    "v_cvt_pk_bf16_f32 v153, v154, v155\n"
    "v_cvt_pk_bf16_f32 v154, v156, v157\n"
    "v_cvt_pk_bf16_f32 v155, v158, v159\n"
    "v_pk_mul_f32 v[24:25], v[8:9], v[236:237] op_sel:[0,1] op_sel_hi:[1,1]\n"
    "v_pk_mul_f32 v[26:27], v[10:11], v[236:237] op_sel:[0,1] op_sel_hi:[1,1]\n"
    "v_pk_fma_f32 v[192:193], v[192:193], v[236:237], v[24:25] op_sel_hi:[1,0,1]\n"
    "v_pk_fma_f32 v[194:195], v[194:195], v[236:237], v[26:27] op_sel_hi:[1,0,1]\n"
    "v_pk_mul_f32 v[24:25], v[12:13], v[236:237] op_sel:[0,1] op_sel_hi:[1,1]\n"
    "v_pk_mul_f32 v[26:27], v[14:15], v[236:237] op_sel:[0,1] op_sel_hi:[1,1]\n"
    "v_pk_fma_f32 v[196:197], v[196:197], v[236:237], v[24:25] op_sel_hi:[1,0,1]\n"
    "v_pk_fma_f32 v[198:199], v[198:199], v[236:237], v[26:27] op_sel_hi:[1,0,1]\n"
    "v_cvt_pk_bf16_f32 v192, v192, v193\n"
    "v_cvt_pk_bf16_f32 v193, v194, v195\n"
    "v_cvt_pk_bf16_f32 v194, v196, v197\n"
    "v_cvt_pk_bf16_f32 v195, v198, v199\n"
    "ds_write_b128 v29, v[192:195] offset:12288\n"
    "s_add_u32 s99, s86, 12288\n"
    "buffer_store_dwordx4 v[192:195], v29, s[44:47], s99 offen sc1\n"
    "v_pk_mul_f32 v[24:25], v[0:1], v[236:237] op_sel:[0,1] op_sel_hi:[1,1]\n"
    "v_pk_mul_f32 v[26:27], v[2:3], v[236:237] op_sel:[0,1] op_sel_hi:[1,1]\n"
    "v_pk_fma_f32 v[184:185], v[184:185], v[236:237], v[24:25] op_sel_hi:[1,0,1]\n"
    "v_pk_fma_f32 v[186:187], v[186:187], v[236:237], v[26:27] op_sel_hi:[1,0,1]\n"
    "v_pk_mul_f32 v[24:25], v[4:5], v[236:237] op_sel:[0,1] op_sel_hi:[1,1]\n"
    "v_pk_mul_f32 v[26:27], v[6:7], v[236:237] op_sel:[0,1] op_sel_hi:[1,1]\n"
    "v_pk_fma_f32 v[188:189], v[188:189], v[236:237], v[24:25] op_sel_hi:[1,0,1]\n"
    "v_pk_fma_f32 v[190:191], v[190:191], v[236:237], v[26:27] op_sel_hi:[1,0,1]\n"
    "v_pk_mul_f32 v[24:25], v[16:17], v[236:237] op_sel:[0,1] op_sel_hi:[1,1]\n"
    "v_pk_mul_f32 v[26:27], v[18:19], v[236:237] op_sel:[0,1] op_sel_hi:[1,1]\n"
    "v_mul_f32 v236, s96, v236\n"
    "v_pk_fma_f32 v[176:177], v[176:177], v[236:237], v[24:25] op_sel_hi:[1,0,1]\n"
    "v_pk_fma_f32 v[178:179], v[178:179], v[236:237], v[26:27] op_sel_hi:[1,0,1]\n"
    "v_pk_mul_f32 v[24:25], v[20:21], v[236:237] op_sel:[0,1] op_sel_hi:[1,1]\n"
    "v_pk_mul_f32 v[26:27], v[22:23], v[236:237] op_sel:[0,1] op_sel_hi:[1,1]\n"
    "v_pk_fma_f32 v[180:181], v[180:181], v[236:237], v[24:25] op_sel_hi:[1,0,1]\n"
    "v_pk_fma_f32 v[182:183], v[182:183], v[236:237], v[26:27] op_sel_hi:[1,0,1]\n"
    "v_pk_mul_f32 v[24:25], v[176:177], v[184:185]\n"
    "v_pk_fma_f32 v[24:25], v[178:179], v[186:187], v[24:25]\n"
    "v_pk_fma_f32 v[24:25], v[180:181], v[188:189], v[24:25]\n"
    "v_pk_fma_f32 v[24:25], v[182:183], v[190:191], v[24:25]\n"
    "v_cvt_pk_bf16_f32 v240, v24, v25\n"
    "v_cvt_pk_bf16_f32 v184, v184, v185\n"
    "v_cvt_pk_bf16_f32 v185, v186, v187\n"
    "v_cvt_pk_bf16_f32 v186, v188, v189\n"
    "v_cvt_pk_bf16_f32 v187, v190, v191\n"
    "v_mfma_f32_16x16x32_bf16 v[196:199], a[132:135], v[240:243], 0\n"
    "ds_write_b128 v28, v[184:187] offset:12288\n"
    "buffer_store_dwordx4 v[184:187], v28, s[44:47], s99 offen sc1\n"
    "v_cvt_pk_bf16_f32 v176, v176, v177\n"
    "v_cvt_pk_bf16_f32 v177, v178, v179\n"
    "v_cvt_pk_bf16_f32 v178, v180, v181\n"
    "v_cvt_pk_bf16_f32 v179, v182, v183\n"
    "v_pk_mul_f32 v[24:25], v[8:9], v[238:239] op_sel:[0,1] op_sel_hi:[1,1]\n"
    "v_pk_mul_f32 v[26:27], v[10:11], v[238:239] op_sel:[0,1] op_sel_hi:[1,1]\n"
    "v_pk_fma_f32 v[216:217], v[216:217], v[238:239], v[24:25] op_sel_hi:[1,0,1]\n"
    "v_pk_fma_f32 v[218:219], v[218:219], v[238:239], v[26:27] op_sel_hi:[1,0,1]\n"
    "v_pk_mul_f32 v[24:25], v[12:13], v[238:239] op_sel:[0,1] op_sel_hi:[1,1]\n"
    "v_pk_mul_f32 v[26:27], v[14:15], v[238:239] op_sel:[0,1] op_sel_hi:[1,1]\n"
    "v_pk_fma_f32 v[220:221], v[220:221], v[238:239], v[24:25] op_sel_hi:[1,0,1]\n"
    "v_pk_fma_f32 v[222:223], v[222:223], v[238:239], v[26:27] op_sel_hi:[1,0,1]\n"
    "v_cvt_pk_bf16_f32 v216, v216, v217\n"
    "v_cvt_pk_bf16_f32 v217, v218, v219\n"
    "v_cvt_pk_bf16_f32 v218, v220, v221\n"
    "v_cvt_pk_bf16_f32 v219, v222, v223\n"
    "ds_write_b128 v29, v[216:219] offset:14336\n"
    "buffer_store_dwordx4 v[216:219], v29, s[44:47], s99 offen offset:2048 sc1\n"
    "v_pk_mul_f32 v[24:25], v[0:1], v[238:239] op_sel:[0,1] op_sel_hi:[1,1]\n"
    "v_pk_mul_f32 v[26:27], v[2:3], v[238:239] op_sel:[0,1] op_sel_hi:[1,1]\n"
    "v_pk_fma_f32 v[208:209], v[208:209], v[238:239], v[24:25] op_sel_hi:[1,0,1]\n"
    "v_pk_fma_f32 v[210:211], v[210:211], v[238:239], v[26:27] op_sel_hi:[1,0,1]\n"
    "v_pk_mul_f32 v[24:25], v[4:5], v[238:239] op_sel:[0,1] op_sel_hi:[1,1]\n"
    "v_pk_mul_f32 v[26:27], v[6:7], v[238:239] op_sel:[0,1] op_sel_hi:[1,1]\n"
    "v_pk_fma_f32 v[212:213], v[212:213], v[238:239], v[24:25] op_sel_hi:[1,0,1]\n"
    "v_pk_fma_f32 v[214:215], v[214:215], v[238:239], v[26:27] op_sel_hi:[1,0,1]\n"
    "v_pk_mul_f32 v[24:25], v[16:17], v[238:239] op_sel:[0,1] op_sel_hi:[1,1]\n"
    "v_pk_mul_f32 v[26:27], v[18:19], v[238:239] op_sel:[0,1] op_sel_hi:[1,1]\n"
    "v_mul_f32 v238, s96, v238\n"
    "v_pk_fma_f32 v[200:201], v[200:201], v[238:239], v[24:25] op_sel_hi:[1,0,1]\n"
    "v_pk_fma_f32 v[202:203], v[202:203], v[238:239], v[26:27] op_sel_hi:[1,0,1]\n"
    "v_pk_mul_f32 v[24:25], v[20:21], v[238:239] op_sel:[0,1] op_sel_hi:[1,1]\n"
    "v_pk_mul_f32 v[26:27], v[22:23], v[238:239] op_sel:[0,1] op_sel_hi:[1,1]\n"
    "v_pk_fma_f32 v[204:205], v[204:205], v[238:239], v[24:25] op_sel_hi:[1,0,1]\n"
    "v_pk_fma_f32 v[206:207], v[206:207], v[238:239], v[26:27] op_sel_hi:[1,0,1]\n"
    "v_pk_mul_f32 v[24:25], v[200:201], v[208:209]\n"
    "v_pk_fma_f32 v[24:25], v[202:203], v[210:211], v[24:25]\n"
    "v_pk_fma_f32 v[24:25], v[204:205], v[212:213], v[24:25]\n"
    "v_pk_fma_f32 v[24:25], v[206:207], v[214:215], v[24:25]\n"
    "v_cvt_pk_bf16_f32 v240, v24, v25\n"
    "v_cvt_pk_bf16_f32 v208, v208, v209\n"
    "v_cvt_pk_bf16_f32 v209, v210, v211\n"
    "v_cvt_pk_bf16_f32 v210, v212, v213\n"
    "v_cvt_pk_bf16_f32 v211, v214, v215\n"
    "v_mfma_f32_16x16x32_bf16 v[220:223], a[132:135], v[240:243], 0\n"
    "ds_write_b128 v28, v[208:211] offset:14336\n"
    "buffer_store_dwordx4 v[208:211], v28, s[44:47], s99 offen offset:2048 sc1\n"
    "v_cvt_pk_bf16_f32 v200, v200, v201\n"
    "v_cvt_pk_bf16_f32 v201, v202, v203\n"
    "v_cvt_pk_bf16_f32 v202, v204, v205\n"
    "v_cvt_pk_bf16_f32 v203, v206, v207\n"
    "s_mov_b32 s97, 0\n"
    "s_mov_b32 s96, 0x5d800000\n"
    "s_waitcnt lgkmcnt(0)\n"
    "s_barrier\n"
    ".Lk2_xl:\n"
    "ds_read_b128 a[0:3], v244 offset:0\n"
    "ds_read_b128 a[4:7], v244 offset:1024\n"
    "ds_read_b64_tr_b16 a[64:65], v245 offset:0\n"
    "ds_read_b64_tr_b16 a[66:67], v245 offset:1024\n"
    "ds_read_b64_tr_b16 a[68:69], v246 offset:0\n"
    "ds_read_b64_tr_b16 a[70:71], v246 offset:1024\n"
    "ds_read_b128 a[8:11], v244 offset:2048\n"
    "ds_read_b128 a[12:15], v244 offset:3072\n"
    "v_accvgpr_read_b32 v224, a184\n"
    "v_accvgpr_read_b32 v225, a185\n"
    "v_accvgpr_read_b32 v226, a186\n"
    "v_accvgpr_read_b32 v227, a187\n"
    "v_accvgpr_read_b32 v228, a188\n"
    "v_accvgpr_read_b32 v229, a189\n"
    "v_accvgpr_read_b32 v230, a190\n"
    "v_accvgpr_read_b32 v231, a191\n"
    "v_bfe_u32 v252, v250, 4, 2\n"
    "v_lshlrev_b32 v252, 2, v252\n"
    "v_pk_add_f32 v[232:233], v[224:225], v[52:53]\n"
    "v_pk_add_f32 v[234:235], v[226:227], v[52:53]\n"
    "v_pk_add_f32 v[236:237], v[228:229], v[52:53]\n"
    "v_pk_add_f32 v[238:239], v[230:231], v[52:53]\n"
    "s_waitcnt lgkmcnt(3)\n"
    "v_mfma_f32_16x16x32_bf16 v[0:3], a[0:3], v[32:35], v[232:235]\n"
    "ds_read_b64_tr_b16 a[72:73], v245 offset:2048\n"
    "ds_read_b64_tr_b16 a[74:75], v245 offset:3072\n"
    "v_mfma_f32_16x16x32_bf16 v[4:7], a[4:7], v[32:35], v[236:239]\n"
    "ds_read_b64_tr_b16 a[76:77], v246 offset:2048\n"
    "ds_read_b64_tr_b16 a[78:79], v246 offset:3072\n"
    "v_mfma_f32_16x16x32_bf16 v[8:11], a[0:3], v[56:59], v[76:79]\n"
    "ds_read_b128 a[16:19], v244 offset:4096\n"
    "s_nop 0\n"
    "v_exp_f32 v0, v0\n"
    "v_exp_f32 v1, v1\n"
    "v_mfma_f32_16x16x32_bf16 v[12:15], a[4:7], v[56:59], v[76:79]\n"
    "ds_read_b128 a[20:23], v244 offset:5120\n"
    "v_exp_f32 v2, v2\n"
    "v_cvt_pk_bf16_f32 v24, v0, v1\n"
    "v_exp_f32 v3, v3\n"
    "v_exp_f32 v4, v4\n"
    "v_exp_f32 v5, v5\n"
    "v_cvt_pk_bf16_f32 v25, v2, v3\n"
    "v_exp_f32 v6, v6\n"
    "v_exp_f32 v7, v7\n"
    "v_cvt_pk_bf16_f32 v26, v4, v5\n"
    "v_cvt_pk_bf16_f32 v27, v6, v7\n"
    "v_pk_add_f32 v[232:233], v[224:225], v[76:77]\n"
    "v_pk_add_f32 v[234:235], v[226:227], v[76:77]\n"
    "v_pk_add_f32 v[236:237], v[228:229], v[76:77]\n"
    "v_pk_add_f32 v[238:239], v[230:231], v[76:77]\n"
    "s_waitcnt lgkmcnt(3)\n"
    "v_mfma_f32_16x16x32_bf16 v[0:3], a[8:11], v[56:59], v[232:235]\n"
    "ds_read_b64_tr_b16 a[80:81], v245 offset:4096\n"
    "v_exp_f32 v8, v8\n"
    "v_exp_f32 v9, v9\n"
    "v_mfma_f32_16x16x32_bf16 v[4:7], a[12:15], v[56:59], v[236:239]\n"
    "ds_read_b64_tr_b16 a[82:83], v245 offset:5120\n"
    "v_exp_f32 v10, v10\n"
    "v_cvt_pk_bf16_f32 v28, v8, v9\n"
    "v_mfma_f32_16x16x32_bf16 v[40:43], a[64:67], v[24:27], 0\n"
    "ds_read_b64_tr_b16 a[84:85], v246 offset:4096\n"
    "v_exp_f32 v11, v11\n"
    "v_exp_f32 v12, v12\n"
    "v_mfma_f32_16x16x32_bf16 v[44:47], a[68:71], v[24:27], 0\n"
    "ds_read_b64_tr_b16 a[86:87], v246 offset:5120\n"
    "v_exp_f32 v13, v13\n"
    "v_cvt_pk_bf16_f32 v29, v10, v11\n"
    "v_mfma_f32_16x16x32_bf16 v[48:51], a[128:131], v[24:27], 0\n"
    "v_exp_f32 v14, v14\n"
    "v_exp_f32 v15, v15\n"
    "v_cvt_pk_bf16_f32 v30, v12, v13\n"
    "v_cvt_pk_bf16_f32 v31, v14, v15\n"
    "v_mfma_f32_16x16x32_bf16 v[8:11], a[0:3], v[80:83], v[100:103]\n"
    "ds_read_b128 a[24:27], v244 offset:6144\n"
    "v_exp_f32 v0, v0\n"
    "v_exp_f32 v1, v1\n"
    "v_mfma_f32_16x16x32_bf16 v[12:15], a[4:7], v[80:83], v[100:103]\n"
    "ds_read_b128 a[28:31], v244 offset:7168\n"
    "v_exp_f32 v2, v2\n"
    "v_cvt_pk_bf16_f32 v24, v0, v1\n"
    "v_mfma_f32_16x16x32_bf16 v[64:67], a[64:67], v[28:31], 0\n"
    "ds_read_b64_tr_b16 a[88:89], v245 offset:6144\n"
    "v_exp_f32 v3, v3\n"
    "v_exp_f32 v4, v4\n"
    "v_mfma_f32_16x16x32_bf16 v[68:71], a[68:71], v[28:31], 0\n"
    "ds_read_b64_tr_b16 a[90:91], v245 offset:7168\n"
    "v_exp_f32 v5, v5\n"
    "v_cvt_pk_bf16_f32 v25, v2, v3\n"
    "v_mfma_f32_16x16x32_bf16 v[72:75], a[128:131], v[28:31], 0\n"
    "ds_read_b64_tr_b16 a[92:93], v246 offset:6144\n"
    "v_exp_f32 v6, v6\n"
    "v_exp_f32 v7, v7\n"
    "v_cvt_pk_bf16_f32 v26, v4, v5\n"
    "v_cvt_pk_bf16_f32 v27, v6, v7\n"
    "v_mfma_f32_16x16x32_bf16 v[0:3], a[8:11], v[80:83], v[100:103]\n"
    "ds_read_b64_tr_b16 a[94:95], v246 offset:7168\n"
    "v_exp_f32 v8, v8\n"
    "v_exp_f32 v9, v9\n"
    "v_mfma_f32_16x16x32_bf16 v[4:7], a[12:15], v[80:83], v[100:103]\n"
    "v_exp_f32 v10, v10\n"
    "v_cvt_pk_bf16_f32 v28, v8, v9\n"
    "v_mfma_f32_16x16x32_bf16 v[64:67], a[72:75], v[24:27], v[64:67]\n"
    "v_exp_f32 v11, v11\n"
    "v_exp_f32 v12, v12\n"
    "s_waitcnt lgkmcnt(3)\n"
    "v_mfma_f32_16x16x32_bf16 v[68:71], a[76:79], v[24:27], v[68:71]\n"
    "v_exp_f32 v13, v13\n"
    "v_cvt_pk_bf16_f32 v29, v10, v11\n"
    "v_mfma_f32_16x16x32_bf16 v[72:75], a[128:131], v[24:27], v[72:75]\n"
    "v_exp_f32 v14, v14\n"
    "v_exp_f32 v15, v15\n"
    "v_cvt_pk_bf16_f32 v30, v12, v13\n"
    "v_cvt_pk_bf16_f32 v31, v14, v15\n"
    "v_pk_add_f32 v[232:233], v[224:225], v[100:101]\n"
    "v_pk_add_f32 v[234:235], v[226:227], v[100:101]\n"
    "v_pk_add_f32 v[236:237], v[228:229], v[100:101]\n"
    "v_pk_add_f32 v[238:239], v[230:231], v[100:101]\n"
    "v_mfma_f32_16x16x32_bf16 v[8:11], a[16:19], v[80:83], v[232:235]\n"
    "ds_read_b128 a[32:35], v244 offset:8192\n"
    "v_exp_f32 v0, v0\n"
    "v_exp_f32 v1, v1\n"
    "v_mfma_f32_16x16x32_bf16 v[12:15], a[20:23], v[80:83], v[236:239]\n"
    "ds_read_b128 a[36:39], v244 offset:9216\n"
    "v_exp_f32 v2, v2\n"
    "v_cvt_pk_bf16_f32 v24, v0, v1\n"
    "v_mfma_f32_16x16x32_bf16 v[88:91], a[64:67], v[28:31], 0\n"
    "ds_read_b64_tr_b16 a[96:97], v245 offset:8192\n"
    "v_exp_f32 v3, v3\n"
    "v_exp_f32 v4, v4\n"
    "v_mfma_f32_16x16x32_bf16 v[92:95], a[68:71], v[28:31], 0\n"
    "ds_read_b64_tr_b16 a[98:99], v245 offset:9216\n"
    "v_exp_f32 v5, v5\n"
    "v_cvt_pk_bf16_f32 v25, v2, v3\n"
    "v_mfma_f32_16x16x32_bf16 v[96:99], a[128:131], v[28:31], 0\n"
    "ds_read_b64_tr_b16 a[100:101], v246 offset:8192\n"
    "v_exp_f32 v6, v6\n"
    "v_exp_f32 v7, v7\n"
    "v_cvt_pk_bf16_f32 v26, v4, v5\n"
    "v_cvt_pk_bf16_f32 v27, v6, v7\n"
    "v_mfma_f32_16x16x32_bf16 v[0:3], a[0:3], v[104:107], v[124:127]\n"
    "ds_read_b64_tr_b16 a[102:103], v246 offset:9216\n"
    "v_exp_f32 v8, v8\n"
    "v_exp_f32 v9, v9\n"
    "v_mfma_f32_16x16x32_bf16 v[4:7], a[4:7], v[104:107], v[124:127]\n"
    "s_add_u32 s93, s92, 1\n"
    "v_mov_b32 v251, s93\n"
    "v_exp_f32 v10, v10\n"
    "v_cvt_pk_bf16_f32 v28, v8, v9\n"
    "v_mfma_f32_16x16x32_bf16 v[88:91], a[72:75], v[24:27], v[88:91]\n"
    "s_waitcnt vmcnt(0)\n"
    "v_exp_f32 v11, v11\n"
    "v_exp_f32 v12, v12\n"
    "v_mfma_f32_16x16x32_bf16 v[92:95], a[76:79], v[24:27], v[92:95]\n"
    "s_mov_b64 exec, 1\n"
    "buffer_store_dword v251, off, s[44:47], s64 sc1\n"
    "s_mov_b64 exec, -1\n"
    "v_exp_f32 v13, v13\n"
    "v_cvt_pk_bf16_f32 v29, v10, v11\n"
    "v_mfma_f32_16x16x32_bf16 v[96:99], a[128:131], v[24:27], v[96:99]\n"
    "v_exp_f32 v14, v14\n"
    "v_exp_f32 v15, v15\n"
    "v_cvt_pk_bf16_f32 v30, v12, v13\n"
    "v_cvt_pk_bf16_f32 v31, v14, v15\n"
    "v_mfma_f32_16x16x32_bf16 v[8:11], a[8:11], v[104:107], v[124:127]\n"
    "ds_read_b128 a[40:43], v244 offset:10240\n"
    "v_exp_f32 v0, v0\n"
    "v_exp_f32 v1, v1\n"
    "v_mfma_f32_16x16x32_bf16 v[12:15], a[12:15], v[104:107], v[124:127]\n"
    "ds_read_b128 a[44:47], v244 offset:11264\n"
    "v_exp_f32 v2, v2\n"
    "v_cvt_pk_bf16_f32 v24, v0, v1\n"
    "v_mfma_f32_16x16x32_bf16 v[88:91], a[80:83], v[28:31], v[88:91]\n"
    "ds_read_b64_tr_b16 a[104:105], v245 offset:10240\n"
    "v_exp_f32 v3, v3\n"
    "v_exp_f32 v4, v4\n"
    "v_mfma_f32_16x16x32_bf16 v[92:95], a[84:87], v[28:31], v[92:95]\n"
    "ds_read_b64_tr_b16 a[106:107], v245 offset:11264\n"
    "v_exp_f32 v5, v5\n"
    "v_cvt_pk_bf16_f32 v25, v2, v3\n"
    "v_mfma_f32_16x16x32_bf16 v[96:99], a[128:131], v[28:31], v[96:99]\n"
    "ds_read_b64_tr_b16 a[108:109], v246 offset:10240\n"
    "v_exp_f32 v6, v6\n"
    "v_exp_f32 v7, v7\n"
    "v_cvt_pk_bf16_f32 v26, v4, v5\n"
    "v_cvt_pk_bf16_f32 v27, v6, v7\n"
    "v_mfma_f32_16x16x32_bf16 v[0:3], a[16:19], v[104:107], v[124:127]\n"
    "ds_read_b64_tr_b16 a[110:111], v246 offset:11264\n"
    "v_exp_f32 v8, v8\n"
    "v_exp_f32 v9, v9\n"
    "v_mfma_f32_16x16x32_bf16 v[4:7], a[20:23], v[104:107], v[124:127]\n"
    "v_exp_f32 v10, v10\n"
    "v_cvt_pk_bf16_f32 v28, v8, v9\n"
    "v_mfma_f32_16x16x32_bf16 v[112:115], a[64:67], v[24:27], 0\n"
    "v_exp_f32 v11, v11\n"
    "v_exp_f32 v12, v12\n"
    "v_mfma_f32_16x16x32_bf16 v[116:119], a[68:71], v[24:27], 0\n"
    "v_exp_f32 v13, v13\n"
    "v_cvt_pk_bf16_f32 v29, v10, v11\n"
    "v_mfma_f32_16x16x32_bf16 v[120:123], a[128:131], v[24:27], 0\n"
    "v_exp_f32 v14, v14\n"
    "v_exp_f32 v15, v15\n"
    "v_cvt_pk_bf16_f32 v30, v12, v13\n"
    "v_cvt_pk_bf16_f32 v31, v14, v15\n"
    "v_pk_add_f32 v[232:233], v[224:225], v[124:125]\n"
    "v_pk_add_f32 v[234:235], v[226:227], v[124:125]\n"
    "v_pk_add_f32 v[236:237], v[228:229], v[124:125]\n"
    "v_pk_add_f32 v[238:239], v[230:231], v[124:125]\n"
    "v_mfma_f32_16x16x32_bf16 v[8:11], a[24:27], v[104:107], v[232:235]\n"
    "ds_read_b128 a[48:51], v244 offset:12288\n"
    "v_exp_f32 v0, v0\n"
    "v_exp_f32 v1, v1\n"
    "v_mfma_f32_16x16x32_bf16 v[12:15], a[28:31], v[104:107], v[236:239]\n"
    "ds_read_b128 a[52:55], v244 offset:13312\n"
    "v_exp_f32 v2, v2\n"
    "v_cvt_pk_bf16_f32 v24, v0, v1\n"
    "v_mfma_f32_16x16x32_bf16 v[112:115], a[72:75], v[28:31], v[112:115]\n"
    "ds_read_b64_tr_b16 a[112:113], v245 offset:12288\n"
    "v_exp_f32 v3, v3\n"
    "v_exp_f32 v4, v4\n"
    "v_mfma_f32_16x16x32_bf16 v[116:119], a[76:79], v[28:31], v[116:119]\n"
    "ds_read_b64_tr_b16 a[114:115], v245 offset:13312\n"
    "v_exp_f32 v5, v5\n"
    "v_cvt_pk_bf16_f32 v25, v2, v3\n"
    "v_mfma_f32_16x16x32_bf16 v[120:123], a[128:131], v[28:31], v[120:123]\n"
    "ds_read_b64_tr_b16 a[116:117], v246 offset:12288\n"
    "v_exp_f32 v6, v6\n"
    "v_exp_f32 v7, v7\n"
    "v_cvt_pk_bf16_f32 v26, v4, v5\n"
    "v_cvt_pk_bf16_f32 v27, v6, v7\n"
    "v_mfma_f32_16x16x32_bf16 v[0:3], a[0:3], v[128:131], v[148:151]\n"
    "ds_read_b64_tr_b16 a[118:119], v246 offset:13312\n"
    "v_exp_f32 v8, v8\n"
    "v_exp_f32 v9, v9\n"
    "v_mfma_f32_16x16x32_bf16 v[4:7], a[4:7], v[128:131], v[148:151]\n"
    "v_exp_f32 v10, v10\n"
    "v_cvt_pk_bf16_f32 v28, v8, v9\n"
    "v_mfma_f32_16x16x32_bf16 v[112:115], a[80:83], v[24:27], v[112:115]\n"
    "v_exp_f32 v11, v11\n"
    "v_exp_f32 v12, v12\n"
    "v_mfma_f32_16x16x32_bf16 v[116:119], a[84:87], v[24:27], v[116:119]\n"
    "v_exp_f32 v13, v13\n"
    "v_cvt_pk_bf16_f32 v29, v10, v11\n"
    "v_mfma_f32_16x16x32_bf16 v[120:123], a[128:131], v[24:27], v[120:123]\n"
    "v_exp_f32 v14, v14\n"
    "v_exp_f32 v15, v15\n"
    "v_cvt_pk_bf16_f32 v30, v12, v13\n"
    "v_cvt_pk_bf16_f32 v31, v14, v15\n"
    "v_mfma_f32_16x16x32_bf16 v[8:11], a[8:11], v[128:131], v[148:151]\n"
    "ds_read_b128 a[56:59], v244 offset:14336\n"
    "v_exp_f32 v0, v0\n"
    "v_exp_f32 v1, v1\n"
    "v_mfma_f32_16x16x32_bf16 v[12:15], a[12:15], v[128:131], v[148:151]\n"
    "ds_read_b128 a[60:63], v244 offset:15360\n"
    "v_exp_f32 v2, v2\n"
    "v_cvt_pk_bf16_f32 v24, v0, v1\n"
    "s_waitcnt lgkmcnt(3)\n"
    "v_mfma_f32_16x16x32_bf16 v[112:115], a[88:91], v[28:31], v[112:115]\n"
    "ds_read_b64_tr_b16 a[120:121], v245 offset:14336\n"
    "v_exp_f32 v3, v3\n"
    "v_exp_f32 v4, v4\n"
    "v_mfma_f32_16x16x32_bf16 v[116:119], a[92:95], v[28:31], v[116:119]\n"
    "ds_read_b64_tr_b16 a[122:123], v245 offset:15360\n"
    "v_exp_f32 v5, v5\n"
    "v_cvt_pk_bf16_f32 v25, v2, v3\n"
    "v_mfma_f32_16x16x32_bf16 v[120:123], a[128:131], v[28:31], v[120:123]\n"
    "ds_read_b64_tr_b16 a[124:125], v246 offset:14336\n"
    "v_exp_f32 v6, v6\n"
    "v_exp_f32 v7, v7\n"
    "v_cvt_pk_bf16_f32 v26, v4, v5\n"
    "v_cvt_pk_bf16_f32 v27, v6, v7\n"
    "v_mfma_f32_16x16x32_bf16 v[0:3], a[16:19], v[128:131], v[148:151]\n"
    "ds_read_b64_tr_b16 a[126:127], v246 offset:15360\n"
    "v_exp_f32 v8, v8\n"
    "v_exp_f32 v9, v9\n"
    "v_mfma_f32_16x16x32_bf16 v[4:7], a[20:23], v[128:131], v[148:151]\n"
    "v_exp_f32 v10, v10\n"
    "v_cvt_pk_bf16_f32 v28, v8, v9\n"
    "v_mfma_f32_16x16x32_bf16 v[136:139], a[64:67], v[24:27], 0\n"
    "v_exp_f32 v11, v11\n"
    "v_exp_f32 v12, v12\n"
    "v_mfma_f32_16x16x32_bf16 v[140:143], a[68:71], v[24:27], 0\n"
    "v_exp_f32 v13, v13\n"
    "v_cvt_pk_bf16_f32 v29, v10, v11\n"
    "v_mfma_f32_16x16x32_bf16 v[144:147], a[128:131], v[24:27], 0\n"
    ".Lk2_m5450:\n"
    ".Lk2_hq3:\n"
    "s_cbranch_execz .Lk2_m9082\n"
    "v_exp_f32 v14, v14\n"
    "v_exp_f32 v15, v15\n"
    "v_cvt_pk_bf16_f32 v30, v12, v13\n"
    "v_cvt_pk_bf16_f32 v31, v14, v15\n"
    "v_mfma_f32_16x16x32_bf16 v[8:11], a[24:27], v[128:131], v[148:151]\n"
    "v_exp_f32 v0, v0\n"
    "v_exp_f32 v1, v1\n"
    "v_mfma_f32_16x16x32_bf16 v[12:15], a[28:31], v[128:131], v[148:151]\n"
    "v_exp_f32 v2, v2\n"
    "v_cvt_pk_bf16_f32 v24, v0, v1\n"
    "v_mfma_f32_16x16x32_bf16 v[136:139], a[72:75], v[28:31], v[136:139]\n"
    "v_exp_f32 v3, v3\n"
    "v_exp_f32 v4, v4\n"
    "v_mfma_f32_16x16x32_bf16 v[140:143], a[76:79], v[28:31], v[140:143]\n"
    "v_exp_f32 v5, v5\n"
    "v_cvt_pk_bf16_f32 v25, v2, v3\n"
    "v_mfma_f32_16x16x32_bf16 v[144:147], a[128:131], v[28:31], v[144:147]\n"
    "v_exp_f32 v6, v6\n"
    "v_exp_f32 v7, v7\n"
    "v_cvt_pk_bf16_f32 v26, v4, v5\n"
    "v_cvt_pk_bf16_f32 v27, v6, v7\n"
    "v_pk_add_f32 v[232:233], v[224:225], v[148:149]\n"
    "v_pk_add_f32 v[234:235], v[226:227], v[148:149]\n"
    "v_pk_add_f32 v[236:237], v[228:229], v[148:149]\n"
    "v_pk_add_f32 v[238:239], v[230:231], v[148:149]\n"
    "v_mfma_f32_16x16x32_bf16 v[0:3], a[32:35], v[128:131], v[232:235]\n"
    "v_exp_f32 v8, v8\n"
    "v_exp_f32 v9, v9\n"
    "v_mfma_f32_16x16x32_bf16 v[4:7], a[36:39], v[128:131], v[236:239]\n"
    "v_exp_f32 v10, v10\n"
    "v_cvt_pk_bf16_f32 v28, v8, v9\n"
    "v_mfma_f32_16x16x32_bf16 v[136:139], a[80:83], v[24:27], v[136:139]\n"
    "v_exp_f32 v11, v11\n"
    "v_exp_f32 v12, v12\n"
    "v_mfma_f32_16x16x32_bf16 v[140:143], a[84:87], v[24:27], v[140:143]\n"
    "v_exp_f32 v13, v13\n"
    "v_cvt_pk_bf16_f32 v29, v10, v11\n"
    "v_mfma_f32_16x16x32_bf16 v[144:147], a[128:131], v[24:27], v[144:147]\n"
    "v_exp_f32 v14, v14\n"
    "v_exp_f32 v15, v15\n"
    "v_cvt_pk_bf16_f32 v30, v12, v13\n"
    "v_cvt_pk_bf16_f32 v31, v14, v15\n"
    "v_mfma_f32_16x16x32_bf16 v[8:11], a[0:3], v[152:155], v[172:175]\n"
    "v_exp_f32 v0, v0\n"
    "v_exp_f32 v1, v1\n"
    "v_mfma_f32_16x16x32_bf16 v[12:15], a[4:7], v[152:155], v[172:175]\n"
    "v_exp_f32 v2, v2\n"
    "v_cvt_pk_bf16_f32 v24, v0, v1\n"
    "v_mfma_f32_16x16x32_bf16 v[136:139], a[88:91], v[28:31], v[136:139]\n"
    "v_exp_f32 v3, v3\n"
    "v_exp_f32 v4, v4\n"
    "v_mfma_f32_16x16x32_bf16 v[140:143], a[92:95], v[28:31], v[140:143]\n"
    "v_exp_f32 v5, v5\n"
    "v_cvt_pk_bf16_f32 v25, v2, v3\n"
    "v_mfma_f32_16x16x32_bf16 v[144:147], a[128:131], v[28:31], v[144:147]\n"
    "v_exp_f32 v6, v6\n"
    "v_exp_f32 v7, v7\n"
    "v_cvt_pk_bf16_f32 v26, v4, v5\n"
    "v_cvt_pk_bf16_f32 v27, v6, v7\n"
    "v_mfma_f32_16x16x32_bf16 v[0:3], a[8:11], v[152:155], v[172:175]\n"
    "v_exp_f32 v8, v8\n"
    "v_exp_f32 v9, v9\n"
    "v_mfma_f32_16x16x32_bf16 v[4:7], a[12:15], v[152:155], v[172:175]\n"
    "v_exp_f32 v10, v10\n"
    "v_cvt_pk_bf16_f32 v28, v8, v9\n"
    "v_mfma_f32_16x16x32_bf16 v[136:139], a[96:99], v[24:27], v[136:139]\n"
    "v_exp_f32 v11, v11\n"
    "v_exp_f32 v12, v12\n"
    "v_mfma_f32_16x16x32_bf16 v[140:143], a[100:103], v[24:27], v[140:143]\n"
    "v_exp_f32 v13, v13\n"
    "v_cvt_pk_bf16_f32 v29, v10, v11\n"
    "v_mfma_f32_16x16x32_bf16 v[144:147], a[128:131], v[24:27], v[144:147]\n"
    "v_exp_f32 v14, v14\n"
    "v_exp_f32 v15, v15\n"
    "v_cvt_pk_bf16_f32 v30, v12, v13\n"
    "v_cvt_pk_bf16_f32 v31, v14, v15\n"
    "v_mfma_f32_16x16x32_bf16 v[8:11], a[16:19], v[152:155], v[172:175]\n"
    "s_mov_b64 exec, 15\n"
    "buffer_load_dword v251, v252, s[44:47], s65 offen sc1\n"
    "s_mov_b64 exec, -1\n"
    "v_exp_f32 v0, v0\n"
    "v_exp_f32 v1, v1\n"
    "v_mfma_f32_16x16x32_bf16 v[12:15], a[20:23], v[152:155], v[172:175]\n"
    "v_exp_f32 v2, v2\n"
    "v_cvt_pk_bf16_f32 v24, v0, v1\n"
    "v_mfma_f32_16x16x32_bf16 v[160:163], a[64:67], v[28:31], 0\n"
    "v_exp_f32 v3, v3\n"
    "v_exp_f32 v4, v4\n"
    "v_mfma_f32_16x16x32_bf16 v[164:167], a[68:71], v[28:31], 0\n"
    "v_exp_f32 v5, v5\n"
    "v_cvt_pk_bf16_f32 v25, v2, v3\n"
    "v_mfma_f32_16x16x32_bf16 v[168:171], a[128:131], v[28:31], 0\n"
    "v_exp_f32 v6, v6\n"
    "v_exp_f32 v7, v7\n"
    "v_cvt_pk_bf16_f32 v26, v4, v5\n"
    "v_cvt_pk_bf16_f32 v27, v6, v7\n"
    "v_mfma_f32_16x16x32_bf16 v[0:3], a[24:27], v[152:155], v[172:175]\n"
    "v_exp_f32 v8, v8\n"
    "v_exp_f32 v9, v9\n"
    "v_mfma_f32_16x16x32_bf16 v[4:7], a[28:31], v[152:155], v[172:175]\n"
    "v_exp_f32 v10, v10\n"
    "v_cvt_pk_bf16_f32 v28, v8, v9\n"
    "v_mfma_f32_16x16x32_bf16 v[160:163], a[72:75], v[24:27], v[160:163]\n"
    "v_exp_f32 v11, v11\n"
    "v_exp_f32 v12, v12\n"
    "v_mfma_f32_16x16x32_bf16 v[164:167], a[76:79], v[24:27], v[164:167]\n"
    "v_exp_f32 v13, v13\n"
    "v_cvt_pk_bf16_f32 v29, v10, v11\n"
    "v_mfma_f32_16x16x32_bf16 v[168:171], a[128:131], v[24:27], v[168:171]\n"
    "v_exp_f32 v14, v14\n"
    "v_exp_f32 v15, v15\n"
    "v_cvt_pk_bf16_f32 v30, v12, v13\n"
    "v_cvt_pk_bf16_f32 v31, v14, v15\n"
    "v_mfma_f32_16x16x32_bf16 v[8:11], a[32:35], v[152:155], v[172:175]\n"
    "v_exp_f32 v0, v0\n"
    "v_exp_f32 v1, v1\n"
    "v_mfma_f32_16x16x32_bf16 v[12:15], a[36:39], v[152:155], v[172:175]\n"
    "v_exp_f32 v2, v2\n"
    "v_cvt_pk_bf16_f32 v24, v0, v1\n"
    "v_mfma_f32_16x16x32_bf16 v[160:163], a[80:83], v[28:31], v[160:163]\n"
    "v_exp_f32 v3, v3\n"
    "v_exp_f32 v4, v4\n"
    "v_mfma_f32_16x16x32_bf16 v[164:167], a[84:87], v[28:31], v[164:167]\n"
    "v_exp_f32 v5, v5\n"
    "v_cvt_pk_bf16_f32 v25, v2, v3\n"
    "v_mfma_f32_16x16x32_bf16 v[168:171], a[128:131], v[28:31], v[168:171]\n"
    "v_exp_f32 v6, v6\n"
    "v_exp_f32 v7, v7\n"
    "v_cvt_pk_bf16_f32 v26, v4, v5\n"
    "v_cvt_pk_bf16_f32 v27, v6, v7\n"
    "v_pk_add_f32 v[232:233], v[224:225], v[172:173]\n"
    "v_pk_add_f32 v[234:235], v[226:227], v[172:173]\n"
    "v_pk_add_f32 v[236:237], v[228:229], v[172:173]\n"
    "v_pk_add_f32 v[238:239], v[230:231], v[172:173]\n"
    "v_mfma_f32_16x16x32_bf16 v[0:3], a[40:43], v[152:155], v[232:235]\n"
    "v_exp_f32 v8, v8\n"
    "v_exp_f32 v9, v9\n"
    "v_mfma_f32_16x16x32_bf16 v[4:7], a[44:47], v[152:155], v[236:239]\n"
    "v_exp_f32 v10, v10\n"
    "v_cvt_pk_bf16_f32 v28, v8, v9\n"
    "v_mfma_f32_16x16x32_bf16 v[160:163], a[88:91], v[24:27], v[160:163]\n"
    "v_exp_f32 v11, v11\n"
    "v_exp_f32 v12, v12\n"
    "v_mfma_f32_16x16x32_bf16 v[164:167], a[92:95], v[24:27], v[164:167]\n"
    "v_exp_f32 v13, v13\n"
    "v_cvt_pk_bf16_f32 v29, v10, v11\n"
    "v_mfma_f32_16x16x32_bf16 v[168:171], a[128:131], v[24:27], v[168:171]\n"
    "v_exp_f32 v14, v14\n"
    "v_exp_f32 v15, v15\n"
    "v_cvt_pk_bf16_f32 v30, v12, v13\n"
    "v_cvt_pk_bf16_f32 v31, v14, v15\n"
    "v_mfma_f32_16x16x32_bf16 v[8:11], a[0:3], v[176:179], v[196:199]\n"
    "v_exp_f32 v0, v0\n"
    "v_exp_f32 v1, v1\n"
    "v_mfma_f32_16x16x32_bf16 v[12:15], a[4:7], v[176:179], v[196:199]\n"
    "v_exp_f32 v2, v2\n"
    "v_cvt_pk_bf16_f32 v24, v0, v1\n"
    "v_mfma_f32_16x16x32_bf16 v[160:163], a[96:99], v[28:31], v[160:163]\n"
    "v_exp_f32 v3, v3\n"
    "v_exp_f32 v4, v4\n"
    "v_mfma_f32_16x16x32_bf16 v[164:167], a[100:103], v[28:31], v[164:167]\n"
    "v_exp_f32 v5, v5\n"
    "v_cvt_pk_bf16_f32 v25, v2, v3\n"
    "v_mfma_f32_16x16x32_bf16 v[168:171], a[128:131], v[28:31], v[168:171]\n"
    "v_exp_f32 v6, v6\n"
    "v_exp_f32 v7, v7\n"
    "v_cvt_pk_bf16_f32 v26, v4, v5\n"
    "v_cvt_pk_bf16_f32 v27, v6, v7\n"
    "v_mfma_f32_16x16x32_bf16 v[0:3], a[8:11], v[176:179], v[196:199]\n"
    "s_waitcnt vmcnt(0)\n"
    "v_exp_f32 v8, v8\n"
    "v_exp_f32 v9, v9\n"
    "v_mfma_f32_16x16x32_bf16 v[4:7], a[12:15], v[176:179], v[196:199]\n"
    "v_cmp_ne_u32 vcc, s93, v251\n"
    "v_exp_f32 v10, v10\n"
    "v_cvt_pk_bf16_f32 v28, v8, v9\n"
    "v_mfma_f32_16x16x32_bf16 v[160:163], a[104:107], v[24:27], v[160:163]\n"
    "s_cmp_eq_u64 vcc, 0\n"
    "s_cbranch_scc0 .Lk2_kslow\n"
    ".Lk2_kok:\n"
    "v_exp_f32 v11, v11\n"
    "v_exp_f32 v12, v12\n"
    "v_mfma_f32_16x16x32_bf16 v[164:167], a[108:111], v[24:27], v[164:167]\n"
    "v_exp_f32 v13, v13\n"
    "v_cvt_pk_bf16_f32 v29, v10, v11\n"
    "v_mfma_f32_16x16x32_bf16 v[168:171], a[128:131], v[24:27], v[168:171]\n"
    "v_exp_f32 v14, v14\n"
    "v_exp_f32 v15, v15\n"
    "v_cvt_pk_bf16_f32 v30, v12, v13\n"
    "v_cvt_pk_bf16_f32 v31, v14, v15\n"
    "v_mfma_f32_16x16x32_bf16 v[8:11], a[16:19], v[176:179], v[196:199]\n"
    "s_add_u32 m0, s90, 12288\n"
    "s_add_u32 s94, s34, 12288\n"
    "v_exp_f32 v0, v0\n"
    "v_exp_f32 v1, v1\n"
    "v_mfma_f32_16x16x32_bf16 v[12:15], a[20:23], v[176:179], v[196:199]\n"
    "buffer_load_dwordx4 v250, s[44:47], s94 offen sc1 lds\n"
    "v_exp_f32 v2, v2\n"
    "v_cvt_pk_bf16_f32 v24, v0, v1\n"
    "v_mfma_f32_16x16x32_bf16 v[184:187], a[64:67], v[28:31], 0\n"
    "buffer_load_dwordx4 v250, s[44:47], s94 offen offset:1024 sc1 lds\n"
    "v_exp_f32 v3, v3\n"
    "v_exp_f32 v4, v4\n"
    "v_mfma_f32_16x16x32_bf16 v[188:191], a[68:71], v[28:31], 0\n"
    "v_exp_f32 v5, v5\n"
    "v_cvt_pk_bf16_f32 v25, v2, v3\n"
    "v_mfma_f32_16x16x32_bf16 v[192:195], a[128:131], v[28:31], 0\n"
    "v_exp_f32 v6, v6\n"
    "v_exp_f32 v7, v7\n"
    "v_cvt_pk_bf16_f32 v26, v4, v5\n"
    "v_cvt_pk_bf16_f32 v27, v6, v7\n"
    "v_mfma_f32_16x16x32_bf16 v[0:3], a[24:27], v[176:179], v[196:199]\n"
    "buffer_load_dwordx4 v250, s[44:47], s94 offen offset:2048 sc1 lds\n"
    "v_exp_f32 v8, v8\n"
    "v_exp_f32 v9, v9\n"
    "v_mfma_f32_16x16x32_bf16 v[4:7], a[28:31], v[176:179], v[196:199]\n"
    "buffer_load_dwordx4 v250, s[44:47], s94 offen offset:3072 sc1 lds\n"
    "v_exp_f32 v10, v10\n"
    "v_cvt_pk_bf16_f32 v28, v8, v9\n"
    "v_mfma_f32_16x16x32_bf16 v[184:187], a[72:75], v[24:27], v[184:187]\n"
    "v_exp_f32 v11, v11\n"
    "v_exp_f32 v12, v12\n"
    "v_mfma_f32_16x16x32_bf16 v[188:191], a[76:79], v[24:27], v[188:191]\n"
    "v_exp_f32 v13, v13\n"
    "v_cvt_pk_bf16_f32 v29, v10, v11\n"
    "v_mfma_f32_16x16x32_bf16 v[192:195], a[128:131], v[24:27], v[192:195]\n"
    "v_exp_f32 v14, v14\n"
    "v_exp_f32 v15, v15\n"
    "v_cvt_pk_bf16_f32 v30, v12, v13\n"
    "v_cvt_pk_bf16_f32 v31, v14, v15\n"
    "v_mfma_f32_16x16x32_bf16 v[8:11], a[32:35], v[176:179], v[196:199]\n"
    "s_add_u32 m0, s90, 8192\n"
    "s_add_u32 s94, s34, 8192\n"
    "v_exp_f32 v0, v0\n"
    "v_exp_f32 v1, v1\n"
    "v_mfma_f32_16x16x32_bf16 v[12:15], a[36:39], v[176:179], v[196:199]\n"
    "buffer_load_dwordx4 v250, s[44:47], s94 offen sc1 lds\n"
    "v_exp_f32 v2, v2\n"
    "v_cvt_pk_bf16_f32 v24, v0, v1\n"
    "v_mfma_f32_16x16x32_bf16 v[184:187], a[80:83], v[28:31], v[184:187]\n"
    "buffer_load_dwordx4 v250, s[44:47], s94 offen offset:1024 sc1 lds\n"
    "v_exp_f32 v3, v3\n"
    "v_exp_f32 v4, v4\n"
    "v_mfma_f32_16x16x32_bf16 v[188:191], a[84:87], v[28:31], v[188:191]\n"
    "v_exp_f32 v5, v5\n"
    "v_cvt_pk_bf16_f32 v25, v2, v3\n"
    "v_mfma_f32_16x16x32_bf16 v[192:195], a[128:131], v[28:31], v[192:195]\n"
    "v_exp_f32 v6, v6\n"
    "v_exp_f32 v7, v7\n"
    "v_cvt_pk_bf16_f32 v26, v4, v5\n"
    "v_cvt_pk_bf16_f32 v27, v6, v7\n"
    "v_mfma_f32_16x16x32_bf16 v[0:3], a[40:43], v[176:179], v[196:199]\n"
    "buffer_load_dwordx4 v250, s[44:47], s94 offen offset:2048 sc1 lds\n"
    "v_exp_f32 v8, v8\n"
    "v_exp_f32 v9, v9\n"
    "v_mfma_f32_16x16x32_bf16 v[4:7], a[44:47], v[176:179], v[196:199]\n"
    "buffer_load_dwordx4 v250, s[44:47], s94 offen offset:3072 sc1 lds\n"
    "v_exp_f32 v10, v10\n"
    "v_cvt_pk_bf16_f32 v28, v8, v9\n"
    "v_mfma_f32_16x16x32_bf16 v[184:187], a[88:91], v[24:27], v[184:187]\n"
    "v_exp_f32 v11, v11\n"
    "v_exp_f32 v12, v12\n"
    "v_mfma_f32_16x16x32_bf16 v[188:191], a[92:95], v[24:27], v[188:191]\n"
    "v_exp_f32 v13, v13\n"
    "v_cvt_pk_bf16_f32 v29, v10, v11\n"
    "v_mfma_f32_16x16x32_bf16 v[192:195], a[128:131], v[24:27], v[192:195]\n"
    "v_exp_f32 v14, v14\n"
    "v_exp_f32 v15, v15\n"
    "v_cvt_pk_bf16_f32 v30, v12, v13\n"
    "v_cvt_pk_bf16_f32 v31, v14, v15\n"
    "v_pk_add_f32 v[232:233], v[224:225], v[196:197]\n"
    "v_pk_add_f32 v[234:235], v[226:227], v[196:197]\n"
    "v_pk_add_f32 v[236:237], v[228:229], v[196:197]\n"
    "v_pk_add_f32 v[238:239], v[230:231], v[196:197]\n"
    "v_mfma_f32_16x16x32_bf16 v[8:11], a[48:51], v[176:179], v[232:235]\n"
    "s_add_u32 m0, s90, 4096\n"
    "s_add_u32 s94, s34, 4096\n"
    "v_exp_f32 v0, v0\n"
    "v_exp_f32 v1, v1\n"
    "v_mfma_f32_16x16x32_bf16 v[12:15], a[52:55], v[176:179], v[236:239]\n"
    "buffer_load_dwordx4 v250, s[44:47], s94 offen sc1 lds\n"
    "v_exp_f32 v2, v2\n"
    "v_cvt_pk_bf16_f32 v24, v0, v1\n"
    "v_mfma_f32_16x16x32_bf16 v[184:187], a[96:99], v[28:31], v[184:187]\n"
    "v_exp_f32 v3, v3\n"
    "v_exp_f32 v4, v4\n"
    "v_mfma_f32_16x16x32_bf16 v[188:191], a[100:103], v[28:31], v[188:191]\n"
    "v_exp_f32 v5, v5\n"
    "v_cvt_pk_bf16_f32 v25, v2, v3\n"
    "v_mfma_f32_16x16x32_bf16 v[192:195], a[128:131], v[28:31], v[192:195]\n"
    "v_exp_f32 v6, v6\n"
    "v_exp_f32 v7, v7\n"
    "v_cvt_pk_bf16_f32 v26, v4, v5\n"
    "v_cvt_pk_bf16_f32 v27, v6, v7\n"
    "v_mfma_f32_16x16x32_bf16 v[0:3], a[0:3], v[200:203], v[220:223]\n"
    "buffer_load_dwordx4 v250, s[44:47], s94 offen offset:1024 sc1 lds\n"
    "v_exp_f32 v8, v8\n"
    "v_exp_f32 v9, v9\n"
    "v_mfma_f32_16x16x32_bf16 v[4:7], a[4:7], v[200:203], v[220:223]\n"
    "v_exp_f32 v10, v10\n"
    "v_cvt_pk_bf16_f32 v28, v8, v9\n"
    "v_mfma_f32_16x16x32_bf16 v[184:187], a[104:107], v[24:27], v[184:187]\n"
    "v_exp_f32 v11, v11\n"
    "v_exp_f32 v12, v12\n"
    "v_mfma_f32_16x16x32_bf16 v[188:191], a[108:111], v[24:27], v[188:191]\n"
    "v_exp_f32 v13, v13\n"
    "v_cvt_pk_bf16_f32 v29, v10, v11\n"
    "v_mfma_f32_16x16x32_bf16 v[192:195], a[128:131], v[24:27], v[192:195]\n"
    "v_exp_f32 v14, v14\n"
    "v_exp_f32 v15, v15\n"
    "v_cvt_pk_bf16_f32 v30, v12, v13\n"
    "v_cvt_pk_bf16_f32 v31, v14, v15\n"
    "v_mfma_f32_16x16x32_bf16 v[8:11], a[8:11], v[200:203], v[220:223]\n"
    "buffer_load_dwordx4 v250, s[44:47], s94 offen offset:2048 sc1 lds\n"
    "v_exp_f32 v0, v0\n"
    "v_exp_f32 v1, v1\n"
    "v_mfma_f32_16x16x32_bf16 v[12:15], a[12:15], v[200:203], v[220:223]\n"
    "v_exp_f32 v2, v2\n"
    "v_cvt_pk_bf16_f32 v24, v0, v1\n"
    "v_mfma_f32_16x16x32_bf16 v[184:187], a[112:115], v[28:31], v[184:187]\n"
    "v_exp_f32 v3, v3\n"
    "v_exp_f32 v4, v4\n"
    "s_waitcnt lgkmcnt(3)\n"
    "v_mfma_f32_16x16x32_bf16 v[188:191], a[116:119], v[28:31], v[188:191]\n"
    "v_exp_f32 v5, v5\n"
    "v_cvt_pk_bf16_f32 v25, v2, v3\n"
    "v_mfma_f32_16x16x32_bf16 v[192:195], a[128:131], v[28:31], v[192:195]\n"
    "v_exp_f32 v6, v6\n"
    "v_exp_f32 v7, v7\n"
    "v_cvt_pk_bf16_f32 v26, v4, v5\n"
    "v_cvt_pk_bf16_f32 v27, v6, v7\n"
    "v_mfma_f32_16x16x32_bf16 v[0:3], a[16:19], v[200:203], v[220:223]\n"
    "buffer_load_dwordx4 v250, s[44:47], s94 offen offset:3072 sc1 lds\n"
    "v_exp_f32 v8, v8\n"
    "v_exp_f32 v9, v9\n"
    "v_mfma_f32_16x16x32_bf16 v[4:7], a[20:23], v[200:203], v[220:223]\n"
    "v_exp_f32 v10, v10\n"
    "v_cvt_pk_bf16_f32 v28, v8, v9\n"
    "v_mfma_f32_16x16x32_bf16 v[208:211], a[64:67], v[24:27], 0\n"
    "v_exp_f32 v11, v11\n"
    "v_exp_f32 v12, v12\n"
    "v_mfma_f32_16x16x32_bf16 v[212:215], a[68:71], v[24:27], 0\n"
    "v_exp_f32 v13, v13\n"
    "v_cvt_pk_bf16_f32 v29, v10, v11\n"
    "v_mfma_f32_16x16x32_bf16 v[216:219], a[128:131], v[24:27], 0\n"
    "v_exp_f32 v14, v14\n"
    "v_exp_f32 v15, v15\n"
    "v_cvt_pk_bf16_f32 v30, v12, v13\n"
    "v_cvt_pk_bf16_f32 v31, v14, v15\n"
    "v_mfma_f32_16x16x32_bf16 v[8:11], a[24:27], v[200:203], v[220:223]\n"
    "v_exp_f32 v0, v0\n"
    "v_exp_f32 v1, v1\n"
    "v_mfma_f32_16x16x32_bf16 v[12:15], a[28:31], v[200:203], v[220:223]\n"
    "v_exp_f32 v2, v2\n"
    "v_cvt_pk_bf16_f32 v24, v0, v1\n"
    "v_mfma_f32_16x16x32_bf16 v[208:211], a[72:75], v[28:31], v[208:211]\n"
    "v_exp_f32 v3, v3\n"
    "v_exp_f32 v4, v4\n"
    "v_mfma_f32_16x16x32_bf16 v[212:215], a[76:79], v[28:31], v[212:215]\n"
    "v_exp_f32 v5, v5\n"
    "v_cvt_pk_bf16_f32 v25, v2, v3\n"
    "v_mfma_f32_16x16x32_bf16 v[216:219], a[128:131], v[28:31], v[216:219]\n"
    "v_exp_f32 v6, v6\n"
    "v_exp_f32 v7, v7\n"
    "v_cvt_pk_bf16_f32 v26, v4, v5\n"
    "v_cvt_pk_bf16_f32 v27, v6, v7\n"
    "v_mfma_f32_16x16x32_bf16 v[0:3], a[32:35], v[200:203], v[220:223]\n"
    "s_add_u32 m0, s90, 0\n"
    "s_add_u32 s94, s34, 0\n"
    "v_exp_f32 v8, v8\n"
    "v_exp_f32 v9, v9\n"
    "v_mfma_f32_16x16x32_bf16 v[4:7], a[36:39], v[200:203], v[220:223]\n"
    "buffer_load_dwordx4 v250, s[44:47], s94 offen sc1 lds\n"
    "v_exp_f32 v10, v10\n"
    "v_cvt_pk_bf16_f32 v28, v8, v9\n"
    "v_mfma_f32_16x16x32_bf16 v[208:211], a[80:83], v[24:27], v[208:211]\n"
    "v_add_u32 v244, 0x10000, v244\n"
    "v_exp_f32 v11, v11\n"
    "v_exp_f32 v12, v12\n"
    "v_mfma_f32_16x16x32_bf16 v[212:215], a[84:87], v[24:27], v[212:215]\n"
    "v_add_u32 v245, 0x10000, v245\n"
    "v_exp_f32 v13, v13\n"
    "v_cvt_pk_bf16_f32 v29, v10, v11\n"
    "v_mfma_f32_16x16x32_bf16 v[216:219], a[128:131], v[24:27], v[216:219]\n"
    "v_add_u32 v246, 0x10000, v246\n"
    "v_exp_f32 v14, v14\n"
    "v_exp_f32 v15, v15\n"
    "v_cvt_pk_bf16_f32 v30, v12, v13\n"
    "v_cvt_pk_bf16_f32 v31, v14, v15\n"
    "v_mfma_f32_16x16x32_bf16 v[8:11], a[40:43], v[200:203], v[220:223]\n"
    "buffer_load_dwordx4 v250, s[44:47], s94 offen offset:1024 sc1 lds\n"
    "v_exp_f32 v0, v0\n"
    "v_exp_f32 v1, v1\n"
    "v_mfma_f32_16x16x32_bf16 v[12:15], a[44:47], v[200:203], v[220:223]\n"
    "s_waitcnt vmcnt(6)\n"
    "s_bitcmp1_b32 s97, 0\n"
    "s_cbranch_scc1 .Lk2_nb0\n"
    "s_barrier\n"
    ".Lk2_nb0:\n"
    "v_exp_f32 v2, v2\n"
    "v_cvt_pk_bf16_f32 v24, v0, v1\n"
    "v_mfma_f32_16x16x32_bf16 v[208:211], a[88:91], v[28:31], v[208:211]\n"
    "ds_read_b128 a[0:3], v244 offset:14336\n"
    "v_exp_f32 v3, v3\n"
    "v_exp_f32 v4, v4\n"
    "v_mfma_f32_16x16x32_bf16 v[212:215], a[92:95], v[28:31], v[212:215]\n"
    "ds_read_b128 a[4:7], v244 offset:15360\n"
    "v_exp_f32 v5, v5\n"
    "v_cvt_pk_bf16_f32 v25, v2, v3\n"
    "v_mfma_f32_16x16x32_bf16 v[216:219], a[128:131], v[28:31], v[216:219]\n"
    "ds_read_b128 a[8:11], v244 offset:12288\n"
    "v_exp_f32 v6, v6\n"
    "v_exp_f32 v7, v7\n"
    "v_cvt_pk_bf16_f32 v26, v4, v5\n"
    "v_cvt_pk_bf16_f32 v27, v6, v7\n"
    "v_mfma_f32_16x16x32_bf16 v[0:3], a[48:51], v[200:203], v[220:223]\n"
    "ds_read_b128 a[12:15], v244 offset:13312\n"
    "v_exp_f32 v8, v8\n"
    "v_exp_f32 v9, v9\n"
    "v_mfma_f32_16x16x32_bf16 v[4:7], a[52:55], v[200:203], v[220:223]\n"
    "buffer_load_dwordx4 v250, s[44:47], s94 offen offset:2048 sc1 lds\n"
    "v_exp_f32 v10, v10\n"
    "v_cvt_pk_bf16_f32 v28, v8, v9\n"
    "v_mfma_f32_16x16x32_bf16 v[208:211], a[96:99], v[24:27], v[208:211]\n"
    "ds_read_b64_tr_b16 a[64:65], v245 offset:14336\n"
    "v_exp_f32 v11, v11\n"
    "v_exp_f32 v12, v12\n"
    "v_mfma_f32_16x16x32_bf16 v[212:215], a[100:103], v[24:27], v[212:215]\n"
    "ds_read_b64_tr_b16 a[66:67], v245 offset:15360\n"
    "v_exp_f32 v13, v13\n"
    "v_cvt_pk_bf16_f32 v29, v10, v11\n"
    "v_mfma_f32_16x16x32_bf16 v[216:219], a[128:131], v[24:27], v[216:219]\n"
    "ds_read_b64_tr_b16 a[68:69], v246 offset:14336\n"
    "v_exp_f32 v14, v14\n"
    "v_exp_f32 v15, v15\n"
    "v_cvt_pk_bf16_f32 v30, v12, v13\n"
    "v_cvt_pk_bf16_f32 v31, v14, v15\n"
    "v_pk_add_f32 v[232:233], v[224:225], v[220:221]\n"
    "v_pk_add_f32 v[234:235], v[226:227], v[220:221]\n"
    "v_pk_add_f32 v[236:237], v[228:229], v[220:221]\n"
    "v_pk_add_f32 v[238:239], v[230:231], v[220:221]\n"
    "v_mfma_f32_16x16x32_bf16 v[8:11], a[56:59], v[200:203], v[232:235]\n"
    "ds_read_b64_tr_b16 a[70:71], v246 offset:15360\n"
    "v_exp_f32 v0, v0\n"
    "v_exp_f32 v1, v1\n"
    "v_mfma_f32_16x16x32_bf16 v[12:15], a[60:63], v[200:203], v[236:239]\n"
    "buffer_load_dwordx4 v250, s[44:47], s94 offen offset:3072 sc1 lds\n"
    "v_exp_f32 v2, v2\n"
    "v_cvt_pk_bf16_f32 v24, v0, v1\n"
    "v_mfma_f32_16x16x32_bf16 v[208:211], a[104:107], v[28:31], v[208:211]\n"
    "ds_read_b64_tr_b16 a[72:73], v245 offset:12288\n"
    "v_exp_f32 v3, v3\n"
    "v_exp_f32 v4, v4\n"
    "v_mfma_f32_16x16x32_bf16 v[212:215], a[108:111], v[28:31], v[212:215]\n"
    "ds_read_b64_tr_b16 a[74:75], v245 offset:13312\n"
    "v_exp_f32 v5, v5\n"
    "v_cvt_pk_bf16_f32 v25, v2, v3\n"
    "v_mfma_f32_16x16x32_bf16 v[216:219], a[128:131], v[28:31], v[216:219]\n"
    "ds_read_b64_tr_b16 a[76:77], v246 offset:12288\n"
    "v_exp_f32 v6, v6\n"
    "v_exp_f32 v7, v7\n"
    "v_cvt_pk_bf16_f32 v26, v4, v5\n"
    "v_cvt_pk_bf16_f32 v27, v6, v7\n"
    "s_mov_b32 s33, 0xf149f2ca\n"
    "v_sub_f32 v224, s33, v224\n"
    "v_sub_f32 v225, s33, v225\n"
    "v_sub_f32 v226, s33, v226\n"
    "v_sub_f32 v227, s33, v227\n"
    "v_sub_f32 v228, s33, v228\n"
    "v_sub_f32 v229, s33, v229\n"
    "v_sub_f32 v230, s33, v230\n"
    "v_sub_f32 v231, s33, v231\n"
    "s_cmp_eq_u32 s57, 0\n"
    "s_cbranch_scc1 .Lk2_q0\n"
    ".Lk2_q1:\n"
    "v_pk_add_f32 v[232:233], v[224:225], v[220:221]\n"
    "v_pk_add_f32 v[234:235], v[226:227], v[220:221]\n"
    "v_pk_add_f32 v[236:237], v[228:229], v[220:221]\n"
    "v_pk_add_f32 v[238:239], v[230:231], v[220:221]\n"
    "s_waitcnt lgkmcnt(3)\n"
    "v_mfma_f32_16x16x32_bf16 v[0:3], a[0:3], v[200:203], v[232:235]\n"
    "ds_read_b64_tr_b16 a[78:79], v246 offset:13312\n"
    "v_exp_f32 v8, v8\n"
    "v_exp_f32 v9, v9\n"
    "v_mfma_f32_16x16x32_bf16 v[4:7], a[4:7], v[200:203], v[236:239]\n"
    "ds_read_b128 a[16:19], v244 offset:10240\n"
    "v_exp_f32 v10, v10\n"
    "v_cvt_pk_bf16_f32 v28, v8, v9\n"
    "v_mfma_f32_16x16x32_bf16 v[208:211], a[112:115], v[24:27], v[208:211]\n"
    "ds_read_b128 a[20:23], v244 offset:11264\n"
    "v_exp_f32 v11, v11\n"
    "v_exp_f32 v12, v12\n"
    "v_mfma_f32_16x16x32_bf16 v[212:215], a[116:119], v[24:27], v[212:215]\n"
    "ds_read_b64_tr_b16 a[80:81], v245 offset:10240\n"
    "v_exp_f32 v13, v13\n"
    "v_cvt_pk_bf16_f32 v29, v10, v11\n"
    "v_mfma_f32_16x16x32_bf16 v[216:219], a[128:131], v[24:27], v[216:219]\n"
    "ds_read_b64_tr_b16 a[82:83], v245 offset:11264\n"
    "v_exp_f32 v14, v14\n"
    "v_exp_f32 v15, v15\n"
    "v_cvt_pk_bf16_f32 v30, v12, v13\n"
    "v_cvt_pk_bf16_f32 v31, v14, v15\n"
    "v_pk_add_f32 v[232:233], v[224:225], v[196:197]\n"
    "v_pk_add_f32 v[234:235], v[226:227], v[196:197]\n"
    "v_pk_add_f32 v[236:237], v[228:229], v[196:197]\n"
    "v_pk_add_f32 v[238:239], v[230:231], v[196:197]\n"
    "v_mfma_f32_16x16x32_bf16 v[8:11], a[8:11], v[176:179], v[232:235]\n"
    "ds_read_b64_tr_b16 a[84:85], v246 offset:10240\n"
    "v_exp_f32 v0, v0\n"
    "v_exp_f32 v1, v1\n"
    "v_mfma_f32_16x16x32_bf16 v[12:15], a[12:15], v[176:179], v[236:239]\n"
    "ds_read_b64_tr_b16 a[86:87], v246 offset:11264\n"
    "v_exp_f32 v2, v2\n"
    "v_cvt_pk_bf16_f32 v24, v0, v1\n"
    "v_mfma_f32_16x16x32_bf16 v[208:211], a[120:123], v[28:31], v[208:211]\n"
    "v_exp_f32 v3, v3\n"
    "v_exp_f32 v4, v4\n"
    "v_mfma_f32_16x16x32_bf16 v[212:215], a[124:127], v[28:31], v[212:215]\n"
    "v_exp_f32 v5, v5\n"
    "v_cvt_pk_bf16_f32 v25, v2, v3\n"
    "v_mfma_f32_16x16x32_bf16 v[216:219], a[128:131], v[28:31], v[216:219]\n"
    "v_exp_f32 v6, v6\n"
    "v_exp_f32 v7, v7\n"
    "v_cvt_pk_bf16_f32 v26, v4, v5\n"
    "v_cvt_pk_bf16_f32 v27, v6, v7\n"
    "v_max3_f32 v255, v48, v72, v96\n"
    "v_min3_f32 v254, v48, v72, v96\n"
    "v_max3_f32 v255, v255, v120, v144\n"
    "v_min3_f32 v254, v254, v120, v144\n"
    "v_max3_f32 v255, v255, v168, v192\n"
    "v_min3_f32 v254, v254, v168, v192\n"
    "v_min_f32 v254, v254, v216\n"
    "v_rcp_f32 v254, v254\n"
    "v_max_f32 v255, v255, v216\n"
    "v_max_f32 v254, v254, v255\n"
    "v_cmp_ngt_f32 vcc, s96, v254\n"
    "s_nop 0\n"
    "s_cmp_eq_u64 vcc, 0\n"
    "s_cbranch_scc0 .Lk2_fl\n"
    ".Lk2_xr:\n"
    "v_mfma_f32_16x16x32_bf16 v[0:3], a[0:3], v[176:179], v[196:199]\n"
    "v_exp_f32 v8, v8\n"
    "v_exp_f32 v9, v9\n"
    "v_mfma_f32_16x16x32_bf16 v[4:7], a[4:7], v[176:179], v[196:199]\n"
    "v_exp_f32 v10, v10\n"
    "v_cvt_pk_bf16_f32 v28, v8, v9\n"
    "v_mfma_f32_16x16x32_bf16 v[16:19], a[64:67], v[24:27], v[208:211]\n"
    "v_exp_f32 v11, v11\n"
    "v_exp_f32 v12, v12\n"
    "v_mfma_f32_16x16x32_bf16 v[20:23], a[68:71], v[24:27], v[212:215]\n"
    "v_exp_f32 v13, v13\n"
    "v_cvt_pk_bf16_f32 v29, v10, v11\n"
    "v_mfma_f32_16x16x32_bf16 v[240:243], a[128:131], v[24:27], v[216:219]\n"
    "v_exp_f32 v14, v14\n"
    "v_exp_f32 v15, v15\n"
    "v_cvt_pk_bf16_f32 v30, v12, v13\n"
    "v_cvt_pk_bf16_f32 v31, v14, v15\n"
    "v_pk_add_f32 v[232:233], v[224:225], v[172:173]\n"
    "v_pk_add_f32 v[234:235], v[226:227], v[172:173]\n"
    "v_pk_add_f32 v[236:237], v[228:229], v[172:173]\n"
    "v_pk_add_f32 v[238:239], v[230:231], v[172:173]\n"
    "s_waitcnt lgkmcnt(3)\n"
    "v_mfma_f32_16x16x32_bf16 v[8:11], a[16:19], v[152:155], v[232:235]\n"
    "ds_read_b128 a[24:27], v244 offset:8192\n"
    "v_exp_f32 v0, v0\n"
    "v_exp_f32 v1, v1\n"
    "v_mfma_f32_16x16x32_bf16 v[12:15], a[20:23], v[152:155], v[236:239]\n"
    "ds_read_b128 a[28:31], v244 offset:9216\n"
    "v_exp_f32 v2, v2\n"
    "v_cvt_pk_bf16_f32 v24, v0, v1\n"
    "v_rcp_f32 v248, v240\n"
    "v_max_f32 v255, v255, v240\n"
    "v_pk_mul_f32 v[16:17], v[16:17], v[248:249] op_sel_hi:[1,0]\n"
    "v_pk_mul_f32 v[18:19], v[18:19], v[248:249] op_sel_hi:[1,0]\n"
    "v_pk_mul_f32 v[20:21], v[20:21], v[248:249] op_sel_hi:[1,0]\n"
    "v_pk_mul_f32 v[22:23], v[22:23], v[248:249] op_sel_hi:[1,0]\n"
    "v_cvt_pk_bf16_f32 v204, v16, v17\n"
    "v_cvt_pk_bf16_f32 v205, v18, v19\n"
    "v_cvt_pk_bf16_f32 v206, v20, v21\n"
    "v_cvt_pk_bf16_f32 v207, v22, v23\n"
    "buffer_store_dwordx4 v[204:207], v250, s[44:47], s29 offen offset:3072 sc1\n"
    "v_mfma_f32_16x16x32_bf16 v[16:19], a[72:75], v[28:31], v[184:187]\n"
    "ds_read_b64_tr_b16 a[88:89], v245 offset:8192\n"
    "v_exp_f32 v3, v3\n"
    "v_exp_f32 v4, v4\n"
    "v_mfma_f32_16x16x32_bf16 v[20:23], a[76:79], v[28:31], v[188:191]\n"
    "ds_read_b64_tr_b16 a[90:91], v245 offset:9216\n"
    "v_exp_f32 v5, v5\n"
    "v_cvt_pk_bf16_f32 v25, v2, v3\n"
    "v_mfma_f32_16x16x32_bf16 v[240:243], a[128:131], v[28:31], v[192:195]\n"
    "ds_read_b64_tr_b16 a[92:93], v246 offset:8192\n"
    "v_exp_f32 v6, v6\n"
    "v_exp_f32 v7, v7\n"
    "v_cvt_pk_bf16_f32 v26, v4, v5\n"
    "v_cvt_pk_bf16_f32 v27, v6, v7\n"
    "v_mfma_f32_16x16x32_bf16 v[0:3], a[8:11], v[152:155], v[172:175]\n"
    "ds_read_b64_tr_b16 a[94:95], v246 offset:9216\n"
    "v_exp_f32 v8, v8\n"
    "v_exp_f32 v9, v9\n"
    "v_mfma_f32_16x16x32_bf16 v[4:7], a[12:15], v[152:155], v[172:175]\n"
    "v_exp_f32 v10, v10\n"
    "v_cvt_pk_bf16_f32 v28, v8, v9\n"
    "v_mfma_f32_16x16x32_bf16 v[16:19], a[64:67], v[24:27], v[16:19]\n"
    "v_exp_f32 v11, v11\n"
    "v_exp_f32 v12, v12\n"
    "v_mfma_f32_16x16x32_bf16 v[20:23], a[68:71], v[24:27], v[20:23]\n"
    "v_exp_f32 v13, v13\n"
    "v_cvt_pk_bf16_f32 v29, v10, v11\n"
    "v_mfma_f32_16x16x32_bf16 v[240:243], a[128:131], v[24:27], v[240:243]\n"
    "v_exp_f32 v14, v14\n"
    "v_exp_f32 v15, v15\n"
    "v_cvt_pk_bf16_f32 v30, v12, v13\n"
    "v_cvt_pk_bf16_f32 v31, v14, v15\n"
    "v_mfma_f32_16x16x32_bf16 v[8:11], a[0:3], v[152:155], v[172:175]\n"
    "v_exp_f32 v0, v0\n"
    "v_exp_f32 v1, v1\n"
    "v_mfma_f32_16x16x32_bf16 v[12:15], a[4:7], v[152:155], v[172:175]\n"
    "v_exp_f32 v2, v2\n"
    "v_cvt_pk_bf16_f32 v24, v0, v1\n"
    "v_rcp_f32 v248, v240\n"
    "v_max_f32 v255, v255, v240\n"
    "v_pk_mul_f32 v[16:17], v[16:17], v[248:249] op_sel_hi:[1,0]\n"
    "v_pk_mul_f32 v[18:19], v[18:19], v[248:249] op_sel_hi:[1,0]\n"
    "v_pk_mul_f32 v[20:21], v[20:21], v[248:249] op_sel_hi:[1,0]\n"
    "v_pk_mul_f32 v[22:23], v[22:23], v[248:249] op_sel_hi:[1,0]\n"
    "v_cvt_pk_bf16_f32 v180, v16, v17\n"
    "v_cvt_pk_bf16_f32 v181, v18, v19\n"
    "v_cvt_pk_bf16_f32 v182, v20, v21\n"
    "v_cvt_pk_bf16_f32 v183, v22, v23\n"
    "buffer_store_dwordx4 v[180:183], v250, s[44:47], s29 offen offset:2048 sc1\n"
    "s_waitcnt lgkmcnt(3)\n"
    "v_mfma_f32_16x16x32_bf16 v[16:19], a[80:83], v[28:31], v[160:163]\n"
    "v_exp_f32 v3, v3\n"
    "v_exp_f32 v4, v4\n"
    "v_mfma_f32_16x16x32_bf16 v[20:23], a[84:87], v[28:31], v[164:167]\n"
    "v_exp_f32 v5, v5\n"
    "v_cvt_pk_bf16_f32 v25, v2, v3\n"
    "v_mfma_f32_16x16x32_bf16 v[240:243], a[128:131], v[28:31], v[168:171]\n"
    "v_exp_f32 v6, v6\n"
    "v_exp_f32 v7, v7\n"
    "v_cvt_pk_bf16_f32 v26, v4, v5\n"
    "v_cvt_pk_bf16_f32 v27, v6, v7\n"
    "v_pk_add_f32 v[232:233], v[224:225], v[148:149]\n"
    "v_pk_add_f32 v[234:235], v[226:227], v[148:149]\n"
    "v_pk_add_f32 v[236:237], v[228:229], v[148:149]\n"
    "v_pk_add_f32 v[238:239], v[230:231], v[148:149]\n"
    "v_mfma_f32_16x16x32_bf16 v[0:3], a[24:27], v[128:131], v[232:235]\n"
    "v_exp_f32 v8, v8\n"
    "v_exp_f32 v9, v9\n"
    "v_mfma_f32_16x16x32_bf16 v[4:7], a[28:31], v[128:131], v[236:239]\n"
    "v_exp_f32 v10, v10\n"
    "v_cvt_pk_bf16_f32 v28, v8, v9\n"
    "v_mfma_f32_16x16x32_bf16 v[16:19], a[72:75], v[24:27], v[16:19]\n"
    ".Lk2_m6104:\n"
    "s_cbranch_execz .Lk2_m8172\n"
    "v_exp_f32 v11, v11\n"
    "v_exp_f32 v12, v12\n"
    "v_mfma_f32_16x16x32_bf16 v[20:23], a[76:79], v[24:27], v[20:23]\n"
    "v_exp_f32 v13, v13\n"
    "v_cvt_pk_bf16_f32 v29, v10, v11\n"
    "v_mfma_f32_16x16x32_bf16 v[240:243], a[128:131], v[24:27], v[240:243]\n"
    "v_exp_f32 v14, v14\n"
    "v_exp_f32 v15, v15\n"
    "v_cvt_pk_bf16_f32 v30, v12, v13\n"
    "v_cvt_pk_bf16_f32 v31, v14, v15\n"
    "v_mfma_f32_16x16x32_bf16 v[8:11], a[16:19], v[128:131], v[148:151]\n"
    "s_waitcnt vmcnt(2)\n"
    "s_bitcmp1_b32 s97, 1\n"
    "s_cbranch_scc1 .Lk2_nb1\n"
    "s_barrier\n"
    ".Lk2_nb1:\n"
    "v_exp_f32 v0, v0\n"
    "v_exp_f32 v1, v1\n"
    "v_mfma_f32_16x16x32_bf16 v[12:15], a[20:23], v[128:131], v[148:151]\n"
    "ds_read_b128 a[32:35], v244 offset:6144\n"
    "v_exp_f32 v2, v2\n"
    "v_cvt_pk_bf16_f32 v24, v0, v1\n"
    "v_mfma_f32_16x16x32_bf16 v[16:19], a[64:67], v[28:31], v[16:19]\n"
    "ds_read_b128 a[36:39], v244 offset:7168\n"
    "v_exp_f32 v3, v3\n"
    "v_exp_f32 v4, v4\n"
    "v_mfma_f32_16x16x32_bf16 v[20:23], a[68:71], v[28:31], v[20:23]\n"
    "ds_read_b64_tr_b16 a[96:97], v245 offset:6144\n"
    "v_exp_f32 v5, v5\n"
    "v_cvt_pk_bf16_f32 v25, v2, v3\n"
    "v_mfma_f32_16x16x32_bf16 v[240:243], a[128:131], v[28:31], v[240:243]\n"
    "ds_read_b64_tr_b16 a[98:99], v245 offset:7168\n"
    "v_exp_f32 v6, v6\n"
    "v_exp_f32 v7, v7\n"
    "v_cvt_pk_bf16_f32 v26, v4, v5\n"
    "v_cvt_pk_bf16_f32 v27, v6, v7\n"
    "v_mfma_f32_16x16x32_bf16 v[0:3], a[8:11], v[128:131], v[148:151]\n"
    "ds_read_b64_tr_b16 a[100:101], v246 offset:6144\n"
    "v_exp_f32 v8, v8\n"
    "v_exp_f32 v9, v9\n"
    "v_mfma_f32_16x16x32_bf16 v[4:7], a[12:15], v[128:131], v[148:151]\n"
    "ds_read_b64_tr_b16 a[102:103], v246 offset:7168\n"
    "v_exp_f32 v10, v10\n"
    "v_cvt_pk_bf16_f32 v28, v8, v9\n"
    "v_rcp_f32 v248, v240\n"
    "v_max_f32 v255, v255, v240\n"
    "v_pk_mul_f32 v[16:17], v[16:17], v[248:249] op_sel_hi:[1,0]\n"
    "v_pk_mul_f32 v[18:19], v[18:19], v[248:249] op_sel_hi:[1,0]\n"
    "v_pk_mul_f32 v[20:21], v[20:21], v[248:249] op_sel_hi:[1,0]\n"
    "v_pk_mul_f32 v[22:23], v[22:23], v[248:249] op_sel_hi:[1,0]\n"
    "v_cvt_pk_bf16_f32 v156, v16, v17\n"
    "v_cvt_pk_bf16_f32 v157, v18, v19\n"
    "v_cvt_pk_bf16_f32 v158, v20, v21\n"
    "v_cvt_pk_bf16_f32 v159, v22, v23\n"
    "s_waitcnt lgkmcnt(3)\n"
    "v_mfma_f32_16x16x32_bf16 v[16:19], a[88:91], v[24:27], v[136:139]\n"
    "v_exp_f32 v11, v11\n"
    "v_exp_f32 v12, v12\n"
    "v_mfma_f32_16x16x32_bf16 v[20:23], a[92:95], v[24:27], v[140:143]\n"
    "v_exp_f32 v13, v13\n"
    "v_cvt_pk_bf16_f32 v29, v10, v11\n"
    "v_mfma_f32_16x16x32_bf16 v[240:243], a[128:131], v[24:27], v[144:147]\n"
    "v_exp_f32 v14, v14\n"
    "v_exp_f32 v15, v15\n"
    "v_cvt_pk_bf16_f32 v30, v12, v13\n"
    "v_cvt_pk_bf16_f32 v31, v14, v15\n"
    "v_mfma_f32_16x16x32_bf16 v[8:11], a[0:3], v[128:131], v[148:151]\n"
    "s_add_u32 s25, s84, s91\n"
    "s_add_u32 m0, s52, 0\n"
    "v_exp_f32 v0, v0\n"
    "v_exp_f32 v1, v1\n"
    "v_mfma_f32_16x16x32_bf16 v[12:15], a[4:7], v[128:131], v[148:151]\n"
    "buffer_load_dwordx4 v249, s[48:51], s25 offen lds\n"
    "v_exp_f32 v2, v2\n"
    "v_cvt_pk_bf16_f32 v24, v0, v1\n"
    "v_mfma_f32_16x16x32_bf16 v[16:19], a[80:83], v[28:31], v[16:19]\n"
    "v_exp_f32 v3, v3\n"
    "v_exp_f32 v4, v4\n"
    "v_mfma_f32_16x16x32_bf16 v[20:23], a[84:87], v[28:31], v[20:23]\n"
    "v_exp_f32 v5, v5\n"
    "v_cvt_pk_bf16_f32 v25, v2, v3\n"
    "v_mfma_f32_16x16x32_bf16 v[240:243], a[128:131], v[28:31], v[240:243]\n"
    "v_exp_f32 v6, v6\n"
    "v_exp_f32 v7, v7\n"
    "v_cvt_pk_bf16_f32 v26, v4, v5\n"
    "v_cvt_pk_bf16_f32 v27, v6, v7\n"
    "v_pk_add_f32 v[232:233], v[224:225], v[124:125]\n"
    "v_pk_add_f32 v[234:235], v[226:227], v[124:125]\n"
    "v_pk_add_f32 v[236:237], v[228:229], v[124:125]\n"
    "v_pk_add_f32 v[238:239], v[230:231], v[124:125]\n"
    "v_mfma_f32_16x16x32_bf16 v[0:3], a[32:35], v[104:107], v[232:235]\n"
    "ds_read_b128 a[40:43], v244 offset:4096\n"
    "v_exp_f32 v8, v8\n"
    "v_exp_f32 v9, v9\n"
    "v_mfma_f32_16x16x32_bf16 v[4:7], a[36:39], v[104:107], v[236:239]\n"
    "ds_read_b128 a[44:47], v244 offset:5120\n"
    "v_exp_f32 v10, v10\n"
    "v_cvt_pk_bf16_f32 v28, v8, v9\n"
    "v_mfma_f32_16x16x32_bf16 v[16:19], a[72:75], v[24:27], v[16:19]\n"
    "ds_read_b64_tr_b16 a[104:105], v245 offset:4096\n"
    "v_exp_f32 v11, v11\n"
    "v_exp_f32 v12, v12\n"
    "v_mfma_f32_16x16x32_bf16 v[20:23], a[76:79], v[24:27], v[20:23]\n"
    "ds_read_b64_tr_b16 a[106:107], v245 offset:5120\n"
    "v_exp_f32 v13, v13\n"
    "v_cvt_pk_bf16_f32 v29, v10, v11\n"
    "v_mfma_f32_16x16x32_bf16 v[240:243], a[128:131], v[24:27], v[240:243]\n"
    "ds_read_b64_tr_b16 a[108:109], v246 offset:4096\n"
    "v_exp_f32 v14, v14\n"
    "v_exp_f32 v15, v15\n"
    "v_cvt_pk_bf16_f32 v30, v12, v13\n"
    "v_cvt_pk_bf16_f32 v31, v14, v15\n"
    "v_mfma_f32_16x16x32_bf16 v[8:11], a[24:27], v[104:107], v[124:127]\n"
    "ds_read_b64_tr_b16 a[110:111], v246 offset:5120\n"
    "v_exp_f32 v0, v0\n"
    "v_exp_f32 v1, v1\n"
    "v_mfma_f32_16x16x32_bf16 v[12:15], a[28:31], v[104:107], v[124:127]\n"
    "buffer_load_dwordx4 v249, s[48:51], s84 offen offset:1024 lds\n"
    "v_exp_f32 v2, v2\n"
    "v_cvt_pk_bf16_f32 v24, v0, v1\n"
    "v_mfma_f32_16x16x32_bf16 v[16:19], a[64:67], v[28:31], v[16:19]\n"
    "s_xor_b32 s84, s84, 0x10000\n"
    "v_exp_f32 v3, v3\n"
    "v_exp_f32 v4, v4\n"
    "v_mfma_f32_16x16x32_bf16 v[20:23], a[68:71], v[28:31], v[20:23]\n"
    "s_add_u32 s25, s84, s91\n"
    "s_add_u32 m0, s52, 2048\n"
    "v_exp_f32 v5, v5\n"
    "v_cvt_pk_bf16_f32 v25, v2, v3\n"
    "v_mfma_f32_16x16x32_bf16 v[240:243], a[128:131], v[28:31], v[240:243]\n"
    "buffer_load_dwordx4 v249, s[48:51], s25 offen lds\n"
    "v_exp_f32 v6, v6\n"
    "v_exp_f32 v7, v7\n"
    "v_cvt_pk_bf16_f32 v26, v4, v5\n"
    "v_cvt_pk_bf16_f32 v27, v6, v7\n"
    "v_mfma_f32_16x16x32_bf16 v[0:3], a[16:19], v[104:107], v[124:127]\n"
    "buffer_load_dwordx4 v249, s[48:51], s84 offen offset:1024 lds\n"
    "v_exp_f32 v8, v8\n"
    "v_exp_f32 v9, v9\n"
    "v_mfma_f32_16x16x32_bf16 v[4:7], a[20:23], v[104:107], v[124:127]\n"
    "s_xor_b32 s84, s84, 0x10000\n"
    "v_exp_f32 v10, v10\n"
    "v_cvt_pk_bf16_f32 v28, v8, v9\n"
    "v_rcp_f32 v248, v240\n"
    "v_max_f32 v255, v255, v240\n"
    "v_pk_mul_f32 v[16:17], v[16:17], v[248:249] op_sel_hi:[1,0]\n"
    "v_pk_mul_f32 v[18:19], v[18:19], v[248:249] op_sel_hi:[1,0]\n"
    "v_pk_mul_f32 v[20:21], v[20:21], v[248:249] op_sel_hi:[1,0]\n"
    "v_pk_mul_f32 v[22:23], v[22:23], v[248:249] op_sel_hi:[1,0]\n"
    "v_cvt_pk_bf16_f32 v132, v16, v17\n"
    "v_cvt_pk_bf16_f32 v133, v18, v19\n"
    "v_cvt_pk_bf16_f32 v134, v20, v21\n"
    "v_cvt_pk_bf16_f32 v135, v22, v23\n"
    "s_waitcnt lgkmcnt(3)\n"
    "v_mfma_f32_16x16x32_bf16 v[16:19], a[96:99], v[24:27], v[112:115]\n"
    "ds_read_b128 a[48:51], v244 offset:2048\n"
    "v_exp_f32 v11, v11\n"
    "v_exp_f32 v12, v12\n"
    "v_mfma_f32_16x16x32_bf16 v[20:23], a[100:103], v[24:27], v[116:119]\n"
    "ds_read_b128 a[52:55], v244 offset:3072\n"
    "v_exp_f32 v13, v13\n"
    "v_cvt_pk_bf16_f32 v29, v10, v11\n"
    "v_mfma_f32_16x16x32_bf16 v[240:243], a[128:131], v[24:27], v[120:123]\n"
    "ds_read_b64_tr_b16 a[112:113], v245 offset:2048\n"
    "v_exp_f32 v14, v14\n"
    "v_exp_f32 v15, v15\n"
    "v_cvt_pk_bf16_f32 v30, v12, v13\n"
    "v_cvt_pk_bf16_f32 v31, v14, v15\n"
    "v_mfma_f32_16x16x32_bf16 v[8:11], a[8:11], v[104:107], v[124:127]\n"
    "ds_read_b64_tr_b16 a[114:115], v245 offset:3072\n"
    "v_exp_f32 v0, v0\n"
    "v_exp_f32 v1, v1\n"
    "v_mfma_f32_16x16x32_bf16 v[12:15], a[12:15], v[104:107], v[124:127]\n"
    "ds_read_b64_tr_b16 a[116:117], v246 offset:2048\n"
    "v_exp_f32 v2, v2\n"
    "v_cvt_pk_bf16_f32 v24, v0, v1\n"
    "v_mfma_f32_16x16x32_bf16 v[16:19], a[88:91], v[28:31], v[16:19]\n"
    "ds_read_b64_tr_b16 a[118:119], v246 offset:3072\n"
    "v_exp_f32 v3, v3\n"
    "v_exp_f32 v4, v4\n"
    "v_mfma_f32_16x16x32_bf16 v[20:23], a[92:95], v[28:31], v[20:23]\n"
    "s_add_u32 s25, s85, s91\n"
    "s_add_u32 m0, s52, 4096\n"
    "v_exp_f32 v5, v5\n"
    "v_cvt_pk_bf16_f32 v25, v2, v3\n"
    "v_mfma_f32_16x16x32_bf16 v[240:243], a[128:131], v[28:31], v[240:243]\n"
    "buffer_load_dwordx4 v249, s[48:51], s25 offen lds\n"
    "v_exp_f32 v6, v6\n"
    "v_exp_f32 v7, v7\n"
    "v_cvt_pk_bf16_f32 v26, v4, v5\n"
    "v_cvt_pk_bf16_f32 v27, v6, v7\n"
    "v_mfma_f32_16x16x32_bf16 v[0:3], a[0:3], v[104:107], v[124:127]\n"
    "buffer_load_dwordx4 v249, s[48:51], s85 offen offset:1024 lds\n"
    "v_exp_f32 v8, v8\n"
    "v_exp_f32 v9, v9\n"
    "v_mfma_f32_16x16x32_bf16 v[4:7], a[4:7], v[104:107], v[124:127]\n"
    "s_xor_b32 s85, s85, 0x10000\n"
    "v_exp_f32 v10, v10\n"
    "v_cvt_pk_bf16_f32 v28, v8, v9\n"
    "v_mfma_f32_16x16x32_bf16 v[16:19], a[80:83], v[24:27], v[16:19]\n"
    "s_add_u32 s25, s85, s91\n"
    "s_add_u32 m0, s52, 6144\n"
    "v_exp_f32 v11, v11\n"
    "v_exp_f32 v12, v12\n"
    "v_mfma_f32_16x16x32_bf16 v[20:23], a[84:87], v[24:27], v[20:23]\n"
    "buffer_load_dwordx4 v249, s[48:51], s25 offen lds\n"
    "v_exp_f32 v13, v13\n"
    "v_cvt_pk_bf16_f32 v29, v10, v11\n"
    "v_mfma_f32_16x16x32_bf16 v[240:243], a[128:131], v[24:27], v[240:243]\n"
    "ds_read_b128 a[56:59], v244 offset:0\n"
    "v_exp_f32 v14, v14\n"
    "v_exp_f32 v15, v15\n"
    "v_cvt_pk_bf16_f32 v30, v12, v13\n"
    "v_cvt_pk_bf16_f32 v31, v14, v15\n"
    "v_pk_add_f32 v[232:233], v[224:225], v[100:101]\n"
    "v_pk_add_f32 v[234:235], v[226:227], v[100:101]\n"
    "v_pk_add_f32 v[236:237], v[228:229], v[100:101]\n"
    "v_pk_add_f32 v[238:239], v[230:231], v[100:101]\n"
    "v_mfma_f32_16x16x32_bf16 v[8:11], a[40:43], v[80:83], v[232:235]\n"
    "ds_read_b128 a[60:63], v244 offset:1024\n"
    "v_exp_f32 v0, v0\n"
    "v_exp_f32 v1, v1\n"
    "v_mfma_f32_16x16x32_bf16 v[12:15], a[44:47], v[80:83], v[236:239]\n"
    "ds_read_b64_tr_b16 a[120:121], v245 offset:0\n"
    "v_exp_f32 v2, v2\n"
    "v_cvt_pk_bf16_f32 v24, v0, v1\n"
    "v_mfma_f32_16x16x32_bf16 v[16:19], a[72:75], v[28:31], v[16:19]\n"
    "ds_read_b64_tr_b16 a[122:123], v245 offset:1024\n"
    "v_exp_f32 v3, v3\n"
    "v_exp_f32 v4, v4\n"
    "v_mfma_f32_16x16x32_bf16 v[20:23], a[76:79], v[28:31], v[20:23]\n"
    "ds_read_b64_tr_b16 a[124:125], v246 offset:0\n"
    "v_exp_f32 v5, v5\n"
    "v_cvt_pk_bf16_f32 v25, v2, v3\n"
    "v_mfma_f32_16x16x32_bf16 v[240:243], a[128:131], v[28:31], v[240:243]\n"
    "ds_read_b64_tr_b16 a[126:127], v246 offset:1024\n"
    "v_exp_f32 v6, v6\n"
    "v_exp_f32 v7, v7\n"
    "v_cvt_pk_bf16_f32 v26, v4, v5\n"
    "v_cvt_pk_bf16_f32 v27, v6, v7\n"
    "v_mfma_f32_16x16x32_bf16 v[0:3], a[32:35], v[80:83], v[100:103]\n"
    "buffer_load_dwordx4 v249, s[48:51], s85 offen offset:1024 lds\n"
    "v_exp_f32 v8, v8\n"
    "v_exp_f32 v9, v9\n"
    "v_mfma_f32_16x16x32_bf16 v[4:7], a[36:39], v[80:83], v[100:103]\n"
    "s_xor_b32 s85, s85, 0x10000\n"
    "v_exp_f32 v10, v10\n"
    "v_cvt_pk_bf16_f32 v28, v8, v9\n"
    "v_mfma_f32_16x16x32_bf16 v[16:19], a[64:67], v[24:27], v[16:19]\n"
    "s_add_u32 s25, s87, s91\n"
    "s_add_u32 m0, s52, 32768\n"
    "v_exp_f32 v11, v11\n"
    "v_exp_f32 v12, v12\n"
    "v_mfma_f32_16x16x32_bf16 v[20:23], a[68:71], v[24:27], v[20:23]\n"
    "buffer_load_dwordx4 v249, s[48:51], s25 offen lds\n"
    "v_exp_f32 v13, v13\n"
    "v_cvt_pk_bf16_f32 v29, v10, v11\n"
    "v_mfma_f32_16x16x32_bf16 v[240:243], a[128:131], v[24:27], v[240:243]\n"
    "buffer_load_dwordx4 v249, s[48:51], s87 offen offset:1024 lds\n"
    "v_exp_f32 v14, v14\n"
    "v_exp_f32 v15, v15\n"
    "v_cvt_pk_bf16_f32 v30, v12, v13\n"
    "v_cvt_pk_bf16_f32 v31, v14, v15\n"
    "v_mfma_f32_16x16x32_bf16 v[8:11], a[24:27], v[80:83], v[100:103]\n"
    "s_xor_b32 s87, s87, 0x10000\n"
    "v_exp_f32 v0, v0\n"
    "v_exp_f32 v1, v1\n"
    "v_mfma_f32_16x16x32_bf16 v[12:15], a[28:31], v[80:83], v[100:103]\n"
    "s_add_u32 s25, s87, s91\n"
    "s_add_u32 m0, s52, 34816\n"
    "v_exp_f32 v2, v2\n"
    "v_cvt_pk_bf16_f32 v24, v0, v1\n"
    "v_rcp_f32 v248, v240\n"
    "v_max_f32 v255, v255, v240\n"
    "v_pk_mul_f32 v[16:17], v[16:17], v[248:249] op_sel_hi:[1,0]\n"
    "v_pk_mul_f32 v[18:19], v[18:19], v[248:249] op_sel_hi:[1,0]\n"
    "v_pk_mul_f32 v[20:21], v[20:21], v[248:249] op_sel_hi:[1,0]\n"
    "v_pk_mul_f32 v[22:23], v[22:23], v[248:249] op_sel_hi:[1,0]\n"
    "v_cvt_pk_bf16_f32 v108, v16, v17\n"
    "v_cvt_pk_bf16_f32 v109, v18, v19\n"
    "v_cvt_pk_bf16_f32 v110, v20, v21\n"
    "v_cvt_pk_bf16_f32 v111, v22, v23\n"
    "s_waitcnt lgkmcnt(3)\n"
    "v_mfma_f32_16x16x32_bf16 v[16:19], a[104:107], v[28:31], v[88:91]\n"
    "buffer_load_dwordx4 v249, s[48:51], s25 offen lds\n"
    "v_exp_f32 v3, v3\n"
    "v_exp_f32 v4, v4\n"
    "v_mfma_f32_16x16x32_bf16 v[20:23], a[108:111], v[28:31], v[92:95]\n"
    "buffer_load_dwordx4 v249, s[48:51], s87 offen offset:1024 lds\n"
    "v_exp_f32 v5, v5\n"
    "v_cvt_pk_bf16_f32 v25, v2, v3\n"
    "v_mfma_f32_16x16x32_bf16 v[240:243], a[128:131], v[28:31], v[96:99]\n"
    "s_xor_b32 s87, s87, 0x10000\n"
    "v_exp_f32 v6, v6\n"
    "v_exp_f32 v7, v7\n"
    "v_cvt_pk_bf16_f32 v26, v4, v5\n"
    "v_cvt_pk_bf16_f32 v27, v6, v7\n"
    "v_mfma_f32_16x16x32_bf16 v[0:3], a[16:19], v[80:83], v[100:103]\n"
    "s_add_u32 s25, s89, s91\n"
    "s_add_u32 m0, s52, 36864\n"
    "v_exp_f32 v8, v8\n"
    "v_exp_f32 v9, v9\n"
    "v_mfma_f32_16x16x32_bf16 v[4:7], a[20:23], v[80:83], v[100:103]\n"
    "buffer_load_dwordx4 v249, s[48:51], s25 offen lds\n"
    "v_exp_f32 v10, v10\n"
    "v_cvt_pk_bf16_f32 v28, v8, v9\n"
    "v_mfma_f32_16x16x32_bf16 v[16:19], a[96:99], v[24:27], v[16:19]\n"
    "v_exp_f32 v11, v11\n"
    "v_exp_f32 v12, v12\n"
    "v_mfma_f32_16x16x32_bf16 v[20:23], a[100:103], v[24:27], v[20:23]\n"
    "v_exp_f32 v13, v13\n"
    "v_cvt_pk_bf16_f32 v29, v10, v11\n"
    "v_mfma_f32_16x16x32_bf16 v[240:243], a[128:131], v[24:27], v[240:243]\n"
    "v_exp_f32 v14, v14\n"
    "v_exp_f32 v15, v15\n"
    "v_cvt_pk_bf16_f32 v30, v12, v13\n"
    "v_cvt_pk_bf16_f32 v31, v14, v15\n"
    "v_mfma_f32_16x16x32_bf16 v[8:11], a[8:11], v[80:83], v[100:103]\n"
    "buffer_load_dwordx4 v249, s[48:51], s89 offen offset:1024 lds\n"
    "v_exp_f32 v0, v0\n"
    "v_exp_f32 v1, v1\n"
    "v_mfma_f32_16x16x32_bf16 v[12:15], a[12:15], v[80:83], v[100:103]\n"
    "s_xor_b32 s89, s89, 0x10000\n"
    "v_exp_f32 v2, v2\n"
    "v_cvt_pk_bf16_f32 v24, v0, v1\n"
    "v_mfma_f32_16x16x32_bf16 v[16:19], a[88:91], v[28:31], v[16:19]\n"
    "s_add_u32 s25, s89, s91\n"
    "s_add_u32 m0, s52, 38912\n"
    "v_exp_f32 v3, v3\n"
    "v_exp_f32 v4, v4\n"
    "v_mfma_f32_16x16x32_bf16 v[20:23], a[92:95], v[28:31], v[20:23]\n"
    "buffer_load_dwordx4 v249, s[48:51], s25 offen lds\n"
    "v_exp_f32 v5, v5\n"
    "v_cvt_pk_bf16_f32 v25, v2, v3\n"
    "v_mfma_f32_16x16x32_bf16 v[240:243], a[128:131], v[28:31], v[240:243]\n"
    "v_exp_f32 v6, v6\n"
    "v_exp_f32 v7, v7\n"
    "v_cvt_pk_bf16_f32 v26, v4, v5\n"
    "v_cvt_pk_bf16_f32 v27, v6, v7\n"
    "v_mfma_f32_16x16x32_bf16 v[0:3], a[0:3], v[80:83], v[100:103]\n"
    "buffer_load_dwordx4 v249, s[48:51], s89 offen offset:1024 lds\n"
    "v_exp_f32 v8, v8\n"
    "v_exp_f32 v9, v9\n"
    "v_mfma_f32_16x16x32_bf16 v[4:7], a[4:7], v[80:83], v[100:103]\n"
    "s_xor_b32 s89, s89, 0x10000\n"
    "v_exp_f32 v10, v10\n"
    "v_cvt_pk_bf16_f32 v28, v8, v9\n"
    "v_mfma_f32_16x16x32_bf16 v[16:19], a[80:83], v[24:27], v[16:19]\n"
    "v_bfe_u32 v251, v250, 4, 2\n"
    "v_exp_f32 v11, v11\n"
    "v_exp_f32 v12, v12\n"
    "v_mfma_f32_16x16x32_bf16 v[20:23], a[84:87], v[24:27], v[20:23]\n"
    "v_and_b32 v252, 0x3c0, v250\n"
    "v_exp_f32 v13, v13\n"
    "v_cvt_pk_bf16_f32 v29, v10, v11\n"
    "v_mfma_f32_16x16x32_bf16 v[240:243], a[128:131], v[24:27], v[240:243]\n"
    "v_lshlrev_b32 v251, 1, v251\n"
    "v_exp_f32 v14, v14\n"
    "v_exp_f32 v15, v15\n"
    "v_cvt_pk_bf16_f32 v30, v12, v13\n"
    "v_cvt_pk_bf16_f32 v31, v14, v15\n"
    "v_pk_add_f32 v[232:233], v[224:225], v[76:77]\n"
    "v_pk_add_f32 v[234:235], v[226:227], v[76:77]\n"
    "v_pk_add_f32 v[236:237], v[228:229], v[76:77]\n"
    "v_pk_add_f32 v[238:239], v[230:231], v[76:77]\n"
    "v_mfma_f32_16x16x32_bf16 v[8:11], a[48:51], v[56:59], v[232:235]\n"
    "v_add_lshl_u32 v252, v252, v251, 1\n"
    "v_exp_f32 v0, v0\n"
    "v_exp_f32 v1, v1\n"
    "v_mfma_f32_16x16x32_bf16 v[12:15], a[52:55], v[56:59], v[236:239]\n"
    "v_exp_f32 v2, v2\n"
    "v_cvt_pk_bf16_f32 v24, v0, v1\n"
    "v_mfma_f32_16x16x32_bf16 v[16:19], a[72:75], v[28:31], v[16:19]\n"
    "v_exp_f32 v3, v3\n"
    "v_exp_f32 v4, v4\n"
    "v_mfma_f32_16x16x32_bf16 v[20:23], a[76:79], v[28:31], v[20:23]\n"
    "v_exp_f32 v5, v5\n"
    "v_cvt_pk_bf16_f32 v25, v2, v3\n"
    "v_mfma_f32_16x16x32_bf16 v[240:243], a[128:131], v[28:31], v[240:243]\n"
    "v_exp_f32 v6, v6\n"
    "v_exp_f32 v7, v7\n"
    "v_cvt_pk_bf16_f32 v26, v4, v5\n"
    "v_cvt_pk_bf16_f32 v27, v6, v7\n"
    "v_mfma_f32_16x16x32_bf16 v[0:3], a[40:43], v[56:59], v[76:79]\n"
    "s_mov_b32 exec_hi, 0\n"
    "buffer_load_dword v251, v252, s[44:47], s82 offen sc1\n"
    "s_mov_b32 exec_hi, -1\n"
    "v_exp_f32 v8, v8\n"
    "v_exp_f32 v9, v9\n"
    "v_mfma_f32_16x16x32_bf16 v[4:7], a[44:47], v[56:59], v[76:79]\n"
    "v_exp_f32 v10, v10\n"
    "v_cvt_pk_bf16_f32 v28, v8, v9\n"
    "v_mfma_f32_16x16x32_bf16 v[16:19], a[64:67], v[24:27], v[16:19]\n"
    "v_exp_f32 v11, v11\n"
    "v_exp_f32 v12, v12\n"
    "v_mfma_f32_16x16x32_bf16 v[20:23], a[68:71], v[24:27], v[20:23]\n"
    "v_exp_f32 v13, v13\n"
    "v_cvt_pk_bf16_f32 v29, v10, v11\n"
    "v_mfma_f32_16x16x32_bf16 v[240:243], a[128:131], v[24:27], v[240:243]\n"
    "v_exp_f32 v14, v14\n"
    "v_exp_f32 v15, v15\n"
    "v_cvt_pk_bf16_f32 v30, v12, v13\n"
    "v_cvt_pk_bf16_f32 v31, v14, v15\n"
    "v_mfma_f32_16x16x32_bf16 v[8:11], a[32:35], v[56:59], v[76:79]\n"
    "v_exp_f32 v0, v0\n"
    "v_exp_f32 v1, v1\n"
    "v_mfma_f32_16x16x32_bf16 v[12:15], a[36:39], v[56:59], v[76:79]\n"
    "v_exp_f32 v2, v2\n"
    "v_cvt_pk_bf16_f32 v24, v0, v1\n"
    "v_rcp_f32 v248, v240\n"
    "v_max_f32 v255, v255, v240\n"
    "v_pk_mul_f32 v[16:17], v[16:17], v[248:249] op_sel_hi:[1,0]\n"
    "v_pk_mul_f32 v[18:19], v[18:19], v[248:249] op_sel_hi:[1,0]\n"
    "v_pk_mul_f32 v[20:21], v[20:21], v[248:249] op_sel_hi:[1,0]\n"
    "v_pk_mul_f32 v[22:23], v[22:23], v[248:249] op_sel_hi:[1,0]\n"
    "v_cvt_pk_bf16_f32 v84, v16, v17\n"
    "v_cvt_pk_bf16_f32 v85, v18, v19\n"
    "v_cvt_pk_bf16_f32 v86, v20, v21\n"
    "v_cvt_pk_bf16_f32 v87, v22, v23\n"
    "v_mfma_f32_16x16x32_bf16 v[16:19], a[112:115], v[28:31], v[64:67]\n"
    "v_exp_f32 v3, v3\n"
    "v_exp_f32 v4, v4\n"
    "v_mfma_f32_16x16x32_bf16 v[20:23], a[116:119], v[28:31], v[68:71]\n"
    "v_exp_f32 v5, v5\n"
    "v_cvt_pk_bf16_f32 v25, v2, v3\n"
    "v_mfma_f32_16x16x32_bf16 v[240:243], a[128:131], v[28:31], v[72:75]\n"
    "v_exp_f32 v6, v6\n"
    "v_exp_f32 v7, v7\n"
    "v_cvt_pk_bf16_f32 v26, v4, v5\n"
    "v_cvt_pk_bf16_f32 v27, v6, v7\n"
    "v_mfma_f32_16x16x32_bf16 v[0:3], a[24:27], v[56:59], v[76:79]\n"
    "v_exp_f32 v8, v8\n"
    "v_exp_f32 v9, v9\n"
    "v_mfma_f32_16x16x32_bf16 v[4:7], a[28:31], v[56:59], v[76:79]\n"
    "v_exp_f32 v10, v10\n"
    "v_cvt_pk_bf16_f32 v28, v8, v9\n"
    "v_mfma_f32_16x16x32_bf16 v[16:19], a[104:107], v[24:27], v[16:19]\n"
    "v_exp_f32 v11, v11\n"
    "v_exp_f32 v12, v12\n"
    "v_mfma_f32_16x16x32_bf16 v[20:23], a[108:111], v[24:27], v[20:23]\n"
    "v_exp_f32 v13, v13\n"
    "v_cvt_pk_bf16_f32 v29, v10, v11\n"
    "v_mfma_f32_16x16x32_bf16 v[240:243], a[128:131], v[24:27], v[240:243]\n"
    "v_exp_f32 v14, v14\n"
    "v_exp_f32 v15, v15\n"
    "v_cvt_pk_bf16_f32 v30, v12, v13\n"
    "v_cvt_pk_bf16_f32 v31, v14, v15\n"
    "v_mfma_f32_16x16x32_bf16 v[8:11], a[16:19], v[56:59], v[76:79]\n"
    "v_exp_f32 v0, v0\n"
    "v_exp_f32 v1, v1\n"
    "v_mfma_f32_16x16x32_bf16 v[12:15], a[20:23], v[56:59], v[76:79]\n"
    "v_exp_f32 v2, v2\n"
    "v_cvt_pk_bf16_f32 v24, v0, v1\n"
    "v_mfma_f32_16x16x32_bf16 v[16:19], a[96:99], v[28:31], v[16:19]\n"
    "v_exp_f32 v3, v3\n"
    "v_exp_f32 v4, v4\n"
    "v_mfma_f32_16x16x32_bf16 v[20:23], a[100:103], v[28:31], v[20:23]\n"
    "v_exp_f32 v5, v5\n"
    "v_cvt_pk_bf16_f32 v25, v2, v3\n"
    "v_mfma_f32_16x16x32_bf16 v[240:243], a[128:131], v[28:31], v[240:243]\n"
    "v_exp_f32 v6, v6\n"
    "v_exp_f32 v7, v7\n"
    "v_cvt_pk_bf16_f32 v26, v4, v5\n"
    "v_cvt_pk_bf16_f32 v27, v6, v7\n"
    "v_mfma_f32_16x16x32_bf16 v[0:3], a[8:11], v[56:59], v[76:79]\n"
    "v_readlane_b32 s91, v247, 0\n"
    "v_exp_f32 v8, v8\n"
    "v_exp_f32 v9, v9\n"
    "v_mfma_f32_16x16x32_bf16 v[4:7], a[12:15], v[56:59], v[76:79]\n"
    "s_waitcnt vmcnt(0)\n"
    "v_exp_f32 v10, v10\n"
    "v_cvt_pk_bf16_f32 v28, v8, v9\n"
    "v_mfma_f32_16x16x32_bf16 v[16:19], a[88:91], v[24:27], v[16:19]\n"
    "s_add_u32 s33, s92, 1\n"
    "v_exp_f32 v11, v11\n"
    "v_exp_f32 v12, v12\n"
    "v_mfma_f32_16x16x32_bf16 v[20:23], a[92:95], v[24:27], v[20:23]\n"
    "v_subrev_u32 v254, s33, v251\n"
    "v_exp_f32 v13, v13\n"
    "v_cvt_pk_bf16_f32 v29, v10, v11\n"
    "v_mfma_f32_16x16x32_bf16 v[240:243], a[128:131], v[24:27], v[240:243]\n"
    "v_cmp_lt_u32 vcc, 2, v254\n"
    "v_exp_f32 v14, v14\n"
    "v_exp_f32 v15, v15\n"
    "v_cvt_pk_bf16_f32 v30, v12, v13\n"
    "v_cvt_pk_bf16_f32 v31, v14, v15\n"
    "v_mfma_f32_16x16x32_bf16 v[8:11], a[0:3], v[56:59], v[76:79]\n"
    "s_cmp_eq_u32 vcc_lo, -1\n"
    "s_cselect_b32 s27, -1, 0\n"
    "v_exp_f32 v0, v0\n"
    "v_exp_f32 v1, v1\n"
    "v_mfma_f32_16x16x32_bf16 v[12:15], a[4:7], v[56:59], v[76:79]\n"
    "v_cmp_gt_u32 vcc, 16, v254\n"
    "v_exp_f32 v2, v2\n"
    "v_cvt_pk_bf16_f32 v24, v0, v1\n"
    "v_mfma_f32_16x16x32_bf16 v[16:19], a[80:83], v[28:31], v[16:19]\n"
    "s_cmp_eq_u32 vcc_lo, -1\n"
    "s_cselect_b32 s88, s67, 1073741824\n"
    "v_exp_f32 v3, v3\n"
    "v_exp_f32 v4, v4\n"
    "v_mfma_f32_16x16x32_bf16 v[20:23], a[84:87], v[28:31], v[20:23]\n"
    "s_add_u32 s84, s91, s88\n"
    "v_exp_f32 v5, v5\n"
    "v_cvt_pk_bf16_f32 v25, v2, v3\n"
    "v_mfma_f32_16x16x32_bf16 v[240:243], a[128:131], v[28:31], v[240:243]\n"
    "v_readlane_b32 s91, v247, 1\n"
    "v_exp_f32 v6, v6\n"
    "v_exp_f32 v7, v7\n"
    "v_cvt_pk_bf16_f32 v26, v4, v5\n"
    "v_cvt_pk_bf16_f32 v27, v6, v7\n"
    "v_pk_add_f32 v[232:233], v[224:225], v[52:53]\n"
    "v_pk_add_f32 v[234:235], v[226:227], v[52:53]\n"
    "v_pk_add_f32 v[236:237], v[228:229], v[52:53]\n"
    "v_pk_add_f32 v[238:239], v[230:231], v[52:53]\n"
    "v_mfma_f32_16x16x32_bf16 v[0:3], a[56:59], v[32:35], v[232:235]\n"
    "s_xor_b32 s85, s84, 2048\n"
    "v_exp_f32 v8, v8\n"
    "v_exp_f32 v9, v9\n"
    "v_mfma_f32_16x16x32_bf16 v[4:7], a[60:63], v[32:35], v[236:239]\n"
    "buffer_load_dwordx4 a[224:227], v253, s[44:47], s84 offen sc1\n"
    "v_exp_f32 v10, v10\n"
    "v_cvt_pk_bf16_f32 v28, v8, v9\n"
    "v_mfma_f32_16x16x32_bf16 v[16:19], a[72:75], v[24:27], v[16:19]\n"
    "buffer_load_dwordx4 a[228:231], v253, s[44:47], s84 offen offset:1024 sc1\n"
    "v_exp_f32 v11, v11\n"
    "v_exp_f32 v12, v12\n"
    "v_mfma_f32_16x16x32_bf16 v[20:23], a[76:79], v[24:27], v[20:23]\n"
    "buffer_load_dwordx4 a[232:235], v253, s[44:47], s85 offen sc1\n"
    "v_exp_f32 v13, v13\n"
    "v_cvt_pk_bf16_f32 v29, v10, v11\n"
    "v_mfma_f32_16x16x32_bf16 v[240:243], a[128:131], v[24:27], v[240:243]\n"
    "buffer_load_dwordx4 a[236:239], v253, s[44:47], s85 offen offset:1024 sc1\n"
    "v_exp_f32 v14, v14\n"
    "v_exp_f32 v15, v15\n"
    "v_cvt_pk_bf16_f32 v30, v12, v13\n"
    "v_cvt_pk_bf16_f32 v31, v14, v15\n"
    "v_mfma_f32_16x16x32_bf16 v[8:11], a[48:51], v[32:35], v[52:55]\n"
    "s_add_u32 s84, s91, s88\n"
    "v_exp_f32 v0, v0\n"
    "v_exp_f32 v1, v1\n"
    "v_mfma_f32_16x16x32_bf16 v[12:15], a[52:55], v[32:35], v[52:55]\n"
    "s_xor_b32 s85, s84, 2048\n"
    "v_exp_f32 v2, v2\n"
    "v_cvt_pk_bf16_f32 v24, v0, v1\n"
    "v_mfma_f32_16x16x32_bf16 v[16:19], a[64:67], v[28:31], v[16:19]\n"
    "buffer_load_dwordx4 a[240:243], v253, s[44:47], s84 offen sc1\n"
    "v_exp_f32 v3, v3\n"
    "v_exp_f32 v4, v4\n"
    "v_mfma_f32_16x16x32_bf16 v[20:23], a[68:71], v[28:31], v[20:23]\n"
    "buffer_load_dwordx4 a[244:247], v253, s[44:47], s84 offen offset:1024 sc1\n"
    "v_exp_f32 v5, v5\n"
    "v_cvt_pk_bf16_f32 v25, v2, v3\n"
    "v_mfma_f32_16x16x32_bf16 v[240:243], a[128:131], v[28:31], v[240:243]\n"
    "buffer_load_dwordx4 a[248:251], v253, s[44:47], s85 offen sc1\n"
    "v_exp_f32 v6, v6\n"
    "v_exp_f32 v7, v7\n"
    "v_cvt_pk_bf16_f32 v26, v4, v5\n"
    "v_cvt_pk_bf16_f32 v27, v6, v7\n"
    "v_mfma_f32_16x16x32_bf16 v[0:3], a[40:43], v[32:35], v[52:55]\n"
    "buffer_load_dwordx4 a[252:255], v253, s[44:47], s85 offen offset:1024 sc1\n"
    "v_exp_f32 v8, v8\n"
    "v_exp_f32 v9, v9\n"
    "v_mfma_f32_16x16x32_bf16 v[4:7], a[44:47], v[32:35], v[52:55]\n"
    "v_exp_f32 v10, v10\n"
    "v_cvt_pk_bf16_f32 v28, v8, v9\n"
    "v_rcp_f32 v248, v240\n"
    "v_max_f32 v255, v255, v240\n"
    "v_pk_mul_f32 v[16:17], v[16:17], v[248:249] op_sel_hi:[1,0]\n"
    "v_pk_mul_f32 v[18:19], v[18:19], v[248:249] op_sel_hi:[1,0]\n"
    "v_pk_mul_f32 v[20:21], v[20:21], v[248:249] op_sel_hi:[1,0]\n"
    "v_pk_mul_f32 v[22:23], v[22:23], v[248:249] op_sel_hi:[1,0]\n"
    "v_cvt_pk_bf16_f32 v60, v16, v17\n"
    "v_cvt_pk_bf16_f32 v61, v18, v19\n"
    "v_cvt_pk_bf16_f32 v62, v20, v21\n"
    "v_cvt_pk_bf16_f32 v63, v22, v23\n"
    "s_waitcnt lgkmcnt(2)\n"
    "v_mfma_f32_16x16x32_bf16 v[16:19], a[120:123], v[24:27], v[40:43]\n"
    "v_exp_f32 v11, v11\n"
    "v_exp_f32 v12, v12\n"
    "s_waitcnt lgkmcnt(0)\n"
    "v_mfma_f32_16x16x32_bf16 v[20:23], a[124:127], v[24:27], v[44:47]\n"
    "v_exp_f32 v13, v13\n"
    "v_cvt_pk_bf16_f32 v29, v10, v11\n"
    "v_mfma_f32_16x16x32_bf16 v[240:243], a[128:131], v[24:27], v[48:51]\n"
    "v_exp_f32 v14, v14\n"
    "v_exp_f32 v15, v15\n"
    "v_cvt_pk_bf16_f32 v30, v12, v13\n"
    "v_cvt_pk_bf16_f32 v31, v14, v15\n"
    "v_mfma_f32_16x16x32_bf16 v[8:11], a[32:35], v[32:35], v[52:55]\n"
    "v_exp_f32 v0, v0\n"
    "v_exp_f32 v1, v1\n"
    "v_mfma_f32_16x16x32_bf16 v[12:15], a[36:39], v[32:35], v[52:55]\n"
    "v_exp_f32 v2, v2\n"
    "v_cvt_pk_bf16_f32 v24, v0, v1\n"
    "v_mfma_f32_16x16x32_bf16 v[16:19], a[112:115], v[28:31], v[16:19]\n"
    "v_exp_f32 v3, v3\n"
    "v_exp_f32 v4, v4\n"
    "v_mfma_f32_16x16x32_bf16 v[20:23], a[116:119], v[28:31], v[20:23]\n"
    "v_exp_f32 v5, v5\n"
    "v_cvt_pk_bf16_f32 v25, v2, v3\n"
    "v_mfma_f32_16x16x32_bf16 v[240:243], a[128:131], v[28:31], v[240:243]\n"
    "v_exp_f32 v6, v6\n"
    "v_exp_f32 v7, v7\n"
    "v_cvt_pk_bf16_f32 v26, v4, v5\n"
    "v_cvt_pk_bf16_f32 v27, v6, v7\n"
    "v_mfma_f32_16x16x32_bf16 v[0:3], a[24:27], v[32:35], v[52:55]\n"
    "buffer_store_dwordx4 v[156:159], v250, s[44:47], s29 offen offset:1024 sc1\n"
    "v_exp_f32 v8, v8\n"
    "v_exp_f32 v9, v9\n"
    "v_mfma_f32_16x16x32_bf16 v[4:7], a[28:31], v[32:35], v[52:55]\n"
    "v_exp_f32 v10, v10\n"
    "v_cvt_pk_bf16_f32 v28, v8, v9\n"
    "v_mfma_f32_16x16x32_bf16 v[16:19], a[104:107], v[24:27], v[16:19]\n"
    "v_exp_f32 v11, v11\n"
    "v_exp_f32 v12, v12\n"
    "v_mfma_f32_16x16x32_bf16 v[20:23], a[108:111], v[24:27], v[20:23]\n"
    "v_exp_f32 v13, v13\n"
    "v_cvt_pk_bf16_f32 v29, v10, v11\n"
    "v_mfma_f32_16x16x32_bf16 v[240:243], a[128:131], v[24:27], v[240:243]\n"
    "v_exp_f32 v14, v14\n"
    "v_exp_f32 v15, v15\n"
    "v_cvt_pk_bf16_f32 v30, v12, v13\n"
    "v_cvt_pk_bf16_f32 v31, v14, v15\n"
    "v_mfma_f32_16x16x32_bf16 v[8:11], a[16:19], v[32:35], v[52:55]\n"
    "buffer_store_dwordx4 v[132:135], v250, s[44:47], s29 offen sc1\n"
    "v_exp_f32 v0, v0\n"
    "v_exp_f32 v1, v1\n"
    "v_mfma_f32_16x16x32_bf16 v[12:15], a[20:23], v[32:35], v[52:55]\n"
    "v_exp_f32 v2, v2\n"
    "v_cvt_pk_bf16_f32 v24, v0, v1\n"
    "v_mfma_f32_16x16x32_bf16 v[16:19], a[96:99], v[28:31], v[16:19]\n"
    "v_exp_f32 v3, v3\n"
    "v_exp_f32 v4, v4\n"
    "v_mfma_f32_16x16x32_bf16 v[20:23], a[100:103], v[28:31], v[20:23]\n"
    "v_exp_f32 v5, v5\n"
    "v_cvt_pk_bf16_f32 v25, v2, v3\n"
    "v_mfma_f32_16x16x32_bf16 v[240:243], a[128:131], v[28:31], v[240:243]\n"
    "v_exp_f32 v6, v6\n"
    "v_exp_f32 v7, v7\n"
    "v_cvt_pk_bf16_f32 v26, v4, v5\n"
    "v_cvt_pk_bf16_f32 v27, v6, v7\n"
    "v_mfma_f32_16x16x32_bf16 v[0:3], a[8:11], v[32:35], v[52:55]\n"
    "buffer_store_dwordx4 v[108:111], v250, s[44:47], s28 offen offset:3072 sc1\n"
    "v_exp_f32 v8, v8\n"
    ".Lk2_m6749:\n"
    "s_cbranch_execz .Lk2_m7874\n"
    "v_exp_f32 v9, v9\n"
    "v_mfma_f32_16x16x32_bf16 v[4:7], a[12:15], v[32:35], v[52:55]\n"
    "v_exp_f32 v10, v10\n"
    "v_cvt_pk_bf16_f32 v28, v8, v9\n"
    "v_mfma_f32_16x16x32_bf16 v[16:19], a[88:91], v[24:27], v[16:19]\n"
    "v_exp_f32 v11, v11\n"
    "v_exp_f32 v12, v12\n"
    "v_mfma_f32_16x16x32_bf16 v[20:23], a[92:95], v[24:27], v[20:23]\n"
    "v_exp_f32 v13, v13\n"
    "v_cvt_pk_bf16_f32 v29, v10, v11\n"
    "v_mfma_f32_16x16x32_bf16 v[240:243], a[128:131], v[24:27], v[240:243]\n"
    "v_exp_f32 v14, v14\n"
    "v_exp_f32 v15, v15\n"
    "v_cvt_pk_bf16_f32 v30, v12, v13\n"
    "v_cvt_pk_bf16_f32 v31, v14, v15\n"
    "v_mfma_f32_16x16x32_bf16 v[8:11], a[0:3], v[32:35], v[52:55]\n"
    "buffer_store_dwordx4 v[84:87], v250, s[44:47], s28 offen offset:2048 sc1\n"
    "v_exp_f32 v0, v0\n"
    "v_exp_f32 v1, v1\n"
    "v_mfma_f32_16x16x32_bf16 v[12:15], a[4:7], v[32:35], v[52:55]\n"
    "v_exp_f32 v2, v2\n"
    "v_cvt_pk_bf16_f32 v24, v0, v1\n"
    "v_mfma_f32_16x16x32_bf16 v[16:19], a[80:83], v[28:31], v[16:19]\n"
    "v_exp_f32 v3, v3\n"
    "v_exp_f32 v4, v4\n"
    "v_mfma_f32_16x16x32_bf16 v[20:23], a[84:87], v[28:31], v[20:23]\n"
    "v_exp_f32 v5, v5\n"
    "v_cvt_pk_bf16_f32 v25, v2, v3\n"
    "v_mfma_f32_16x16x32_bf16 v[240:243], a[128:131], v[28:31], v[240:243]\n"
    "v_exp_f32 v6, v6\n"
    "v_exp_f32 v7, v7\n"
    "v_cvt_pk_bf16_f32 v26, v4, v5\n"
    "v_cvt_pk_bf16_f32 v27, v6, v7\n"
    "v_exp_f32 v8, v8\n"
    "v_exp_f32 v9, v9\n"
    "v_exp_f32 v10, v10\n"
    "v_cvt_pk_bf16_f32 v28, v8, v9\n"
    "v_mfma_f32_16x16x32_bf16 v[16:19], a[72:75], v[24:27], v[16:19]\n"
    "buffer_store_dwordx4 v[60:63], v250, s[44:47], s28 offen offset:1024 sc1\n"
    "v_exp_f32 v11, v11\n"
    "v_exp_f32 v12, v12\n"
    "v_mfma_f32_16x16x32_bf16 v[20:23], a[76:79], v[24:27], v[20:23]\n"
    "v_exp_f32 v13, v13\n"
    "v_cvt_pk_bf16_f32 v29, v10, v11\n"
    "v_mfma_f32_16x16x32_bf16 v[240:243], a[128:131], v[24:27], v[240:243]\n"
    "v_exp_f32 v14, v14\n"
    "v_exp_f32 v15, v15\n"
    "v_cvt_pk_bf16_f32 v30, v12, v13\n"
    "v_cvt_pk_bf16_f32 v31, v14, v15\n"
    "s_nop 1\n"
    "v_mfma_f32_16x16x32_bf16 v[16:19], a[64:67], v[28:31], v[16:19]\n"
    "v_mfma_f32_16x16x32_bf16 v[20:23], a[68:71], v[28:31], v[20:23]\n"
    "v_mfma_f32_16x16x32_bf16 v[240:243], a[128:131], v[28:31], v[240:243]\n"
    "s_nop 7\n"
    "v_rcp_f32 v248, v240\n"
    "v_max_f32 v255, v255, v240\n"
    "v_pk_mul_f32 v[16:17], v[16:17], v[248:249] op_sel_hi:[1,0]\n"
    "v_pk_mul_f32 v[18:19], v[18:19], v[248:249] op_sel_hi:[1,0]\n"
    "v_pk_mul_f32 v[20:21], v[20:21], v[248:249] op_sel_hi:[1,0]\n"
    "v_pk_mul_f32 v[22:23], v[22:23], v[248:249] op_sel_hi:[1,0]\n"
    "v_cvt_pk_bf16_f32 v36, v16, v17\n"
    "v_cvt_pk_bf16_f32 v37, v18, v19\n"
    "v_cvt_pk_bf16_f32 v38, v20, v21\n"
    "v_cvt_pk_bf16_f32 v39, v22, v23\n"
    "buffer_store_dwordx4 v[36:39], v250, s[44:47], s28 offen sc1\n"
    "v_cmp_ngt_f32 vcc, s96, v255\n"
    "s_nop 0\n"
    "s_cmp_eq_u64 vcc, 0\n"
    "s_cbranch_scc0 .Lk2_fr\n"
    ".Lk2_frok:\n"
    "s_cmp_eq_u32 s88, s67\n"
    "s_cbranch_scc0 .Lk2_bslow\n"
    "s_waitcnt vmcnt(6)\n"
    ".Lk2_bok:\n"
    ".Lk2_join:\n"
    ".Lk2_m6825:\n"
    "s_cbranch_execz .Lk2_m8470\n"
    "v_accvgpr_read_b32 v8, a160\n"
    "v_accvgpr_read_b32 v9, a161\n"
    "s_barrier\n"
    "ds_read_b64_tr_b16 a[0:1], v8 offset:1024\n"
    "ds_read_b64_tr_b16 a[2:3], v8 offset:17408\n"
    "ds_read_b64_tr_b16 a[4:5], v8 offset:5120\n"
    "ds_read_b64_tr_b16 a[6:7], v8 offset:21504\n"
    "ds_read_b64_tr_b16 a[8:9], v9 offset:1024\n"
    "ds_read_b64_tr_b16 a[10:11], v9 offset:17408\n"
    "ds_read_b64_tr_b16 a[12:13], v9 offset:5120\n"
    "ds_read_b64_tr_b16 a[14:15], v9 offset:21504\n"
    "v_accvgpr_read_b32 v10, a162\n"
    "v_accvgpr_read_b32 v11, a163\n"
    "v_accvgpr_read_b32 v14, a166\n"
    "v_accvgpr_read_b32 v15, a167\n"
    "v_accvgpr_read_b32 v28, a198\n"
    "v_readlane_b32 s84, v15, 2\n"
    "v_and_b32 v16, 0x3ff, v14\n"
    "v_add_u32 v14, s67, v14\n"
    "s_mov_b32 s89, 3\n"
    "s_mov_b32 s90, 4\n"
    "s_waitcnt lgkmcnt(4)\n"
    "v_mfma_f32_16x16x32_bf16 v[64:67], a[0:3], a[224:227], 0\n"
    "ds_read_b64_tr_b16 a[16:17], v10 offset:1024\n"
    "v_mfma_f32_16x16x32_bf16 v[64:67], a[4:7], a[240:243], v[64:67]\n"
    "v_readlane_b32 s85, v15, s89\n"
    "v_mfma_f32_16x16x32_bf16 v[112:115], a[4:7], a[244:247], 0\n"
    "ds_read_b64_tr_b16 a[18:19], v10 offset:17408\n"
    "s_add_u32 s89, s89, 1\n"
    "v_mfma_f32_16x16x32_bf16 v[112:115], a[0:3], a[228:231], v[112:115]\n"
    "buffer_load_dwordx4 a[64:67], v14, s[44:47], s84 offen sc1\n"
    "v_mfma_f32_16x16x32_bf16 v[160:163], a[0:3], a[232:235], 0\n"
    "ds_read_b64_tr_b16 a[20:21], v10 offset:5120\n"
    "v_mfma_f32_16x16x32_bf16 v[160:163], a[4:7], a[248:251], v[160:163]\n"
    "v_xor_b32 v7, 2048, v14\n"
    "v_mfma_f32_16x16x32_bf16 v[208:211], a[4:7], a[252:255], 0\n"
    "ds_read_b64_tr_b16 a[22:23], v10 offset:21504\n"
    "s_waitcnt lgkmcnt(4)\n"
    "v_mfma_f32_16x16x32_bf16 v[208:211], a[0:3], a[236:239], v[208:211]\n"
    "buffer_load_dwordx4 a[68:71], v14, s[44:47], s84 offen offset:1024 sc1\n"
    "v_mfma_f32_16x16x32_bf16 v[68:71], a[8:11], a[224:227], 0\n"
    "ds_read_b64_tr_b16 a[24:25], v11 offset:1024\n"
    "v_mfma_f32_16x16x32_bf16 v[68:71], a[12:15], a[240:243], v[68:71]\n"
    "v_readlane_b32 s86, v28, s90\n"
    "v_mfma_f32_16x16x32_bf16 v[116:119], a[12:15], a[244:247], 0\n"
    "ds_read_b64_tr_b16 a[26:27], v11 offset:17408\n"
    "s_add_u32 s90, s90, 1\n"
    "v_mfma_f32_16x16x32_bf16 v[116:119], a[8:11], a[228:231], v[116:119]\n"
    "buffer_load_dwordx4 a[72:75], v7, s[44:47], s84 offen sc1\n"
    "v_mfma_f32_16x16x32_bf16 v[164:167], a[8:11], a[232:235], 0\n"
    "ds_read_b64_tr_b16 a[28:29], v11 offset:5120\n"
    "v_mfma_f32_16x16x32_bf16 v[164:167], a[12:15], a[248:251], v[164:167]\n"
    "v_accvgpr_read_b32 v12, a164\n"
    "v_mfma_f32_16x16x32_bf16 v[212:215], a[12:15], a[252:255], 0\n"
    "ds_read_b64_tr_b16 a[30:31], v11 offset:21504\n"
    "s_waitcnt lgkmcnt(4)\n"
    "v_mfma_f32_16x16x32_bf16 v[212:215], a[8:11], a[236:239], v[212:215]\n"
    "buffer_load_dwordx4 a[76:79], v7, s[44:47], s84 offen offset:1024 sc1\n"
    "v_mfma_f32_16x16x32_bf16 v[72:75], a[16:19], a[224:227], 0\n"
    "ds_read_b64_tr_b16 a[32:33], v8 offset:0\n"
    "v_mfma_f32_16x16x32_bf16 v[72:75], a[20:23], a[240:243], v[72:75]\n"
    "v_readlane_b32 s84, v15, s89\n"
    "v_mfma_f32_16x16x32_bf16 v[120:123], a[20:23], a[244:247], 0\n"
    "ds_read_b64_tr_b16 a[34:35], v8 offset:16384\n"
    "s_add_u32 s89, s89, 1\n"
    "v_mfma_f32_16x16x32_bf16 v[120:123], a[16:19], a[228:231], v[120:123]\n"
    "buffer_load_dwordx4 a[80:83], v14, s[44:47], s85 offen sc1\n"
    "v_mfma_f32_16x16x32_bf16 v[168:171], a[16:19], a[232:235], 0\n"
    "ds_read_b64_tr_b16 a[36:37], v8 offset:4096\n"
    "v_mfma_f32_16x16x32_bf16 v[168:171], a[20:23], a[248:251], v[168:171]\n"
    "v_accvgpr_read_b32 v13, a165\n"
    "v_mfma_f32_16x16x32_bf16 v[216:219], a[20:23], a[252:255], 0\n"
    "ds_read_b64_tr_b16 a[38:39], v8 offset:20480\n"
    "s_waitcnt lgkmcnt(4)\n"
    "v_mfma_f32_16x16x32_bf16 v[216:219], a[16:19], a[236:239], v[216:219]\n"
    "buffer_load_dwordx4 a[84:87], v14, s[44:47], s85 offen offset:1024 sc1\n"
    "v_mfma_f32_16x16x32_bf16 v[76:79], a[24:27], a[224:227], 0\n"
    "ds_read_b64_tr_b16 a[40:41], v9 offset:0\n"
    "v_mfma_f32_16x16x32_bf16 v[76:79], a[28:31], a[240:243], v[76:79]\n"
    "v_readlane_b32 s87, v28, s90\n"
    "v_mfma_f32_16x16x32_bf16 v[124:127], a[28:31], a[244:247], 0\n"
    "ds_read_b64_tr_b16 a[42:43], v9 offset:16384\n"
    "s_add_u32 s90, s90, 1\n"
    "v_mfma_f32_16x16x32_bf16 v[124:127], a[24:27], a[228:231], v[124:127]\n"
    "buffer_load_dwordx4 a[88:91], v7, s[44:47], s85 offen sc1\n"
    "v_mfma_f32_16x16x32_bf16 v[172:175], a[24:27], a[232:235], 0\n"
    "ds_read_b64_tr_b16 a[44:45], v9 offset:4096\n"
    "v_mfma_f32_16x16x32_bf16 v[172:175], a[28:31], a[248:251], v[172:175]\n"
    "s_xor_b32 s95, s52, 0x10000\n"
    "v_mfma_f32_16x16x32_bf16 v[220:223], a[28:31], a[252:255], 0\n"
    "ds_read_b64_tr_b16 a[46:47], v9 offset:20480\n"
    "s_waitcnt lgkmcnt(4)\n"
    "v_mfma_f32_16x16x32_bf16 v[220:223], a[24:27], a[236:239], v[220:223]\n"
    "buffer_load_dwordx4 a[92:95], v7, s[44:47], s85 offen offset:1024 sc1\n"
    "v_mfma_f32_16x16x32_bf16 v[40:43], a[32:35], a[224:227], 0\n"
    "ds_read_b64_tr_b16 a[48:49], v10 offset:0\n"
    "v_mfma_f32_16x16x32_bf16 v[40:43], a[36:39], a[240:243], v[40:43]\n"
    "s_add_u32 m0, s95, 0\n"
    "v_mfma_f32_16x16x32_bf16 v[88:91], a[36:39], a[244:247], 0\n"
    "ds_read_b64_tr_b16 a[50:51], v10 offset:16384\n"
    "v_add_u32 v5, 63488, v12\n"
    "v_mfma_f32_16x16x32_bf16 v[88:91], a[32:35], a[228:231], v[88:91]\n"
    "buffer_load_dwordx4 v12, s[48:51], s86 offen lds\n"
    "v_mfma_f32_16x16x32_bf16 v[136:139], a[32:35], a[232:235], 0\n"
    "ds_read_b64_tr_b16 a[52:53], v10 offset:4096\n"
    "v_mfma_f32_16x16x32_bf16 v[136:139], a[36:39], a[248:251], v[136:139]\n"
    "v_add_u32 v6, 63488, v13\n"
    "v_mfma_f32_16x16x32_bf16 v[184:187], a[36:39], a[252:255], 0\n"
    "ds_read_b64_tr_b16 a[54:55], v10 offset:20480\n"
    "s_waitcnt lgkmcnt(4)\n"
    "v_mfma_f32_16x16x32_bf16 v[184:187], a[32:35], a[236:239], v[184:187]\n"
    "buffer_load_dwordx4 v13, s[48:51], s86 offen offset:1024 lds\n"
    "v_mfma_f32_16x16x32_bf16 v[44:47], a[40:43], a[224:227], 0\n"
    "ds_read_b64_tr_b16 a[56:57], v11 offset:0\n"
    "v_mfma_f32_16x16x32_bf16 v[44:47], a[44:47], a[240:243], v[44:47]\n"
    "v_bfe_u32 v19, v16, 4, 2\n"
    "v_mfma_f32_16x16x32_bf16 v[92:95], a[44:47], a[244:247], 0\n"
    "ds_read_b64_tr_b16 a[58:59], v11 offset:16384\n"
    "v_and_b32 v17, 0x3c0, v16\n"
    "v_mfma_f32_16x16x32_bf16 v[92:95], a[40:43], a[228:231], v[92:95]\n"
    "buffer_load_dwordx4 v5, s[48:51], s86 offen offset:2048 lds\n"
    "v_mfma_f32_16x16x32_bf16 v[140:143], a[40:43], a[232:235], 0\n"
    "ds_read_b64_tr_b16 a[60:61], v11 offset:4096\n"
    "v_mfma_f32_16x16x32_bf16 v[140:143], a[44:47], a[248:251], v[140:143]\n"
    "v_lshlrev_b32 v19, 1, v19\n"
    "v_mfma_f32_16x16x32_bf16 v[188:191], a[44:47], a[252:255], 0\n"
    "ds_read_b64_tr_b16 a[62:63], v11 offset:20480\n"
    "s_waitcnt lgkmcnt(4)\n"
    "v_mfma_f32_16x16x32_bf16 v[188:191], a[40:43], a[236:239], v[188:191]\n"
    "buffer_load_dwordx4 v6, s[48:51], s86 offen offset:3072 lds\n"
    "v_mfma_f32_16x16x32_bf16 v[48:51], a[48:51], a[224:227], 0\n"
    "ds_read_b64_tr_b16 a[0:1], v8 offset:33792\n"
    "v_mfma_f32_16x16x32_bf16 v[48:51], a[52:55], a[240:243], v[48:51]\n"
    "s_add_u32 m0, s95, 4096\n"
    "v_mfma_f32_16x16x32_bf16 v[96:99], a[52:55], a[244:247], 0\n"
    "ds_read_b64_tr_b16 a[2:3], v8 offset:50176\n"
    "v_add_lshl_u32 v17, v17, v19, 1\n"
    "v_mfma_f32_16x16x32_bf16 v[96:99], a[48:51], a[228:231], v[96:99]\n"
    "buffer_load_dwordx4 v12, s[48:51], s87 offen lds\n"
    "v_mfma_f32_16x16x32_bf16 v[144:147], a[48:51], a[232:235], 0\n"
    "ds_read_b64_tr_b16 a[4:5], v8 offset:37888\n"
    "v_mfma_f32_16x16x32_bf16 v[144:147], a[52:55], a[248:251], v[144:147]\n"
    "v_and_b32 v18, 0x780, v17\n"
    "v_mfma_f32_16x16x32_bf16 v[192:195], a[52:55], a[252:255], 0\n"
    "ds_read_b64_tr_b16 a[6:7], v8 offset:54272\n"
    "s_waitcnt lgkmcnt(4)\n"
    "v_mfma_f32_16x16x32_bf16 v[192:195], a[48:51], a[236:239], v[192:195]\n"
    "buffer_load_dwordx4 v13, s[48:51], s87 offen offset:1024 lds\n"
    "v_mfma_f32_16x16x32_bf16 v[52:55], a[56:59], a[224:227], 0\n"
    "ds_read_b64_tr_b16 a[8:9], v9 offset:33792\n"
    "v_mfma_f32_16x16x32_bf16 v[52:55], a[60:63], a[240:243], v[52:55]\n"
    "s_mov_b32 s94, 1024\n"
    "v_mfma_f32_16x16x32_bf16 v[100:103], a[60:63], a[244:247], 0\n"
    "ds_read_b64_tr_b16 a[10:11], v9 offset:50176\n"
    "s_bitcmp1_b32 s35, 2\n"
    "s_cselect_b32 s93, 768, s94\n"
    "s_cselect_b32 s94, 128, 0\n"
    "v_mfma_f32_16x16x32_bf16 v[100:103], a[56:59], a[228:231], v[100:103]\n"
    "buffer_load_dwordx4 v5, s[48:51], s87 offen offset:2048 lds\n"
    "v_mfma_f32_16x16x32_bf16 v[148:151], a[56:59], a[232:235], 0\n"
    "ds_read_b64_tr_b16 a[12:13], v9 offset:37888\n"
    "v_mfma_f32_16x16x32_bf16 v[148:151], a[60:63], a[248:251], v[148:151]\n"
    "v_min_u32 v18, s93, v18\n"
    "v_mfma_f32_16x16x32_bf16 v[196:199], a[60:63], a[252:255], 0\n"
    "ds_read_b64_tr_b16 a[14:15], v9 offset:54272\n"
    "s_waitcnt vmcnt(7) lgkmcnt(4)\n"
    "v_mfma_f32_16x16x32_bf16 v[196:199], a[56:59], a[236:239], v[196:199]\n"
    "buffer_load_dwordx4 v6, s[48:51], s87 offen offset:3072 lds\n"
    "v_mfma_f32_16x16x32_bf16 v[64:67], a[0:3], a[64:67], v[64:67]\n"
    "ds_read_b64_tr_b16 a[16:17], v10 offset:33792\n"
    "v_mfma_f32_16x16x32_bf16 v[64:67], a[4:7], a[80:83], v[64:67]\n"
    "v_readlane_b32 s85, v15, s89\n"
    "v_mfma_f32_16x16x32_bf16 v[112:115], a[4:7], a[84:87], v[112:115]\n"
    "ds_read_b64_tr_b16 a[18:19], v10 offset:50176\n"
    "s_add_u32 s89, s89, 1\n"
    "v_mfma_f32_16x16x32_bf16 v[112:115], a[0:3], a[68:71], v[112:115]\n"
    "buffer_load_dwordx4 a[224:227], v14, s[44:47], s84 offen sc1\n"
    "v_mfma_f32_16x16x32_bf16 v[160:163], a[0:3], a[72:75], v[160:163]\n"
    "ds_read_b64_tr_b16 a[20:21], v10 offset:37888\n"
    "v_mfma_f32_16x16x32_bf16 v[160:163], a[4:7], a[88:91], v[160:163]\n"
    "v_and_or_b32 v18, v17, 12, v18\n"
    "v_mfma_f32_16x16x32_bf16 v[208:211], a[4:7], a[92:95], v[208:211]\n"
    "ds_read_b64_tr_b16 a[22:23], v10 offset:54272\n"
    "s_waitcnt lgkmcnt(4)\n"
    "v_mfma_f32_16x16x32_bf16 v[208:211], a[0:3], a[76:79], v[208:211]\n"
    "buffer_load_dwordx4 a[228:231], v14, s[44:47], s84 offen offset:1024 sc1\n"
    "v_mfma_f32_16x16x32_bf16 v[68:71], a[8:11], a[64:67], v[68:71]\n"
    "ds_read_b64_tr_b16 a[24:25], v11 offset:33792\n"
    "v_mfma_f32_16x16x32_bf16 v[68:71], a[12:15], a[80:83], v[68:71]\n"
    "v_readlane_b32 s86, v28, s90\n"
    "v_mfma_f32_16x16x32_bf16 v[116:119], a[12:15], a[84:87], v[116:119]\n"
    "ds_read_b64_tr_b16 a[26:27], v11 offset:50176\n"
    "s_add_u32 s90, s90, 1\n"
    "v_mfma_f32_16x16x32_bf16 v[116:119], a[8:11], a[68:71], v[116:119]\n"
    "buffer_load_dwordx4 a[232:235], v7, s[44:47], s84 offen sc1\n"
    "v_mfma_f32_16x16x32_bf16 v[164:167], a[8:11], a[72:75], v[164:167]\n"
    "ds_read_b64_tr_b16 a[28:29], v11 offset:37888\n"
    "v_mfma_f32_16x16x32_bf16 v[164:167], a[12:15], a[88:91], v[164:167]\n"
    "s_add_u32 s94, s94, 2048\n"
    "v_mfma_f32_16x16x32_bf16 v[212:215], a[12:15], a[92:95], v[212:215]\n"
    "ds_read_b64_tr_b16 a[30:31], v11 offset:54272\n"
    "s_waitcnt lgkmcnt(4)\n"
    "v_mfma_f32_16x16x32_bf16 v[212:215], a[8:11], a[76:79], v[212:215]\n"
    "buffer_load_dwordx4 a[236:239], v7, s[44:47], s84 offen offset:1024 sc1\n"
    "v_mfma_f32_16x16x32_bf16 v[72:75], a[16:19], a[64:67], v[72:75]\n"
    "ds_read_b64_tr_b16 a[32:33], v8 offset:32768\n"
    "v_mfma_f32_16x16x32_bf16 v[72:75], a[20:23], a[80:83], v[72:75]\n"
    "v_readlane_b32 s84, v15, s89\n"
    "v_mfma_f32_16x16x32_bf16 v[120:123], a[20:23], a[84:87], v[120:123]\n"
    "ds_read_b64_tr_b16 a[34:35], v8 offset:49152\n"
    "s_add_u32 s89, s89, 1\n"
    "v_mfma_f32_16x16x32_bf16 v[120:123], a[16:19], a[68:71], v[120:123]\n"
    "buffer_load_dwordx4 a[240:243], v14, s[44:47], s85 offen sc1\n"
    "v_mfma_f32_16x16x32_bf16 v[168:171], a[16:19], a[72:75], v[168:171]\n"
    "ds_read_b64_tr_b16 a[36:37], v8 offset:36864\n"
    "v_mfma_f32_16x16x32_bf16 v[168:171], a[20:23], a[88:91], v[168:171]\n"
    "v_and_b32 v17, 0x3ff, v17\n"
    "v_mfma_f32_16x16x32_bf16 v[216:219], a[20:23], a[92:95], v[216:219]\n"
    "ds_read_b64_tr_b16 a[38:39], v8 offset:53248\n"
    "s_waitcnt lgkmcnt(4)\n"
    "v_mfma_f32_16x16x32_bf16 v[216:219], a[16:19], a[76:79], v[216:219]\n"
    "buffer_load_dwordx4 a[244:247], v14, s[44:47], s85 offen offset:1024 sc1\n"
    "v_mfma_f32_16x16x32_bf16 v[76:79], a[24:27], a[64:67], v[76:79]\n"
    "ds_read_b64_tr_b16 a[40:41], v9 offset:32768\n"
    "v_mfma_f32_16x16x32_bf16 v[76:79], a[28:31], a[80:83], v[76:79]\n"
    "v_readlane_b32 s87, v28, s90\n"
    "v_mfma_f32_16x16x32_bf16 v[124:127], a[28:31], a[84:87], v[124:127]\n"
    "ds_read_b64_tr_b16 a[42:43], v9 offset:49152\n"
    "s_add_u32 s90, s90, 1\n"
    "v_mfma_f32_16x16x32_bf16 v[124:127], a[24:27], a[68:71], v[124:127]\n"
    "buffer_load_dwordx4 a[248:251], v7, s[44:47], s85 offen sc1\n"
    "v_mfma_f32_16x16x32_bf16 v[172:175], a[24:27], a[72:75], v[172:175]\n"
    "ds_read_b64_tr_b16 a[44:45], v9 offset:36864\n"
    "v_mfma_f32_16x16x32_bf16 v[172:175], a[28:31], a[88:91], v[172:175]\n"
    "v_xor_b32 v8, 0x10000, v8\n"
    "v_mfma_f32_16x16x32_bf16 v[220:223], a[28:31], a[92:95], v[220:223]\n"
    "ds_read_b64_tr_b16 a[46:47], v9 offset:53248\n"
    "s_waitcnt lgkmcnt(4)\n"
    "v_mfma_f32_16x16x32_bf16 v[220:223], a[24:27], a[76:79], v[220:223]\n"
    "buffer_load_dwordx4 a[252:255], v7, s[44:47], s85 offen offset:1024 sc1\n"
    "v_mfma_f32_16x16x32_bf16 v[40:43], a[32:35], a[64:67], v[40:43]\n"
    "ds_read_b64_tr_b16 a[48:49], v10 offset:32768\n"
    "v_mfma_f32_16x16x32_bf16 v[40:43], a[36:39], a[80:83], v[40:43]\n"
    "s_add_u32 m0, s95, 32768\n"
    "v_mfma_f32_16x16x32_bf16 v[88:91], a[36:39], a[84:87], v[88:91]\n"
    "ds_read_b64_tr_b16 a[50:51], v10 offset:49152\n"
    "v_add_u32 v18, s94, v18\n"
    "v_mfma_f32_16x16x32_bf16 v[88:91], a[32:35], a[68:71], v[88:91]\n"
    "buffer_load_dwordx4 v12, s[48:51], s86 offen lds\n"
    "v_mfma_f32_16x16x32_bf16 v[136:139], a[32:35], a[72:75], v[136:139]\n"
    "ds_read_b64_tr_b16 a[52:53], v10 offset:36864\n"
    "v_mfma_f32_16x16x32_bf16 v[136:139], a[36:39], a[88:91], v[136:139]\n"
    "v_xor_b32 v9, 0x10000, v9\n"
    "v_mfma_f32_16x16x32_bf16 v[184:187], a[36:39], a[92:95], v[184:187]\n"
    "ds_read_b64_tr_b16 a[54:55], v10 offset:53248\n"
    "s_waitcnt vmcnt(9) lgkmcnt(4)\n"
    "v_mfma_f32_16x16x32_bf16 v[184:187], a[32:35], a[76:79], v[184:187]\n"
    "buffer_load_dwordx4 v13, s[48:51], s86 offen offset:1024 lds\n"
    "v_mfma_f32_16x16x32_bf16 v[44:47], a[40:43], a[64:67], v[44:47]\n"
    "ds_read_b64_tr_b16 a[56:57], v11 offset:32768\n"
    "v_mfma_f32_16x16x32_bf16 v[44:47], a[44:47], a[80:83], v[44:47]\n"
    "s_cmp_lg_u32 s52, 0\n"
    "s_cselect_b32 s27, -1, s27\n"
    "v_mfma_f32_16x16x32_bf16 v[92:95], a[44:47], a[84:87], v[92:95]\n"
    "ds_read_b64_tr_b16 a[58:59], v11 offset:49152\n"
    "v_xor_b32 v10, 0x10000, v10\n"
    "v_mfma_f32_16x16x32_bf16 v[92:95], a[40:43], a[68:71], v[92:95]\n"
    "buffer_load_dwordx4 v5, s[48:51], s86 offen offset:2048 lds\n"
    "v_mfma_f32_16x16x32_bf16 v[140:143], a[40:43], a[72:75], v[140:143]\n"
    "ds_read_b64_tr_b16 a[60:61], v11 offset:36864\n"
    "v_mfma_f32_16x16x32_bf16 v[140:143], a[44:47], a[88:91], v[140:143]\n"
    "s_barrier\n"
    "v_mfma_f32_16x16x32_bf16 v[188:191], a[44:47], a[92:95], v[188:191]\n"
    "ds_read_b64_tr_b16 a[62:63], v11 offset:53248\n"
    "s_waitcnt lgkmcnt(4)\n"
    "v_mfma_f32_16x16x32_bf16 v[188:191], a[40:43], a[76:79], v[188:191]\n"
    "buffer_load_dwordx4 v6, s[48:51], s86 offen offset:3072 lds\n"
    "v_mfma_f32_16x16x32_bf16 v[48:51], a[48:51], a[64:67], v[48:51]\n"
    "ds_read_b64_tr_b16 a[0:1], v8 offset:1024\n"
    "v_mfma_f32_16x16x32_bf16 v[48:51], a[52:55], a[80:83], v[48:51]\n"
    "s_add_u32 m0, s95, 36864\n"
    "v_mfma_f32_16x16x32_bf16 v[96:99], a[52:55], a[84:87], v[96:99]\n"
    "ds_read_b64_tr_b16 a[2:3], v8 offset:17408\n"
    "v_accvgpr_read_b32 v0, a194\n"
    "s_add_u32 s33, s92, 10\n"
    "v_mov_b32 v22, s33\n"
    "s_waitcnt vmcnt(28)\n"
    "s_mov_b64 exec, 1\n"
    "buffer_store_dword v22, off, s[44:47], s66 sc1\n"
    "s_mov_b64 exec, -1\n"
    "v_mfma_f32_16x16x32_bf16 v[96:99], a[48:51], a[68:71], v[96:99]\n"
    "buffer_load_dwordx4 v12, s[48:51], s87 offen lds\n"
    "v_mfma_f32_16x16x32_bf16 v[144:147], a[48:51], a[72:75], v[144:147]\n"
    "ds_read_b64_tr_b16 a[4:5], v8 offset:5120\n"
    "v_mfma_f32_16x16x32_bf16 v[144:147], a[52:55], a[88:91], v[144:147]\n"
    "v_xor_b32 v11, 0x10000, v11\n"
    "v_mfma_f32_16x16x32_bf16 v[192:195], a[52:55], a[92:95], v[192:195]\n"
    "ds_read_b64_tr_b16 a[6:7], v8 offset:21504\n"
    "s_waitcnt lgkmcnt(4)\n"
    "v_mfma_f32_16x16x32_bf16 v[192:195], a[48:51], a[76:79], v[192:195]\n"
    "buffer_load_dwordx4 v13, s[48:51], s87 offen offset:1024 lds\n"
    "v_mfma_f32_16x16x32_bf16 v[52:55], a[56:59], a[64:67], v[52:55]\n"
    "ds_read_b64_tr_b16 a[8:9], v9 offset:1024\n"
    "v_mfma_f32_16x16x32_bf16 v[52:55], a[60:63], a[80:83], v[52:55]\n"
    "s_xor_b32 s95, s95, 0x10000\n"
    "v_mfma_f32_16x16x32_bf16 v[100:103], a[60:63], a[84:87], v[100:103]\n"
    "ds_read_b64_tr_b16 a[10:11], v9 offset:17408\n"
    "v_accvgpr_read_b32 v4, a195\n"
    "v_mfma_f32_16x16x32_bf16 v[100:103], a[56:59], a[68:71], v[100:103]\n"
    "buffer_load_dwordx4 v5, s[48:51], s87 offen offset:2048 lds\n"
    "v_mfma_f32_16x16x32_bf16 v[148:151], a[56:59], a[72:75], v[148:151]\n"
    "ds_read_b64_tr_b16 a[12:13], v9 offset:5120\n"
    "v_mfma_f32_16x16x32_bf16 v[148:151], a[60:63], a[88:91], v[148:151]\n"
    "v_xor_b32 v1, 32, v0\n"
    "v_mfma_f32_16x16x32_bf16 v[196:199], a[60:63], a[92:95], v[196:199]\n"
    "ds_read_b64_tr_b16 a[14:15], v9 offset:21504\n"
    "s_waitcnt vmcnt(8) lgkmcnt(4)\n"
    "v_mfma_f32_16x16x32_bf16 v[196:199], a[56:59], a[76:79], v[196:199]\n"
    "buffer_load_dwordx4 v6, s[48:51], s87 offen offset:3072 lds\n"
    "v_mfma_f32_16x16x32_bf16 v[64:67], a[0:3], a[224:227], v[64:67]\n"
    "ds_read_b64_tr_b16 a[16:17], v10 offset:1024\n"
    "v_mfma_f32_16x16x32_bf16 v[64:67], a[4:7], a[240:243], v[64:67]\n"
    "v_readlane_b32 s85, v15, s89\n"
    "v_mfma_f32_16x16x32_bf16 v[112:115], a[4:7], a[244:247], v[112:115]\n"
    "ds_read_b64_tr_b16 a[18:19], v10 offset:17408\n"
    "s_add_u32 s89, s89, 1\n"
    "v_mfma_f32_16x16x32_bf16 v[112:115], a[0:3], a[228:231], v[112:115]\n"
    "buffer_load_dwordx4 a[64:67], v14, s[44:47], s84 offen sc1\n"
    "v_mfma_f32_16x16x32_bf16 v[160:163], a[0:3], a[232:235], v[160:163]\n"
    "ds_read_b64_tr_b16 a[20:21], v10 offset:5120\n"
    "v_mfma_f32_16x16x32_bf16 v[160:163], a[4:7], a[248:251], v[160:163]\n"
    "s_cmp_eq_u32 s27, 0\n"
    "s_cbranch_scc1 .Lk2_pha\n"
    ".Lk2_pra:\n"
    "v_mfma_f32_16x16x32_bf16 v[208:211], a[4:7], a[252:255], v[208:211]\n"
    "ds_read_b64_tr_b16 a[22:23], v10 offset:21504\n"
    "s_waitcnt lgkmcnt(4)\n"
    "v_mfma_f32_16x16x32_bf16 v[208:211], a[0:3], a[236:239], v[208:211]\n"
    "buffer_load_dwordx4 a[68:71], v14, s[44:47], s84 offen offset:1024 sc1\n"
    "v_mfma_f32_16x16x32_bf16 v[68:71], a[8:11], a[224:227], v[68:71]\n"
    "ds_read_b64_tr_b16 a[24:25], v11 offset:1024\n"
    "v_mfma_f32_16x16x32_bf16 v[68:71], a[12:15], a[240:243], v[68:71]\n"
    "v_readlane_b32 s86, v28, s90\n"
    "v_mfma_f32_16x16x32_bf16 v[116:119], a[12:15], a[244:247], v[116:119]\n"
    "ds_read_b64_tr_b16 a[26:27], v11 offset:17408\n"
    "s_add_u32 s90, s90, 1\n"
    "v_mfma_f32_16x16x32_bf16 v[116:119], a[8:11], a[228:231], v[116:119]\n"
    "buffer_load_dwordx4 a[72:75], v7, s[44:47], s84 offen sc1\n"
    "v_mfma_f32_16x16x32_bf16 v[164:167], a[8:11], a[232:235], v[164:167]\n"
    "ds_read_b64_tr_b16 a[28:29], v11 offset:5120\n"
    "v_mfma_f32_16x16x32_bf16 v[164:167], a[12:15], a[248:251], v[164:167]\n"
    "v_xor_b32 v2, 64, v0\n"
    "v_mfma_f32_16x16x32_bf16 v[212:215], a[12:15], a[252:255], v[212:215]\n"
    "ds_read_b64_tr_b16 a[30:31], v11 offset:21504\n"
    "s_waitcnt lgkmcnt(4)\n"
    "v_mfma_f32_16x16x32_bf16 v[212:215], a[8:11], a[236:239], v[212:215]\n"
    "buffer_load_dwordx4 a[76:79], v7, s[44:47], s84 offen offset:1024 sc1\n"
    "v_mfma_f32_16x16x32_bf16 v[72:75], a[16:19], a[224:227], v[72:75]\n"
    "ds_read_b64_tr_b16 a[32:33], v8 offset:0\n"
    "v_mfma_f32_16x16x32_bf16 v[72:75], a[20:23], a[240:243], v[72:75]\n"
    "v_readlane_b32 s84, v15, s89\n"
    "v_mfma_f32_16x16x32_bf16 v[120:123], a[20:23], a[244:247], v[120:123]\n"
    "ds_read_b64_tr_b16 a[34:35], v8 offset:16384\n"
    "s_add_u32 s89, s89, 1\n"
    "v_mfma_f32_16x16x32_bf16 v[120:123], a[16:19], a[228:231], v[120:123]\n"
    "buffer_load_dwordx4 a[80:83], v14, s[44:47], s85 offen sc1\n"
    "v_mfma_f32_16x16x32_bf16 v[168:171], a[16:19], a[232:235], v[168:171]\n"
    "ds_read_b64_tr_b16 a[36:37], v8 offset:4096\n"
    "v_mfma_f32_16x16x32_bf16 v[168:171], a[20:23], a[248:251], v[168:171]\n"
    "v_xor_b32 v3, 0x60, v0\n"
    "v_mfma_f32_16x16x32_bf16 v[216:219], a[20:23], a[252:255], v[216:219]\n"
    "ds_read_b64_tr_b16 a[38:39], v8 offset:20480\n"
    "s_waitcnt lgkmcnt(4)\n"
    "v_mfma_f32_16x16x32_bf16 v[216:219], a[16:19], a[236:239], v[216:219]\n"
    "buffer_load_dwordx4 a[84:87], v14, s[44:47], s85 offen offset:1024 sc1\n"
    "v_mfma_f32_16x16x32_bf16 v[76:79], a[24:27], a[224:227], v[76:79]\n"
    "ds_read_b64 a[96:97], v0 offset:0\n"
    "ds_read_b64 a[98:99], v4 offset:0\n"
    "ds_read_b64 a[112:113], v0 offset:2048\n"
    "ds_read_b64 a[114:115], v4 offset:0\n"
    "ds_read_b64_tr_b16 a[40:41], v9 offset:0\n"
    "v_mfma_f32_16x16x32_bf16 v[76:79], a[28:31], a[240:243], v[76:79]\n"
    "v_readlane_b32 s87, v28, s90\n"
    "v_mfma_f32_16x16x32_bf16 v[124:127], a[28:31], a[244:247], v[124:127]\n"
    "ds_read_b64_tr_b16 a[42:43], v9 offset:16384\n"
    "s_add_u32 s90, s90, 1\n"
    "v_mfma_f32_16x16x32_bf16 v[124:127], a[24:27], a[228:231], v[124:127]\n"
    "buffer_load_dwordx4 a[88:91], v7, s[44:47], s85 offen sc1\n"
    "v_mfma_f32_16x16x32_bf16 v[172:175], a[24:27], a[232:235], v[172:175]\n"
    "ds_read_b64_tr_b16 a[44:45], v9 offset:4096\n"
    "v_mfma_f32_16x16x32_bf16 v[172:175], a[28:31], a[248:251], v[172:175]\n"
    "v_mfma_f32_16x16x32_bf16 v[220:223], a[28:31], a[252:255], v[220:223]\n"
    "ds_read_b64_tr_b16 a[46:47], v9 offset:20480\n"
    "s_waitcnt lgkmcnt(4)\n"
    "v_mfma_f32_16x16x32_bf16 v[220:223], a[24:27], a[236:239], v[220:223]\n"
    "buffer_load_dwordx4 a[92:95], v7, s[44:47], s85 offen offset:1024 sc1\n"
    "v_mfma_f32_16x16x32_bf16 v[40:43], a[32:35], a[224:227], v[40:43]\n"
    "ds_read_b64 a[100:101], v1 offset:0\n"
    "ds_read_b64 a[102:103], v4 offset:32\n"
    "ds_read_b64 a[116:117], v1 offset:2048\n"
    "ds_read_b64 a[118:119], v4 offset:32\n"
    "ds_read_b64_tr_b16 a[48:49], v10 offset:0\n"
    "v_mfma_f32_16x16x32_bf16 v[40:43], a[36:39], a[240:243], v[40:43]\n"
    "s_add_u32 m0, s95, 0\n"
    "v_mfma_f32_16x16x32_bf16 v[88:91], a[36:39], a[244:247], v[88:91]\n"
    "ds_read_b64_tr_b16 a[50:51], v10 offset:16384\n"
    "v_mfma_f32_16x16x32_bf16 v[88:91], a[32:35], a[228:231], v[88:91]\n"
    "buffer_load_dwordx4 v12, s[48:51], s86 offen lds\n"
    "v_mfma_f32_16x16x32_bf16 v[136:139], a[32:35], a[232:235], v[136:139]\n"
    "ds_read_b64_tr_b16 a[52:53], v10 offset:4096\n"
    "v_mfma_f32_16x16x32_bf16 v[136:139], a[36:39], a[248:251], v[136:139]\n"
    "v_mfma_f32_16x16x32_bf16 v[184:187], a[36:39], a[252:255], v[184:187]\n"
    "ds_read_b64_tr_b16 a[54:55], v10 offset:20480\n"
    "s_waitcnt vmcnt(8) lgkmcnt(4)\n"
    "v_mfma_f32_16x16x32_bf16 v[184:187], a[32:35], a[236:239], v[184:187]\n"
    "buffer_load_dwordx4 v13, s[48:51], s86 offen offset:1024 lds\n"
    "v_mfma_f32_16x16x32_bf16 v[44:47], a[40:43], a[224:227], v[44:47]\n"
    "ds_read_b64 a[104:105], v2 offset:0\n"
    "ds_read_b64 a[106:107], v4 offset:64\n"
    "ds_read_b64 a[120:121], v2 offset:2048\n"
    "ds_read_b64 a[122:123], v4 offset:64\n"
    "ds_read_b64_tr_b16 a[56:57], v11 offset:0\n"
    "v_mfma_f32_16x16x32_bf16 v[44:47], a[44:47], a[240:243], v[44:47]\n"
    "s_cmp_eq_u32 s27, 0\n"
    "v_mfma_f32_16x16x32_bf16 v[92:95], a[44:47], a[244:247], v[92:95]\n"
    "ds_read_b64_tr_b16 a[58:59], v11 offset:16384\n"
    "s_cbranch_scc1 .Lk2_phb\n"
    ".Lk2_pok:\n"
    "v_mfma_f32_16x16x32_bf16 v[92:95], a[40:43], a[228:231], v[92:95]\n"
    "buffer_load_dwordx4 v5, s[48:51], s86 offen offset:2048 lds\n"
    "v_mfma_f32_16x16x32_bf16 v[140:143], a[40:43], a[232:235], v[140:143]\n"
    "ds_read_b64_tr_b16 a[60:61], v11 offset:4096\n"
    "v_mfma_f32_16x16x32_bf16 v[140:143], a[44:47], a[248:251], v[140:143]\n"
    "s_barrier\n"
    "v_mfma_f32_16x16x32_bf16 v[188:191], a[44:47], a[252:255], v[188:191]\n"
    "ds_read_b64_tr_b16 a[62:63], v11 offset:20480\n"
    "s_waitcnt lgkmcnt(4)\n"
    "v_mfma_f32_16x16x32_bf16 v[188:191], a[40:43], a[236:239], v[188:191]\n"
    "buffer_load_dwordx4 v6, s[48:51], s86 offen offset:3072 lds\n"
    "v_mfma_f32_16x16x32_bf16 v[48:51], a[48:51], a[224:227], v[48:51]\n"
    "ds_read_b64 a[108:109], v3 offset:0\n"
    "ds_read_b64 a[110:111], v4 offset:96\n"
    "ds_read_b64 a[124:125], v3 offset:2048\n"
    "ds_read_b64 a[126:127], v4 offset:96\n"
    "ds_read_b64_tr_b16 a[0:1], v8 offset:33792\n"
    "v_mfma_f32_16x16x32_bf16 v[48:51], a[52:55], a[240:243], v[48:51]\n"
    "s_add_u32 m0, s95, 4096\n"
    "v_mfma_f32_16x16x32_bf16 v[96:99], a[52:55], a[244:247], v[96:99]\n"
    "ds_read_b64_tr_b16 a[2:3], v8 offset:50176\n"
    "s_cmp_lg_u32 s52, 0\n"
    "s_cselect_b32 s27, 0, 18\n"
    "s_or_b32 s31, s27, 16\n"
    "v_mfma_f32_16x16x32_bf16 v[96:99], a[48:51], a[228:231], v[96:99]\n"
    "buffer_load_dwordx4 v12, s[48:51], s87 offen lds\n"
    "v_mfma_f32_16x16x32_bf16 v[144:147], a[48:51], a[232:235], v[144:147]\n"
    "ds_read_b64_tr_b16 a[4:5], v8 offset:37888\n"
    "v_mfma_f32_16x16x32_bf16 v[144:147], a[52:55], a[248:251], v[144:147]\n"
    "s_bitcmp1_b32 s35, 2\n"
    "s_cselect_b32 s93, 6, 6\n"
    "v_mfma_f32_16x16x32_bf16 v[192:195], a[52:55], a[252:255], v[192:195]\n"
    "ds_read_b64_tr_b16 a[6:7], v8 offset:54272\n"
    "s_waitcnt lgkmcnt(4)\n"
    "v_mfma_f32_16x16x32_bf16 v[192:195], a[48:51], a[236:239], v[192:195]\n"
    "buffer_load_dwordx4 v13, s[48:51], s87 offen offset:1024 lds\n"
    "v_mfma_f32_16x16x32_bf16 v[52:55], a[56:59], a[224:227], v[52:55]\n"
    "ds_read_b64_tr_b16 a[8:9], v9 offset:33792\n"
    "v_mfma_f32_16x16x32_bf16 v[52:55], a[60:63], a[240:243], v[52:55]\n"
    "s_mov_b32 s88, 0\n"
    "v_mfma_f32_16x16x32_bf16 v[100:103], a[60:63], a[244:247], v[100:103]\n"
    "ds_read_b64_tr_b16 a[10:11], v9 offset:50176\n"
    "v_mfma_f32_16x16x32_bf16 v[100:103], a[56:59], a[228:231], v[100:103]\n"
    "buffer_load_dwordx4 v5, s[48:51], s87 offen offset:2048 lds\n"
    "v_mfma_f32_16x16x32_bf16 v[148:151], a[56:59], a[232:235], v[148:151]\n"
    "ds_read_b64_tr_b16 a[12:13], v9 offset:37888\n"
    "v_mfma_f32_16x16x32_bf16 v[148:151], a[60:63], a[248:251], v[148:151]\n"
    "v_mfma_f32_16x16x32_bf16 v[196:199], a[60:63], a[252:255], v[196:199]\n"
    "ds_read_b64_tr_b16 a[14:15], v9 offset:54272\n"
    "s_waitcnt vmcnt(7) lgkmcnt(4)\n"
    "v_mfma_f32_16x16x32_bf16 v[196:199], a[56:59], a[236:239], v[196:199]\n"
    "buffer_load_dwordx4 v6, s[48:51], s87 offen offset:3072 lds\n"
    ".Lk2_p2loop:\n"
    "v_mfma_f32_16x16x32_bf16 v[64:67], a[0:3], a[64:67], v[64:67]\n"
    "ds_read_b64_tr_b16 a[16:17], v10 offset:33792\n"
    "v_mfma_f32_16x16x32_bf16 v[64:67], a[4:7], a[80:83], v[64:67]\n"
    "v_readlane_b32 s85, v15, s89\n"
    "v_mfma_f32_16x16x32_bf16 v[112:115], a[4:7], a[84:87], v[112:115]\n"
    "ds_read_b64_tr_b16 a[18:19], v10 offset:50176\n"
    "s_add_u32 s89, s89, 1\n"
    "v_mfma_f32_16x16x32_bf16 v[112:115], a[0:3], a[68:71], v[112:115]\n"
    "buffer_load_dwordx4 a[224:227], v14, s[44:47], s84 offen sc1\n"
    "v_mfma_f32_16x16x32_bf16 v[160:163], a[0:3], a[72:75], v[160:163]\n"
    "ds_read_b64_tr_b16 a[20:21], v10 offset:37888\n"
    "v_mfma_f32_16x16x32_bf16 v[160:163], a[4:7], a[88:91], v[160:163]\n"
    "v_mfma_f32_16x16x32_bf16 v[208:211], a[4:7], a[92:95], v[208:211]\n"
    ".Lk2_m7320:\n"
    "s_cbranch_execz .Lk2_m8623\n"
    "ds_read_b64_tr_b16 a[22:23], v10 offset:54272\n"
    "s_waitcnt lgkmcnt(4)\n"
    "v_mfma_f32_16x16x32_bf16 v[208:211], a[0:3], a[76:79], v[208:211]\n"
    "buffer_load_dwordx4 a[228:231], v14, s[44:47], s84 offen offset:1024 sc1\n"
    "v_mfma_f32_16x16x32_bf16 v[68:71], a[8:11], a[64:67], v[68:71]\n"
    "ds_read_b64_tr_b16 a[24:25], v11 offset:33792\n"
    "v_mfma_f32_16x16x32_bf16 v[68:71], a[12:15], a[80:83], v[68:71]\n"
    "v_readlane_b32 s86, v28, s90\n"
    "v_mfma_f32_16x16x32_bf16 v[116:119], a[12:15], a[84:87], v[116:119]\n"
    "ds_read_b64_tr_b16 a[26:27], v11 offset:50176\n"
    "s_add_u32 s90, s90, 1\n"
    "v_mfma_f32_16x16x32_bf16 v[116:119], a[8:11], a[68:71], v[116:119]\n"
    "buffer_load_dwordx4 a[232:235], v7, s[44:47], s84 offen sc1\n"
    "v_mfma_f32_16x16x32_bf16 v[164:167], a[8:11], a[72:75], v[164:167]\n"
    "ds_read_b64_tr_b16 a[28:29], v11 offset:37888\n"
    "v_mfma_f32_16x16x32_bf16 v[164:167], a[12:15], a[88:91], v[164:167]\n"
    "v_mfma_f32_16x16x32_bf16 v[212:215], a[12:15], a[92:95], v[212:215]\n"
    "ds_read_b64_tr_b16 a[30:31], v11 offset:54272\n"
    "s_waitcnt lgkmcnt(4)\n"
    "v_mfma_f32_16x16x32_bf16 v[212:215], a[8:11], a[76:79], v[212:215]\n"
    "buffer_load_dwordx4 a[236:239], v7, s[44:47], s84 offen offset:1024 sc1\n"
    "v_mfma_f32_16x16x32_bf16 v[72:75], a[16:19], a[64:67], v[72:75]\n"
    "ds_read_b64_tr_b16 a[32:33], v8 offset:32768\n"
    "v_mfma_f32_16x16x32_bf16 v[72:75], a[20:23], a[80:83], v[72:75]\n"
    "v_readlane_b32 s84, v15, s89\n"
    "v_mfma_f32_16x16x32_bf16 v[120:123], a[20:23], a[84:87], v[120:123]\n"
    "ds_read_b64_tr_b16 a[34:35], v8 offset:49152\n"
    "s_add_u32 s89, s89, 1\n"
    "v_mfma_f32_16x16x32_bf16 v[120:123], a[16:19], a[68:71], v[120:123]\n"
    "buffer_load_dwordx4 a[240:243], v14, s[44:47], s85 offen sc1\n"
    "v_mfma_f32_16x16x32_bf16 v[168:171], a[16:19], a[72:75], v[168:171]\n"
    "ds_read_b64_tr_b16 a[36:37], v8 offset:36864\n"
    "v_mfma_f32_16x16x32_bf16 v[168:171], a[20:23], a[88:91], v[168:171]\n"
    "v_mfma_f32_16x16x32_bf16 v[216:219], a[20:23], a[92:95], v[216:219]\n"
    "ds_read_b64_tr_b16 a[38:39], v8 offset:53248\n"
    "s_waitcnt lgkmcnt(4)\n"
    "v_mfma_f32_16x16x32_bf16 v[216:219], a[16:19], a[76:79], v[216:219]\n"
    "buffer_load_dwordx4 a[244:247], v14, s[44:47], s85 offen offset:1024 sc1\n"
    "v_mfma_f32_16x16x32_bf16 v[76:79], a[24:27], a[64:67], v[76:79]\n"
    "ds_read_b64_tr_b16 a[40:41], v9 offset:32768\n"
    "v_mfma_f32_16x16x32_bf16 v[76:79], a[28:31], a[80:83], v[76:79]\n"
    "v_readlane_b32 s87, v28, s90\n"
    "v_mfma_f32_16x16x32_bf16 v[124:127], a[28:31], a[84:87], v[124:127]\n"
    "ds_read_b64_tr_b16 a[42:43], v9 offset:49152\n"
    "s_add_u32 s90, s90, 1\n"
    "v_mfma_f32_16x16x32_bf16 v[124:127], a[24:27], a[68:71], v[124:127]\n"
    "buffer_load_dwordx4 a[248:251], v7, s[44:47], s85 offen sc1\n"
    "v_mfma_f32_16x16x32_bf16 v[172:175], a[24:27], a[72:75], v[172:175]\n"
    "ds_read_b64_tr_b16 a[44:45], v9 offset:36864\n"
    "v_mfma_f32_16x16x32_bf16 v[172:175], a[28:31], a[88:91], v[172:175]\n"
    "v_xor_b32 v8, 0x10000, v8\n"
    "v_mfma_f32_16x16x32_bf16 v[220:223], a[28:31], a[92:95], v[220:223]\n"
    "ds_read_b64_tr_b16 a[46:47], v9 offset:53248\n"
    "s_waitcnt lgkmcnt(4)\n"
    "v_mfma_f32_16x16x32_bf16 v[220:223], a[24:27], a[76:79], v[220:223]\n"
    "buffer_load_dwordx4 a[252:255], v7, s[44:47], s85 offen offset:1024 sc1\n"
    "v_mfma_f32_16x16x32_bf16 v[40:43], a[32:35], a[64:67], v[40:43]\n"
    "ds_read_b64_tr_b16 a[48:49], v10 offset:32768\n"
    "v_mfma_f32_16x16x32_bf16 v[40:43], a[36:39], a[80:83], v[40:43]\n"
    "s_add_u32 m0, s95, 32768\n"
    "v_mfma_f32_16x16x32_bf16 v[88:91], a[36:39], a[84:87], v[88:91]\n"
    "ds_read_b64_tr_b16 a[50:51], v10 offset:49152\n"
    "v_mfma_f32_16x16x32_bf16 v[88:91], a[32:35], a[68:71], v[88:91]\n"
    "buffer_load_dwordx4 v12, s[48:51], s86 offen lds\n"
    "v_mfma_f32_16x16x32_bf16 v[136:139], a[32:35], a[72:75], v[136:139]\n"
    "ds_read_b64_tr_b16 a[52:53], v10 offset:36864\n"
    "v_mfma_f32_16x16x32_bf16 v[136:139], a[36:39], a[88:91], v[136:139]\n"
    "v_xor_b32 v9, 0x10000, v9\n"
    "v_mfma_f32_16x16x32_bf16 v[184:187], a[36:39], a[92:95], v[184:187]\n"
    "ds_read_b64_tr_b16 a[54:55], v10 offset:53248\n"
    "s_waitcnt vmcnt(9) lgkmcnt(4)\n"
    "v_mfma_f32_16x16x32_bf16 v[184:187], a[32:35], a[76:79], v[184:187]\n"
    "buffer_load_dwordx4 v13, s[48:51], s86 offen offset:1024 lds\n"
    "v_mfma_f32_16x16x32_bf16 v[44:47], a[40:43], a[64:67], v[44:47]\n"
    "ds_read_b64_tr_b16 a[56:57], v11 offset:32768\n"
    "v_mfma_f32_16x16x32_bf16 v[44:47], a[44:47], a[80:83], v[44:47]\n"
    "v_mfma_f32_16x16x32_bf16 v[92:95], a[44:47], a[84:87], v[92:95]\n"
    "ds_read_b64_tr_b16 a[58:59], v11 offset:49152\n"
    "v_xor_b32 v10, 0x10000, v10\n"
    "v_mfma_f32_16x16x32_bf16 v[92:95], a[40:43], a[68:71], v[92:95]\n"
    "buffer_load_dwordx4 v5, s[48:51], s86 offen offset:2048 lds\n"
    "v_mfma_f32_16x16x32_bf16 v[140:143], a[40:43], a[72:75], v[140:143]\n"
    "ds_read_b64_tr_b16 a[60:61], v11 offset:36864\n"
    "v_mfma_f32_16x16x32_bf16 v[140:143], a[44:47], a[88:91], v[140:143]\n"
    "s_barrier\n"
    "v_mfma_f32_16x16x32_bf16 v[188:191], a[44:47], a[92:95], v[188:191]\n"
    "ds_read_b64_tr_b16 a[62:63], v11 offset:53248\n"
    "s_waitcnt lgkmcnt(4)\n"
    "v_mfma_f32_16x16x32_bf16 v[188:191], a[40:43], a[76:79], v[188:191]\n"
    "buffer_load_dwordx4 v6, s[48:51], s86 offen offset:3072 lds\n"
    "v_mfma_f32_16x16x32_bf16 v[48:51], a[48:51], a[64:67], v[48:51]\n"
    "ds_read_b64_tr_b16 a[0:1], v8 offset:1024\n"
    "v_mfma_f32_16x16x32_bf16 v[48:51], a[52:55], a[80:83], v[48:51]\n"
    "s_add_u32 m0, s95, 36864\n"
    "v_mfma_f32_16x16x32_bf16 v[96:99], a[52:55], a[84:87], v[96:99]\n"
    "ds_read_b64_tr_b16 a[2:3], v8 offset:17408\n"
    "v_mfma_f32_16x16x32_bf16 v[96:99], a[48:51], a[68:71], v[96:99]\n"
    "buffer_load_dwordx4 v12, s[48:51], s87 offen lds\n"
    "v_mfma_f32_16x16x32_bf16 v[144:147], a[48:51], a[72:75], v[144:147]\n"
    "ds_read_b64_tr_b16 a[4:5], v8 offset:5120\n"
    "v_mfma_f32_16x16x32_bf16 v[144:147], a[52:55], a[88:91], v[144:147]\n"
    "v_xor_b32 v11, 0x10000, v11\n"
    "v_mfma_f32_16x16x32_bf16 v[192:195], a[52:55], a[92:95], v[192:195]\n"
    "ds_read_b64_tr_b16 a[6:7], v8 offset:21504\n"
    "s_waitcnt lgkmcnt(4)\n"
    "v_mfma_f32_16x16x32_bf16 v[192:195], a[48:51], a[76:79], v[192:195]\n"
    "buffer_load_dwordx4 v13, s[48:51], s87 offen offset:1024 lds\n"
    "v_mfma_f32_16x16x32_bf16 v[52:55], a[56:59], a[64:67], v[52:55]\n"
    "ds_read_b64_tr_b16 a[8:9], v9 offset:1024\n"
    "v_mfma_f32_16x16x32_bf16 v[52:55], a[60:63], a[80:83], v[52:55]\n"
    "s_xor_b32 s95, s95, 0x10000\n"
    "v_mfma_f32_16x16x32_bf16 v[100:103], a[60:63], a[84:87], v[100:103]\n"
    "ds_read_b64_tr_b16 a[10:11], v9 offset:17408\n"
    "v_mfma_f32_16x16x32_bf16 v[100:103], a[56:59], a[68:71], v[100:103]\n"
    "buffer_load_dwordx4 v5, s[48:51], s87 offen offset:2048 lds\n"
    "v_mfma_f32_16x16x32_bf16 v[148:151], a[56:59], a[72:75], v[148:151]\n"
    "ds_read_b64_tr_b16 a[12:13], v9 offset:5120\n"
    "v_mfma_f32_16x16x32_bf16 v[148:151], a[60:63], a[88:91], v[148:151]\n"
    "v_mfma_f32_16x16x32_bf16 v[196:199], a[60:63], a[92:95], v[196:199]\n"
    "ds_read_b64_tr_b16 a[14:15], v9 offset:21504\n"
    "s_waitcnt vmcnt(7) lgkmcnt(4)\n"
    "v_mfma_f32_16x16x32_bf16 v[196:199], a[56:59], a[76:79], v[196:199]\n"
    "buffer_load_dwordx4 v6, s[48:51], s87 offen offset:3072 lds\n"
    "v_mfma_f32_16x16x32_bf16 v[64:67], a[0:3], a[224:227], v[64:67]\n"
    "ds_read_b64_tr_b16 a[16:17], v10 offset:1024\n"
    "v_mfma_f32_16x16x32_bf16 v[64:67], a[4:7], a[240:243], v[64:67]\n"
    "v_readlane_b32 s85, v15, s89\n"
    "v_mfma_f32_16x16x32_bf16 v[112:115], a[4:7], a[244:247], v[112:115]\n"
    "ds_read_b64_tr_b16 a[18:19], v10 offset:17408\n"
    "s_add_u32 s89, s89, 1\n"
    "v_mfma_f32_16x16x32_bf16 v[112:115], a[0:3], a[228:231], v[112:115]\n"
    "buffer_load_dwordx4 a[64:67], v14, s[44:47], s84 offen sc1\n"
    "v_mfma_f32_16x16x32_bf16 v[160:163], a[0:3], a[232:235], v[160:163]\n"
    "ds_read_b64_tr_b16 a[20:21], v10 offset:5120\n"
    "v_mfma_f32_16x16x32_bf16 v[160:163], a[4:7], a[248:251], v[160:163]\n"
    "s_bitcmp1_b32 s31, s88\n"
    "s_cbranch_scc1 .Lk2_lha\n"
    ".Lk2_lra:\n"
    "v_mfma_f32_16x16x32_bf16 v[208:211], a[4:7], a[252:255], v[208:211]\n"
    "ds_read_b64_tr_b16 a[22:23], v10 offset:21504\n"
    "s_waitcnt lgkmcnt(4)\n"
    "v_mfma_f32_16x16x32_bf16 v[208:211], a[0:3], a[236:239], v[208:211]\n"
    "buffer_load_dwordx4 a[68:71], v14, s[44:47], s84 offen offset:1024 sc1\n"
    "v_mfma_f32_16x16x32_bf16 v[68:71], a[8:11], a[224:227], v[68:71]\n"
    "ds_read_b64_tr_b16 a[24:25], v11 offset:1024\n"
    "v_mfma_f32_16x16x32_bf16 v[68:71], a[12:15], a[240:243], v[68:71]\n"
    "v_readlane_b32 s86, v28, s90\n"
    "v_mfma_f32_16x16x32_bf16 v[116:119], a[12:15], a[244:247], v[116:119]\n"
    "ds_read_b64_tr_b16 a[26:27], v11 offset:17408\n"
    "s_add_u32 s90, s90, 1\n"
    "v_mfma_f32_16x16x32_bf16 v[116:119], a[8:11], a[228:231], v[116:119]\n"
    "buffer_load_dwordx4 a[72:75], v7, s[44:47], s84 offen sc1\n"
    "v_mfma_f32_16x16x32_bf16 v[164:167], a[8:11], a[232:235], v[164:167]\n"
    "ds_read_b64_tr_b16 a[28:29], v11 offset:5120\n"
    "v_mfma_f32_16x16x32_bf16 v[164:167], a[12:15], a[248:251], v[164:167]\n"
    "v_mfma_f32_16x16x32_bf16 v[212:215], a[12:15], a[252:255], v[212:215]\n"
    "ds_read_b64_tr_b16 a[30:31], v11 offset:21504\n"
    "s_waitcnt lgkmcnt(4)\n"
    "v_mfma_f32_16x16x32_bf16 v[212:215], a[8:11], a[236:239], v[212:215]\n"
    "buffer_load_dwordx4 a[76:79], v7, s[44:47], s84 offen offset:1024 sc1\n"
    "v_mfma_f32_16x16x32_bf16 v[72:75], a[16:19], a[224:227], v[72:75]\n"
    "ds_read_b64_tr_b16 a[32:33], v8 offset:0\n"
    "v_mfma_f32_16x16x32_bf16 v[72:75], a[20:23], a[240:243], v[72:75]\n"
    "v_readlane_b32 s84, v15, s89\n"
    "v_mfma_f32_16x16x32_bf16 v[120:123], a[20:23], a[244:247], v[120:123]\n"
    "ds_read_b64_tr_b16 a[34:35], v8 offset:16384\n"
    "s_add_u32 s89, s89, 1\n"
    "v_mfma_f32_16x16x32_bf16 v[120:123], a[16:19], a[228:231], v[120:123]\n"
    "buffer_load_dwordx4 a[80:83], v14, s[44:47], s85 offen sc1\n"
    "v_mfma_f32_16x16x32_bf16 v[168:171], a[16:19], a[232:235], v[168:171]\n"
    "ds_read_b64_tr_b16 a[36:37], v8 offset:4096\n"
    "v_mfma_f32_16x16x32_bf16 v[168:171], a[20:23], a[248:251], v[168:171]\n"
    "v_mfma_f32_16x16x32_bf16 v[216:219], a[20:23], a[252:255], v[216:219]\n"
    "ds_read_b64_tr_b16 a[38:39], v8 offset:20480\n"
    "s_waitcnt lgkmcnt(4)\n"
    "v_mfma_f32_16x16x32_bf16 v[216:219], a[16:19], a[236:239], v[216:219]\n"
    "buffer_load_dwordx4 a[84:87], v14, s[44:47], s85 offen offset:1024 sc1\n"
    "v_mfma_f32_16x16x32_bf16 v[76:79], a[24:27], a[224:227], v[76:79]\n"
    "ds_read_b64_tr_b16 a[40:41], v9 offset:0\n"
    "v_mfma_f32_16x16x32_bf16 v[76:79], a[28:31], a[240:243], v[76:79]\n"
    "v_readlane_b32 s87, v28, s90\n"
    "v_mfma_f32_16x16x32_bf16 v[124:127], a[28:31], a[244:247], v[124:127]\n"
    "ds_read_b64_tr_b16 a[42:43], v9 offset:16384\n"
    "s_add_u32 s90, s90, 1\n"
    "v_mfma_f32_16x16x32_bf16 v[124:127], a[24:27], a[228:231], v[124:127]\n"
    "buffer_load_dwordx4 a[88:91], v7, s[44:47], s85 offen sc1\n"
    "v_mfma_f32_16x16x32_bf16 v[172:175], a[24:27], a[232:235], v[172:175]\n"
    "ds_read_b64_tr_b16 a[44:45], v9 offset:4096\n"
    "v_mfma_f32_16x16x32_bf16 v[172:175], a[28:31], a[248:251], v[172:175]\n"
    "v_mfma_f32_16x16x32_bf16 v[220:223], a[28:31], a[252:255], v[220:223]\n"
    "ds_read_b64_tr_b16 a[46:47], v9 offset:20480\n"
    "s_waitcnt lgkmcnt(4)\n"
    "v_mfma_f32_16x16x32_bf16 v[220:223], a[24:27], a[236:239], v[220:223]\n"
    "buffer_load_dwordx4 a[92:95], v7, s[44:47], s85 offen offset:1024 sc1\n"
    "v_mfma_f32_16x16x32_bf16 v[40:43], a[32:35], a[224:227], v[40:43]\n"
    "ds_read_b64_tr_b16 a[48:49], v10 offset:0\n"
    "v_mfma_f32_16x16x32_bf16 v[40:43], a[36:39], a[240:243], v[40:43]\n"
    "s_add_u32 m0, s95, 0\n"
    "v_mfma_f32_16x16x32_bf16 v[88:91], a[36:39], a[244:247], v[88:91]\n"
    "ds_read_b64_tr_b16 a[50:51], v10 offset:16384\n"
    "v_mfma_f32_16x16x32_bf16 v[88:91], a[32:35], a[228:231], v[88:91]\n"
    "buffer_load_dwordx4 v12, s[48:51], s86 offen lds\n"
    "v_mfma_f32_16x16x32_bf16 v[136:139], a[32:35], a[232:235], v[136:139]\n"
    "ds_read_b64_tr_b16 a[52:53], v10 offset:4096\n"
    "v_mfma_f32_16x16x32_bf16 v[136:139], a[36:39], a[248:251], v[136:139]\n"
    "v_mfma_f32_16x16x32_bf16 v[184:187], a[36:39], a[252:255], v[184:187]\n"
    "ds_read_b64_tr_b16 a[54:55], v10 offset:20480\n"
    "s_waitcnt vmcnt(8) lgkmcnt(4)\n"
    "v_mfma_f32_16x16x32_bf16 v[184:187], a[32:35], a[236:239], v[184:187]\n"
    "buffer_load_dwordx4 v13, s[48:51], s86 offen offset:1024 lds\n"
    "v_mfma_f32_16x16x32_bf16 v[44:47], a[40:43], a[224:227], v[44:47]\n"
    "ds_read_b64_tr_b16 a[56:57], v11 offset:0\n"
    "v_mfma_f32_16x16x32_bf16 v[44:47], a[44:47], a[240:243], v[44:47]\n"
    "s_bitcmp1_b32 s27, s88\n"
    "v_mfma_f32_16x16x32_bf16 v[92:95], a[44:47], a[244:247], v[92:95]\n"
    "ds_read_b64_tr_b16 a[58:59], v11 offset:16384\n"
    "s_cbranch_scc1 .Lk2_lhb\n"
    ".Lk2_lok:\n"
    "v_mfma_f32_16x16x32_bf16 v[92:95], a[40:43], a[228:231], v[92:95]\n"
    "buffer_load_dwordx4 v5, s[48:51], s86 offen offset:2048 lds\n"
    "v_mfma_f32_16x16x32_bf16 v[140:143], a[40:43], a[232:235], v[140:143]\n"
    "ds_read_b64_tr_b16 a[60:61], v11 offset:4096\n"
    "v_mfma_f32_16x16x32_bf16 v[140:143], a[44:47], a[248:251], v[140:143]\n"
    "s_barrier\n"
    "v_mfma_f32_16x16x32_bf16 v[188:191], a[44:47], a[252:255], v[188:191]\n"
    "ds_read_b64_tr_b16 a[62:63], v11 offset:20480\n"
    "s_waitcnt lgkmcnt(4)\n"
    "v_mfma_f32_16x16x32_bf16 v[188:191], a[40:43], a[236:239], v[188:191]\n"
    "buffer_load_dwordx4 v6, s[48:51], s86 offen offset:3072 lds\n"
    "v_mfma_f32_16x16x32_bf16 v[48:51], a[48:51], a[224:227], v[48:51]\n"
    "ds_read_b64_tr_b16 a[0:1], v8 offset:33792\n"
    "v_mfma_f32_16x16x32_bf16 v[48:51], a[52:55], a[240:243], v[48:51]\n"
    "s_add_u32 m0, s95, 4096\n"
    "v_mfma_f32_16x16x32_bf16 v[96:99], a[52:55], a[244:247], v[96:99]\n"
    "ds_read_b64_tr_b16 a[2:3], v8 offset:50176\n"
    "s_add_u32 s88, s88, 1\n"
    "v_mfma_f32_16x16x32_bf16 v[96:99], a[48:51], a[228:231], v[96:99]\n"
    "buffer_load_dwordx4 v12, s[48:51], s87 offen lds\n"
    "v_mfma_f32_16x16x32_bf16 v[144:147], a[48:51], a[232:235], v[144:147]\n"
    "ds_read_b64_tr_b16 a[4:5], v8 offset:37888\n"
    "v_mfma_f32_16x16x32_bf16 v[144:147], a[52:55], a[248:251], v[144:147]\n"
    "v_mfma_f32_16x16x32_bf16 v[192:195], a[52:55], a[252:255], v[192:195]\n"
    "ds_read_b64_tr_b16 a[6:7], v8 offset:54272\n"
    "s_waitcnt lgkmcnt(4)\n"
    "v_mfma_f32_16x16x32_bf16 v[192:195], a[48:51], a[236:239], v[192:195]\n"
    "buffer_load_dwordx4 v13, s[48:51], s87 offen offset:1024 lds\n"
    "v_mfma_f32_16x16x32_bf16 v[52:55], a[56:59], a[224:227], v[52:55]\n"
    "ds_read_b64_tr_b16 a[8:9], v9 offset:33792\n"
    "v_mfma_f32_16x16x32_bf16 v[52:55], a[60:63], a[240:243], v[52:55]\n"
    "v_mfma_f32_16x16x32_bf16 v[100:103], a[60:63], a[244:247], v[100:103]\n"
    "ds_read_b64_tr_b16 a[10:11], v9 offset:50176\n"
    "v_mfma_f32_16x16x32_bf16 v[100:103], a[56:59], a[228:231], v[100:103]\n"
    "buffer_load_dwordx4 v5, s[48:51], s87 offen offset:2048 lds\n"
    "v_mfma_f32_16x16x32_bf16 v[148:151], a[56:59], a[232:235], v[148:151]\n"
    "ds_read_b64_tr_b16 a[12:13], v9 offset:37888\n"
    "v_mfma_f32_16x16x32_bf16 v[148:151], a[60:63], a[248:251], v[148:151]\n"
    "s_cmp_lg_u32 s88, s93\n"
    "v_mfma_f32_16x16x32_bf16 v[196:199], a[60:63], a[252:255], v[196:199]\n"
    "ds_read_b64_tr_b16 a[14:15], v9 offset:54272\n"
    "s_waitcnt vmcnt(7) lgkmcnt(4)\n"
    "v_mfma_f32_16x16x32_bf16 v[196:199], a[56:59], a[236:239], v[196:199]\n"
    "buffer_load_dwordx4 v6, s[48:51], s87 offen offset:3072 lds\n"
    "s_cbranch_scc1 .Lk2_p2loop\n"
    ".Lk2_zseg:\n"
    "s_bitcmp1_b32 s35, 2\n"
    "s_cbranch_scc1 .Lk2_zs\n"
    "v_mfma_f32_16x16x32_bf16 v[64:67], a[0:3], a[64:67], v[64:67]\n"
    "ds_read_b64_tr_b16 a[16:17], v10 offset:33792\n"
    "v_mfma_f32_16x16x32_bf16 v[64:67], a[4:7], a[80:83], v[64:67]\n"
    "buffer_load_dwordx4 a[224:227], v14, s[44:47], s84 offen sc1\n"
    "v_mfma_f32_16x16x32_bf16 v[112:115], a[4:7], a[84:87], v[112:115]\n"
    "v_xor_b32 v24, 0x10000, v8\n"
    "v_xor_b32 v25, 0x10000, v9\n"
    "ds_read_b64_tr_b16 a[18:19], v10 offset:50176\n"
    "v_mfma_f32_16x16x32_bf16 v[112:115], a[0:3], a[68:71], v[112:115]\n"
    "v_readlane_b32 s85, v15, s89\n"
    "s_add_u32 s89, s89, 1\n"
    "ds_read_b64_tr_b16 a[20:21], v10 offset:37888\n"
    "v_mfma_f32_16x16x32_bf16 v[160:163], a[0:3], a[72:75], v[160:163]\n"
    "buffer_load_dwordx4 a[228:231], v14, s[44:47], s84 offen offset:1024 sc1\n"
    "v_mfma_f32_16x16x32_bf16 v[160:163], a[4:7], a[88:91], v[160:163]\n"
    "v_xor_b32 v26, 0x10000, v10\n"
    "v_xor_b32 v27, 0x10000, v11\n"
    "ds_read_b64_tr_b16 a[22:23], v10 offset:54272\n"
    "v_mfma_f32_16x16x32_bf16 v[208:211], a[4:7], a[92:95], v[208:211]\n"
    "v_readlane_b32 s86, v28, s90\n"
    "s_add_u32 s90, s90, 1\n"
    "ds_read_b64_tr_b16 a[24:25], v11 offset:33792\n"
    "v_mfma_f32_16x16x32_bf16 v[208:211], a[0:3], a[76:79], v[208:211]\n"
    "buffer_load_dwordx4 a[232:235], v7, s[44:47], s84 offen sc1\n"
    "s_waitcnt lgkmcnt(4)\n"
    "v_mfma_f32_16x16x32_bf16 v[68:71], a[8:11], a[64:67], v[68:71]\n"
    "v_add_u32 v29, s52, v16\n"
    "v_add_u32 v29, 0x20000, v29\n"
    "ds_read_b64_tr_b16 a[26:27], v11 offset:50176\n"
    "v_mfma_f32_16x16x32_bf16 v[68:71], a[12:15], a[80:83], v[68:71]\n"
    "v_readlane_b32 s87, v28, s90\n"
    "s_add_u32 s90, s90, 1\n"
    "ds_read_b64_tr_b16 a[28:29], v11 offset:37888\n"
    "v_mfma_f32_16x16x32_bf16 v[116:119], a[12:15], a[84:87], v[116:119]\n"
    "buffer_load_dwordx4 a[236:239], v7, s[44:47], s84 offen offset:1024 sc1\n"
    "v_mfma_f32_16x16x32_bf16 v[116:119], a[8:11], a[68:71], v[116:119]\n"
    "v_xor_b32 v31, 0x2000, v29\n"
    "ds_read_b64_tr_b16 a[30:31], v11 offset:54272\n"
    "v_mfma_f32_16x16x32_bf16 v[164:167], a[8:11], a[72:75], v[164:167]\n"
    "v_readlane_b32 s34, v28, s90\n"
    "s_add_u32 s90, s90, 1\n"
    "v_mfma_f32_16x16x32_bf16 v[164:167], a[12:15], a[88:91], v[164:167]\n"
    "buffer_load_dwordx4 a[240:243], v14, s[44:47], s85 offen sc1\n"
    "v_mfma_f32_16x16x32_bf16 v[212:215], a[12:15], a[92:95], v[212:215]\n"
    "ds_read_b64_tr_b16 a[32:33], v8 offset:32768\n"
    "v_mfma_f32_16x16x32_bf16 v[212:215], a[8:11], a[76:79], v[212:215]\n"
    "v_readlane_b32 s84, v15, s89\n"
    "s_add_u32 s89, s89, 1\n"
    "ds_read_b64_tr_b16 a[34:35], v8 offset:49152\n"
    "s_waitcnt lgkmcnt(4)\n"
    "v_mfma_f32_16x16x32_bf16 v[72:75], a[16:19], a[64:67], v[72:75]\n"
    "buffer_load_dwordx4 a[244:247], v14, s[44:47], s85 offen offset:1024 sc1\n"
    "v_mfma_f32_16x16x32_bf16 v[72:75], a[20:23], a[80:83], v[72:75]\n"
    "ds_read_b64_tr_b16 a[36:37], v8 offset:36864\n"
    "v_mfma_f32_16x16x32_bf16 v[120:123], a[20:23], a[84:87], v[120:123]\n"
    "s_xor_b32 s94, s95, 0x10000\n"
    "ds_read_b64_tr_b16 a[38:39], v8 offset:53248\n"
    "v_mfma_f32_16x16x32_bf16 v[120:123], a[16:19], a[68:71], v[120:123]\n"
    "buffer_load_dwordx4 a[248:251], v7, s[44:47], s85 offen sc1\n"
    "v_mfma_f32_16x16x32_bf16 v[168:171], a[16:19], a[72:75], v[168:171]\n"
    "v_mfma_f32_16x16x32_bf16 v[168:171], a[20:23], a[88:91], v[168:171]\n"
    "v_mfma_f32_16x16x32_bf16 v[216:219], a[20:23], a[92:95], v[216:219]\n"
    "buffer_load_dwordx4 a[252:255], v7, s[44:47], s85 offen offset:1024 sc1\n"
    "v_mfma_f32_16x16x32_bf16 v[216:219], a[16:19], a[76:79], v[216:219]\n"
    "v_mfma_f32_16x16x32_bf16 v[76:79], a[24:27], a[64:67], v[76:79]\n"
    "s_add_u32 m0, s95, 32768\n"
    "ds_read_b64_tr_b16 a[40:41], v9 offset:32768\n"
    "s_waitcnt lgkmcnt(4)\n"
    "v_mfma_f32_16x16x32_bf16 v[76:79], a[28:31], a[80:83], v[76:79]\n"
    "buffer_load_dwordx4 v13, s[48:51], s86 offen offset:1024 lds\n"
    "v_mfma_f32_16x16x32_bf16 v[124:127], a[28:31], a[84:87], v[124:127]\n"
    "ds_read_b64_tr_b16 a[42:43], v9 offset:49152\n"
    "v_mfma_f32_16x16x32_bf16 v[124:127], a[24:27], a[68:71], v[124:127]\n"
    "ds_read_b64_tr_b16 a[44:45], v9 offset:36864\n"
    "v_mfma_f32_16x16x32_bf16 v[172:175], a[24:27], a[72:75], v[172:175]\n"
    "buffer_load_dwordx4 v6, s[48:51], s86 offen offset:3072 lds\n"
    "v_mfma_f32_16x16x32_bf16 v[172:175], a[28:31], a[88:91], v[172:175]\n"
    "ds_read_b64_tr_b16 a[46:47], v9 offset:53248\n"
    "v_mfma_f32_16x16x32_bf16 v[220:223], a[28:31], a[92:95], v[220:223]\n"
    "s_add_u32 m0, s95, 36864\n"
    "v_mfma_f32_16x16x32_bf16 v[220:223], a[24:27], a[76:79], v[220:223]\n"
    "buffer_load_dwordx4 v13, s[48:51], s87 offen offset:1024 lds\n"
    "s_waitcnt lgkmcnt(4)\n"
    "v_mfma_f32_16x16x32_bf16 v[40:43], a[32:35], a[64:67], v[40:43]\n"
    "v_mfma_f32_16x16x32_bf16 v[40:43], a[36:39], a[80:83], v[40:43]\n"
    "s_waitcnt vmcnt(11)\n"
    "s_barrier\n"
    "v_mfma_f32_16x16x32_bf16 v[40:43], a[208:211], a[96:99], v[40:43]\n"
    "buffer_load_dwordx4 v6, s[48:51], s87 offen offset:3072 lds\n"
    "v_mfma_f32_16x16x32_bf16 v[88:91], a[36:39], a[84:87], v[88:91]\n"
    "ds_read_b64_tr_b16 a[48:49], v24 offset:1024\n"
    "v_mfma_f32_16x16x32_bf16 v[88:91], a[32:35], a[68:71], v[88:91]\n"
    "ds_read_b64_tr_b16 a[50:51], v24 offset:17408\n"
    "v_mfma_f32_16x16x32_bf16 v[88:91], a[208:211], a[112:115], v[88:91]\n"
    "buffer_load_dwordx4 v[224:227], v14, s[44:47], s84 offen sc1\n"
    "v_mfma_f32_16x16x32_bf16 v[136:139], a[32:35], a[72:75], v[136:139]\n"
    "ds_read_b64_tr_b16 a[52:53], v24 offset:5120\n"
    "v_mfma_f32_16x16x32_bf16 v[136:139], a[36:39], a[88:91], v[136:139]\n"
    "v_readlane_b32 s85, v15, s89\n"
    "s_add_u32 s89, s89, 1\n"
    "ds_read_b64_tr_b16 a[54:55], v24 offset:21504\n"
    "v_mfma_f32_16x16x32_bf16 v[184:187], a[36:39], a[92:95], v[184:187]\n"
    "buffer_load_dwordx4 v[228:231], v14, s[44:47], s84 offen offset:1024 sc1\n"
    "v_mfma_f32_16x16x32_bf16 v[184:187], a[32:35], a[76:79], v[184:187]\n"
    "s_waitcnt lgkmcnt(4)\n"
    "v_mfma_f32_16x16x32_bf16 v[44:47], a[40:43], a[64:67], v[44:47]\n"
    "ds_read_b64_tr_b16 a[56:57], v25 offset:1024\n"
    "v_mfma_f32_16x16x32_bf16 v[44:47], a[44:47], a[80:83], v[44:47]\n"
    "buffer_load_dwordx4 v[232:235], v7, s[44:47], s84 offen sc1\n"
    "v_mfma_f32_16x16x32_bf16 v[44:47], a[208:211], a[100:103], v[44:47]\n"
    "ds_read_b64_tr_b16 a[58:59], v25 offset:17408\n"
    "v_mfma_f32_16x16x32_bf16 v[92:95], a[44:47], a[84:87], v[92:95]\n"
    "ds_read_b64_tr_b16 a[60:61], v25 offset:5120\n"
    "v_mfma_f32_16x16x32_bf16 v[92:95], a[40:43], a[68:71], v[92:95]\n"
    "buffer_load_dwordx4 v[236:239], v7, s[44:47], s84 offen offset:1024 sc1\n"
    "v_mfma_f32_16x16x32_bf16 v[92:95], a[208:211], a[116:119], v[92:95]\n"
    "ds_read_b64_tr_b16 a[62:63], v25 offset:21504\n"
    "v_mfma_f32_16x16x32_bf16 v[140:143], a[40:43], a[72:75], v[140:143]\n"
    "v_mfma_f32_16x16x32_bf16 v[140:143], a[44:47], a[88:91], v[140:143]\n"
    "buffer_load_dwordx4 v[240:243], v14, s[44:47], s85 offen sc1\n"
    "v_mfma_f32_16x16x32_bf16 v[188:191], a[44:47], a[92:95], v[188:191]\n"
    "ds_read_b64_tr_b16 a[0:1], v26 offset:1024\n"
    "v_mfma_f32_16x16x32_bf16 v[188:191], a[40:43], a[76:79], v[188:191]\n"
    "v_readlane_b32 s84, v15, s89\n"
    "s_add_u32 s89, s89, 1\n"
    "ds_read_b64_tr_b16 a[2:3], v26 offset:17408\n"
    "s_waitcnt vmcnt(9)\n"
    "s_waitcnt lgkmcnt(4)\n"
    "v_mfma_f32_16x16x32_bf16 v[64:67], a[48:51], a[224:227], v[64:67]\n"
    "buffer_load_dwordx4 v[244:247], v14, s[44:47], s85 offen offset:1024 sc1\n"
    "v_mfma_f32_16x16x32_bf16 v[64:67], a[52:55], a[240:243], v[64:67]\n"
    "ds_read_b64_tr_b16 a[4:5], v26 offset:5120\n"
    "v_mfma_f32_16x16x32_bf16 v[112:115], a[52:55], a[244:247], v[112:115]\n"
    "ds_read_b64_tr_b16 a[6:7], v26 offset:21504\n"
    "v_mfma_f32_16x16x32_bf16 v[112:115], a[48:51], a[228:231], v[112:115]\n"
    "buffer_load_dwordx4 v[248:251], v7, s[44:47], s85 offen sc1\n"
    "v_mfma_f32_16x16x32_bf16 v[160:163], a[48:51], a[232:235], v[160:163]\n"
    "v_mfma_f32_16x16x32_bf16 v[160:163], a[52:55], a[248:251], v[160:163]\n"
    "v_mfma_f32_16x16x32_bf16 v[208:211], a[52:55], a[252:255], v[208:211]\n"
    "buffer_load_dwordx4 v[252:255], v7, s[44:47], s85 offen offset:1024 sc1\n"
    "v_mfma_f32_16x16x32_bf16 v[208:211], a[48:51], a[236:239], v[208:211]\n"
    "ds_read_b64_tr_b16 a[8:9], v27 offset:1024\n"
    "v_mfma_f32_16x16x32_bf16 v[68:71], a[56:59], a[224:227], v[68:71]\n"
    "s_add_u32 m0, s94, 0\n"
    "ds_read_b64_tr_b16 a[10:11], v27 offset:17408\n"
    "s_waitcnt lgkmcnt(4)\n"
    "v_mfma_f32_16x16x32_bf16 v[68:71], a[60:63], a[240:243], v[68:71]\n"
    "buffer_load_dwordx4 v13, s[48:51], s34 offen offset:1024 lds\n"
    "v_mfma_f32_16x16x32_bf16 v[116:119], a[60:63], a[244:247], v[116:119]\n"
    "ds_read_b64_tr_b16 a[12:13], v27 offset:5120\n"
    "v_mfma_f32_16x16x32_bf16 v[116:119], a[56:59], a[228:231], v[116:119]\n"
    "ds_read_b64_tr_b16 a[14:15], v27 offset:21504\n"
    "v_mfma_f32_16x16x32_bf16 v[164:167], a[56:59], a[232:235], v[164:167]\n"
    "buffer_load_dwordx4 v6, s[48:51], s34 offen offset:3072 lds\n"
    "v_mfma_f32_16x16x32_bf16 v[164:167], a[60:63], a[248:251], v[164:167]\n"
    "s_waitcnt vmcnt(10)\n"
    "s_barrier\n"
    "v_mfma_f32_16x16x32_bf16 v[212:215], a[60:63], a[252:255], v[212:215]\n"
    "ds_read_b64_tr_b16 a[16:17], v24 offset:33792\n"
    "v_mfma_f32_16x16x32_bf16 v[212:215], a[56:59], a[236:239], v[212:215]\n"
    "buffer_load_dwordx4 a[136:139], v14, s[44:47], s84 offen sc1\n"
    "v_mfma_f32_16x16x32_bf16 v[72:75], a[0:3], a[224:227], v[72:75]\n"
    "ds_read_b64_tr_b16 a[18:19], v24 offset:50176\n"
    "s_waitcnt lgkmcnt(4)\n"
    "v_mfma_f32_16x16x32_bf16 v[72:75], a[4:7], a[240:243], v[72:75]\n"
    "ds_read_b64_tr_b16 a[20:21], v24 offset:37888\n"
    "v_mfma_f32_16x16x32_bf16 v[120:123], a[4:7], a[244:247], v[120:123]\n"
    "buffer_load_dwordx4 a[140:143], v14, s[44:47], s84 offen offset:1024 sc1\n"
    "s_waitcnt vmcnt(2)\n"
    "s_barrier\n"
    "v_mfma_f32_16x16x32_bf16 v[120:123], a[0:3], a[228:231], v[120:123]\n"
    "ds_read_b64_tr_b16 a[22:23], v24 offset:54272\n"
    "v_mfma_f32_16x16x32_bf16 v[168:171], a[0:3], a[232:235], v[168:171]\n"
    "ds_read_b64_tr_b16 a[24:25], v8 offset:1024\n"
    "v_mfma_f32_16x16x32_bf16 v[168:171], a[4:7], a[248:251], v[168:171]\n"
    "buffer_load_dwordx4 a[144:147], v7, s[44:47], s84 offen sc1\n"
    "v_mfma_f32_16x16x32_bf16 v[216:219], a[4:7], a[252:255], v[216:219]\n"
    "ds_read_b64_tr_b16 a[26:27], v8 offset:17408\n"
    "v_mfma_f32_16x16x32_bf16 v[216:219], a[0:3], a[236:239], v[216:219]\n"
    "ds_read_b64_tr_b16 a[32:33], v25 offset:33792\n"
    "v_mfma_f32_16x16x32_bf16 v[76:79], a[8:11], a[224:227], v[76:79]\n"
    "buffer_load_dwordx4 a[148:151], v7, s[44:47], s84 offen offset:1024 sc1\n"
    "s_waitcnt lgkmcnt(4)\n"
    "v_mfma_f32_16x16x32_bf16 v[76:79], a[12:15], a[240:243], v[76:79]\n"
    "ds_read_b64_tr_b16 a[34:35], v25 offset:50176\n"
    "v_mfma_f32_16x16x32_bf16 v[124:127], a[12:15], a[244:247], v[124:127]\n"
    "ds_read_b64_tr_b16 a[36:37], v25 offset:37888\n"
    "v_mfma_f32_16x16x32_bf16 v[124:127], a[8:11], a[228:231], v[124:127]\n"
    "s_mov_b64 exec, 1\n"
    "buffer_load_dword v21, off, s[44:47], s83 sc1\n"
    "s_mov_b64 exec, -1\n"
    "v_mfma_f32_16x16x32_bf16 v[172:175], a[8:11], a[232:235], v[172:175]\n"
    "ds_read_b64_tr_b16 a[38:39], v25 offset:54272\n"
    "v_mfma_f32_16x16x32_bf16 v[172:175], a[12:15], a[248:251], v[172:175]\n"
    "s_add_u32 m0, s95, 32768\n"
    "ds_read_b64_tr_b16 a[40:41], v9 offset:1024\n"
    "v_mfma_f32_16x16x32_bf16 v[220:223], a[12:15], a[252:255], v[220:223]\n"
    "buffer_load_dwordx4 v12, s[48:51], s86 offen lds\n"
    "v_mfma_f32_16x16x32_bf16 v[220:223], a[8:11], a[236:239], v[220:223]\n"
    "ds_read_b64_tr_b16 a[42:43], v9 offset:17408\n"
    "s_waitcnt vmcnt(8)\n"
    "v_mfma_f32_16x16x32_bf16 v[160:163], a[16:19], v[232:235], v[160:163]\n"
    "ds_read_b64_tr_b16 a[48:49], v26 offset:33792\n"
    "s_waitcnt lgkmcnt(4)\n"
    "v_mfma_f32_16x16x32_bf16 v[160:163], a[20:23], v[248:251], v[160:163]\n"
    "buffer_load_dwordx4 v5, s[48:51], s86 offen offset:2048 lds\n"
    "s_waitcnt vmcnt(3)\n"
    "v_mfma_f32_16x16x32_bf16 v[160:163], a[24:27], a[144:147], v[160:163]\n"
    "ds_read_b64_tr_b16 a[50:51], v26 offset:50176\n"
    "v_mfma_f32_16x16x32_bf16 v[208:211], a[24:27], a[148:151], v[208:211]\n"
    "s_add_u32 m0, s95, 36864\n"
    "ds_read_b64_tr_b16 a[52:53], v26 offset:37888\n"
    "v_mfma_f32_16x16x32_bf16 v[208:211], a[20:23], v[252:255], v[208:211]\n"
    "buffer_load_dwordx4 v12, s[48:51], s87 offen lds\n"
    "ds_read_b64_tr_b16 a[54:55], v26 offset:54272\n"
    "v_mfma_f32_16x16x32_bf16 v[208:211], a[16:19], v[236:239], v[208:211]\n"
    "ds_read_b64_tr_b16 a[56:57], v10 offset:1024\n"
    "v_mfma_f32_16x16x32_bf16 v[164:167], a[32:35], v[232:235], v[164:167]\n"
    "ds_read_b64_tr_b16 a[58:59], v10 offset:17408\n"
    "s_waitcnt lgkmcnt(4)\n"
    "v_mfma_f32_16x16x32_bf16 v[164:167], a[36:39], v[248:251], v[164:167]\n"
    "buffer_load_dwordx4 v5, s[48:51], s87 offen offset:2048 lds\n"
    "v_mfma_f32_16x16x32_bf16 v[164:167], a[40:43], a[144:147], v[164:167]\n"
    "ds_read_b64_tr_b16 a[0:1], v27 offset:33792\n"
    "ds_write_b128 v29, v[160:163] offset:0\n"
    "v_mfma_f32_16x16x32_bf16 v[212:215], a[40:43], a[148:151], v[212:215]\n"
    "s_add_u32 m0, s94, 0\n"
    "ds_read_b64_tr_b16 a[2:3], v27 offset:50176\n"
    "v_mfma_f32_16x16x32_bf16 v[212:215], a[36:39], v[252:255], v[212:215]\n"
    "buffer_load_dwordx4 v12, s[48:51], s34 offen lds\n"
    "ds_read_b64_tr_b16 a[4:5], v27 offset:37888\n"
    "ds_read_b64_tr_b16 a[6:7], v27 offset:54272\n"
    "v_mfma_f32_16x16x32_bf16 v[212:215], a[32:35], v[236:239], v[212:215]\n"
    "ds_read_b64_tr_b16 a[8:9], v11 offset:1024\n"
    "ds_write_b128 v29, v[208:211] offset:4096\n"
    "v_mfma_f32_16x16x32_bf16 v[168:171], a[48:51], v[232:235], v[168:171]\n"
    "ds_read_b64_tr_b16 a[10:11], v11 offset:17408\n"
    "s_waitcnt lgkmcnt(4)\n"
    "v_mfma_f32_16x16x32_bf16 v[168:171], a[52:55], v[248:251], v[168:171]\n"
    "buffer_load_dwordx4 v5, s[48:51], s34 offen offset:2048 lds\n"
    "v_mfma_f32_16x16x32_bf16 v[168:171], a[56:59], a[144:147], v[168:171]\n"
    "ds_write_b128 v29, v[164:167] offset:1024\n"
    "v_mfma_f32_16x16x32_bf16 v[216:219], a[56:59], a[148:151], v[216:219]\n"
    "v_mfma_f32_16x16x32_bf16 v[216:219], a[52:55], v[252:255], v[216:219]\n"
    "v_mfma_f32_16x16x32_bf16 v[216:219], a[48:51], v[236:239], v[216:219]\n"
    "ds_write_b128 v29, v[212:215] offset:5120\n"
    "v_mfma_f32_16x16x32_bf16 v[172:175], a[0:3], v[232:235], v[172:175]\n"
    "s_waitcnt lgkmcnt(4)\n"
    "v_mfma_f32_16x16x32_bf16 v[172:175], a[4:7], v[248:251], v[172:175]\n"
    "s_waitcnt lgkmcnt(2)\n"
    "v_mfma_f32_16x16x32_bf16 v[172:175], a[8:11], a[144:147], v[172:175]\n"
    "ds_write_b128 v29, v[168:171] offset:2048\n"
    "v_mfma_f32_16x16x32_bf16 v[220:223], a[8:11], a[148:151], v[220:223]\n"
    "v_mfma_f32_16x16x32_bf16 v[220:223], a[4:7], v[252:255], v[220:223]\n"
    "v_mfma_f32_16x16x32_bf16 v[220:223], a[0:3], v[236:239], v[220:223]\n"
    "ds_write_b128 v29, v[216:219] offset:6144\n"
    "v_mfma_f32_16x16x32_bf16 v[64:67], a[16:19], v[224:227], v[64:67]\n"
    "v_mfma_f32_16x16x32_bf16 v[64:67], a[20:23], v[240:243], v[64:67]\n"
    "v_mfma_f32_16x16x32_bf16 v[64:67], a[24:27], a[136:139], v[64:67]\n"
    "ds_write_b128 v29, v[172:175] offset:3072\n"
    "v_mfma_f32_16x16x32_bf16 v[112:115], a[24:27], a[140:143], v[112:115]\n"
    "v_mfma_f32_16x16x32_bf16 v[112:115], a[20:23], v[244:247], v[112:115]\n"
    "v_mfma_f32_16x16x32_bf16 v[112:115], a[16:19], v[228:231], v[112:115]\n"
    "ds_write_b128 v29, v[220:223] offset:7168\n"
    "v_mfma_f32_16x16x32_bf16 v[68:71], a[32:35], v[224:227], v[68:71]\n"
    "ds_read_b64_tr_b16 a[16:17], v10 offset:32768\n"
    "v_mfma_f32_16x16x32_bf16 v[68:71], a[36:39], v[240:243], v[68:71]\n"
    "ds_read_b64_tr_b16 a[18:19], v10 offset:49152\n"
    "s_waitcnt lgkmcnt(2)\n"
    "s_barrier\n"
    "v_mfma_f32_16x16x32_bf16 v[68:71], a[40:43], a[136:139], v[68:71]\n"
    "ds_read_b64_tr_b16 a[20:21], v10 offset:36864\n"
    "ds_read_b128 v[36:39], v31 offset:0\n"
    "s_waitcnt vmcnt(6)\n"
    "v_cmp_ne_u32 vcc, v22, v21\n"
    "v_mfma_f32_16x16x32_bf16 v[116:119], a[40:43], a[140:143], v[116:119]\n"
    "ds_read_b64_tr_b16 a[22:23], v10 offset:53248\n"
    "ds_read_b128 v[128:131], v31 offset:4096\n"
    "v_mfma_f32_16x16x32_bf16 v[116:119], a[36:39], v[244:247], v[116:119]\n"
    "ds_read_b128 v[60:63], v31 offset:1024\n"
    "s_bitcmp0_b32 vcc_lo, 0\n"
    "s_cbranch_scc0 .Lk2_rslowa\n"
    ".Lk2_roka:\n"
    "v_mfma_f32_16x16x32_bf16 v[116:119], a[32:35], v[228:231], v[116:119]\n"
    "ds_read_b128 v[132:135], v31 offset:5120\n"
    "v_mfma_f32_16x16x32_bf16 v[72:75], a[48:51], v[224:227], v[72:75]\n"
    "ds_read_b128 v[84:87], v31 offset:2048\n"
    ".Lk2_m7874:\n"
    "s_cbranch_execz .Lk2_m9962\n"
    "v_mfma_f32_16x16x32_bf16 v[72:75], a[52:55], v[240:243], v[72:75]\n"
    "ds_read_b128 v[152:155], v31 offset:6144\n"
    "v_mfma_f32_16x16x32_bf16 v[72:75], a[56:59], a[136:139], v[72:75]\n"
    "ds_read_b128 v[108:111], v31 offset:3072\n"
    "s_waitcnt lgkmcnt(4)\n"
    "v_add_f32 v64, v64, v36\n"
    "v_add_f32 v65, v65, v37\n"
    "v_mfma_f32_16x16x32_bf16 v[120:123], a[56:59], a[140:143], v[120:123]\n"
    "ds_read_b128 v[156:159], v31 offset:7168\n"
    "v_add_f32 v66, v66, v38\n"
    "v_add_f32 v67, v67, v39\n"
    "v_mfma_f32_16x16x32_bf16 v[120:123], a[52:55], v[244:247], v[120:123]\n"
    "ds_read_b64_tr_b16 a[24:25], v11 offset:32768\n"
    "v_add_f32 v112, v112, v128\n"
    "v_add_f32 v113, v113, v129\n"
    "v_mfma_f32_16x16x32_bf16 v[120:123], a[48:51], v[228:231], v[120:123]\n"
    "ds_read_b64_tr_b16 a[26:27], v11 offset:49152\n"
    "v_add_f32 v114, v114, v130\n"
    "v_add_f32 v115, v115, v131\n"
    "v_mfma_f32_16x16x32_bf16 v[76:79], a[0:3], v[224:227], v[76:79]\n"
    "ds_read_b64_tr_b16 a[28:29], v11 offset:36864\n"
    "v_add_f32 v68, v68, v60\n"
    "v_add_f32 v69, v69, v61\n"
    "v_mfma_f32_16x16x32_bf16 v[76:79], a[4:7], v[240:243], v[76:79]\n"
    "ds_read_b64_tr_b16 a[30:31], v11 offset:53248\n"
    "v_add_f32 v70, v70, v62\n"
    "v_add_f32 v71, v71, v63\n"
    "v_mfma_f32_16x16x32_bf16 v[76:79], a[8:11], a[136:139], v[76:79]\n"
    "s_waitcnt lgkmcnt(4)\n"
    "v_add_f32 v116, v116, v132\n"
    "v_add_f32 v117, v117, v133\n"
    "v_cvt_pk_bf16_f32 v64, v64, v65\n"
    "v_cvt_pk_bf16_f32 v65, v66, v67\n"
    "v_mfma_f32_16x16x32_bf16 v[124:127], a[8:11], a[140:143], v[124:127]\n"
    "v_add_f32 v118, v118, v134\n"
    "v_add_f32 v119, v119, v135\n"
    "v_cvt_pk_bf16_f32 v66, v68, v69\n"
    "v_cvt_pk_bf16_f32 v67, v70, v71\n"
    "v_mfma_f32_16x16x32_bf16 v[124:127], a[4:7], v[244:247], v[124:127]\n"
    "v_add_f32 v72, v72, v84\n"
    "v_add_f32 v73, v73, v85\n"
    "v_min_u32 v64, -2, v64\n"
    "v_min_u32 v65, -2, v65\n"
    "v_mfma_f32_16x16x32_bf16 v[124:127], a[0:3], v[228:231], v[124:127]\n"
    "v_add_f32 v74, v74, v86\n"
    "v_add_f32 v75, v75, v87\n"
    "v_min_u32 v66, -2, v66\n"
    "v_min_u32 v67, -2, v67\n"
    "v_mfma_f32_16x16x32_bf16 v[48:51], a[16:19], a[64:67], v[48:51]\n"
    "v_add_f32 v120, v120, v152\n"
    "v_add_f32 v121, v121, v153\n"
    "buffer_store_dwordx4 v[64:67], v16, s[44:47], s80 offen sc1\n"
    "v_mfma_f32_16x16x32_bf16 v[48:51], a[20:23], a[80:83], v[48:51]\n"
    "v_add_f32 v122, v122, v154\n"
    "v_add_f32 v123, v123, v155\n"
    "v_cvt_pk_bf16_f32 v112, v112, v113\n"
    "v_cvt_pk_bf16_f32 v113, v114, v115\n"
    "v_mfma_f32_16x16x32_bf16 v[48:51], a[208:211], a[104:107], v[48:51]\n"
    "ds_read_b64_tr_b16 a[32:33], v24 offset:0\n"
    "v_add_f32 v76, v76, v108\n"
    "v_add_f32 v77, v77, v109\n"
    "v_cvt_pk_bf16_f32 v114, v116, v117\n"
    "v_cvt_pk_bf16_f32 v115, v118, v119\n"
    "v_mfma_f32_16x16x32_bf16 v[96:99], a[20:23], a[84:87], v[96:99]\n"
    "ds_read_b64_tr_b16 a[34:35], v24 offset:16384\n"
    "v_add_f32 v78, v78, v110\n"
    "v_add_f32 v79, v79, v111\n"
    "v_min_u32 v112, -2, v112\n"
    "v_min_u32 v113, -2, v113\n"
    "v_mfma_f32_16x16x32_bf16 v[96:99], a[16:19], a[68:71], v[96:99]\n"
    "ds_read_b64_tr_b16 a[36:37], v24 offset:4096\n"
    "v_add_f32 v124, v124, v156\n"
    "v_add_f32 v125, v125, v157\n"
    "v_min_u32 v114, -2, v114\n"
    "v_min_u32 v115, -2, v115\n"
    "v_mfma_f32_16x16x32_bf16 v[96:99], a[208:211], a[120:123], v[96:99]\n"
    "ds_read_b64_tr_b16 a[38:39], v24 offset:20480\n"
    "v_add_f32 v126, v126, v158\n"
    "v_add_f32 v127, v127, v159\n"
    "buffer_store_dwordx4 v[112:115], v16, s[44:47], s80 offen offset:2048 sc1\n"
    "v_mfma_f32_16x16x32_bf16 v[144:147], a[16:19], a[72:75], v[144:147]\n"
    "v_cvt_pk_bf16_f32 v72, v72, v73\n"
    "v_cvt_pk_bf16_f32 v73, v74, v75\n"
    "v_mfma_f32_16x16x32_bf16 v[144:147], a[20:23], a[88:91], v[144:147]\n"
    "v_cvt_pk_bf16_f32 v74, v76, v77\n"
    "v_cvt_pk_bf16_f32 v75, v78, v79\n"
    "v_mfma_f32_16x16x32_bf16 v[192:195], a[20:23], a[92:95], v[192:195]\n"
    "v_min_u32 v72, -2, v72\n"
    "v_min_u32 v73, -2, v73\n"
    "v_mfma_f32_16x16x32_bf16 v[192:195], a[16:19], a[76:79], v[192:195]\n"
    "v_min_u32 v74, -2, v74\n"
    "v_min_u32 v75, -2, v75\n"
    "s_waitcnt lgkmcnt(4)\n"
    "v_mfma_f32_16x16x32_bf16 v[52:55], a[24:27], a[64:67], v[52:55]\n"
    "ds_read_b64_tr_b16 a[40:41], v25 offset:0\n"
    "buffer_store_dwordx4 v[72:75], v16, s[44:47], s80 offen offset:1024 sc1\n"
    "v_mfma_f32_16x16x32_bf16 v[52:55], a[28:31], a[80:83], v[52:55]\n"
    "ds_read_b64_tr_b16 a[42:43], v25 offset:16384\n"
    "v_cvt_pk_bf16_f32 v120, v120, v121\n"
    "v_cvt_pk_bf16_f32 v121, v122, v123\n"
    "v_mfma_f32_16x16x32_bf16 v[52:55], a[208:211], a[108:111], v[52:55]\n"
    "ds_read_b64_tr_b16 a[44:45], v25 offset:4096\n"
    "v_cvt_pk_bf16_f32 v122, v124, v125\n"
    "v_cvt_pk_bf16_f32 v123, v126, v127\n"
    "v_mfma_f32_16x16x32_bf16 v[100:103], a[28:31], a[84:87], v[100:103]\n"
    "ds_read_b64_tr_b16 a[46:47], v25 offset:20480\n"
    "v_min_u32 v120, -2, v120\n"
    "v_min_u32 v121, -2, v121\n"
    "v_mfma_f32_16x16x32_bf16 v[100:103], a[24:27], a[68:71], v[100:103]\n"
    "v_min_u32 v122, -2, v122\n"
    "v_min_u32 v123, -2, v123\n"
    "v_mfma_f32_16x16x32_bf16 v[100:103], a[208:211], a[124:127], v[100:103]\n"
    "buffer_store_dwordx4 v[120:123], v16, s[44:47], s80 offen offset:3072 sc1\n"
    "v_mfma_f32_16x16x32_bf16 v[148:151], a[24:27], a[72:75], v[148:151]\n"
    "v_mfma_f32_16x16x32_bf16 v[148:151], a[28:31], a[88:91], v[148:151]\n"
    "v_mfma_f32_16x16x32_bf16 v[196:199], a[28:31], a[92:95], v[196:199]\n"
    "ds_read_b64_tr_b16 a[48:49], v26 offset:0\n"
    "v_mfma_f32_16x16x32_bf16 v[196:199], a[24:27], a[76:79], v[196:199]\n"
    "ds_read_b64_tr_b16 a[50:51], v26 offset:16384\n"
    "s_waitcnt lgkmcnt(4)\n"
    "v_mfma_f32_16x16x32_bf16 v[40:43], a[32:35], a[224:227], v[40:43]\n"
    "ds_read_b64_tr_b16 a[52:53], v26 offset:4096\n"
    "v_mfma_f32_16x16x32_bf16 v[40:43], a[36:39], a[240:243], v[40:43]\n"
    "ds_read_b64_tr_b16 a[54:55], v26 offset:20480\n"
    "v_mfma_f32_16x16x32_bf16 v[88:91], a[36:39], a[244:247], v[88:91]\n"
    "v_mfma_f32_16x16x32_bf16 v[88:91], a[32:35], a[228:231], v[88:91]\n"
    "v_mfma_f32_16x16x32_bf16 v[136:139], a[32:35], a[232:235], v[136:139]\n"
    "v_mfma_f32_16x16x32_bf16 v[136:139], a[36:39], a[248:251], v[136:139]\n"
    "v_mfma_f32_16x16x32_bf16 v[184:187], a[36:39], a[252:255], v[184:187]\n"
    "ds_read_b64_tr_b16 a[56:57], v27 offset:0\n"
    "v_mfma_f32_16x16x32_bf16 v[184:187], a[32:35], a[236:239], v[184:187]\n"
    "ds_read_b64_tr_b16 a[58:59], v27 offset:16384\n"
    "v_mfma_f32_16x16x32_bf16 v[44:47], a[40:43], a[224:227], v[44:47]\n"
    "ds_read_b64_tr_b16 a[60:61], v27 offset:4096\n"
    "s_waitcnt lgkmcnt(4)\n"
    "v_mfma_f32_16x16x32_bf16 v[44:47], a[44:47], a[240:243], v[44:47]\n"
    "ds_read_b64_tr_b16 a[62:63], v27 offset:20480\n"
    "v_mfma_f32_16x16x32_bf16 v[92:95], a[44:47], a[244:247], v[92:95]\n"
    "v_mfma_f32_16x16x32_bf16 v[92:95], a[40:43], a[228:231], v[92:95]\n"
    "v_mfma_f32_16x16x32_bf16 v[140:143], a[40:43], a[232:235], v[140:143]\n"
    "v_mfma_f32_16x16x32_bf16 v[140:143], a[44:47], a[248:251], v[140:143]\n"
    "s_waitcnt vmcnt(4)\n"
    "s_barrier\n"
    "v_mfma_f32_16x16x32_bf16 v[188:191], a[44:47], a[252:255], v[188:191]\n"
    "ds_read_b64_tr_b16 a[0:1], v24 offset:32768\n"
    "v_mfma_f32_16x16x32_bf16 v[188:191], a[40:43], a[236:239], v[188:191]\n"
    "ds_read_b64_tr_b16 a[2:3], v24 offset:49152\n"
    "v_mfma_f32_16x16x32_bf16 v[48:51], a[48:51], a[224:227], v[48:51]\n"
    "ds_read_b64_tr_b16 a[4:5], v24 offset:36864\n"
    "s_waitcnt lgkmcnt(4)\n"
    "v_mfma_f32_16x16x32_bf16 v[48:51], a[52:55], a[240:243], v[48:51]\n"
    "ds_read_b64_tr_b16 a[6:7], v24 offset:53248\n"
    "v_mfma_f32_16x16x32_bf16 v[96:99], a[52:55], a[244:247], v[96:99]\n"
    "ds_read_b64_tr_b16 a[8:9], v8 offset:0\n"
    "v_mfma_f32_16x16x32_bf16 v[96:99], a[48:51], a[228:231], v[96:99]\n"
    "ds_read_b64_tr_b16 a[10:11], v8 offset:16384\n"
    "v_mfma_f32_16x16x32_bf16 v[144:147], a[48:51], a[232:235], v[144:147]\n"
    "ds_read_b64_tr_b16 a[16:17], v25 offset:32768\n"
    "v_mfma_f32_16x16x32_bf16 v[144:147], a[52:55], a[248:251], v[144:147]\n"
    "ds_read_b64_tr_b16 a[18:19], v25 offset:49152\n"
    "v_mfma_f32_16x16x32_bf16 v[192:195], a[52:55], a[252:255], v[192:195]\n"
    "ds_read_b64_tr_b16 a[20:21], v25 offset:36864\n"
    "v_mfma_f32_16x16x32_bf16 v[192:195], a[48:51], a[236:239], v[192:195]\n"
    "ds_read_b64_tr_b16 a[22:23], v25 offset:53248\n"
    "v_mfma_f32_16x16x32_bf16 v[52:55], a[56:59], a[224:227], v[52:55]\n"
    "ds_read_b64_tr_b16 a[24:25], v9 offset:0\n"
    "s_waitcnt lgkmcnt(4)\n"
    "v_mfma_f32_16x16x32_bf16 v[52:55], a[60:63], a[240:243], v[52:55]\n"
    "ds_read_b64_tr_b16 a[26:27], v9 offset:16384\n"
    "v_mfma_f32_16x16x32_bf16 v[100:103], a[60:63], a[244:247], v[100:103]\n"
    "ds_read_b64_tr_b16 a[32:33], v26 offset:32768\n"
    "v_mfma_f32_16x16x32_bf16 v[100:103], a[56:59], a[228:231], v[100:103]\n"
    "ds_read_b64_tr_b16 a[34:35], v26 offset:49152\n"
    "v_mfma_f32_16x16x32_bf16 v[148:151], a[56:59], a[232:235], v[148:151]\n"
    "ds_read_b64_tr_b16 a[36:37], v26 offset:36864\n"
    "v_mfma_f32_16x16x32_bf16 v[148:151], a[60:63], a[248:251], v[148:151]\n"
    "ds_read_b64_tr_b16 a[38:39], v26 offset:53248\n"
    "v_mfma_f32_16x16x32_bf16 v[196:199], a[60:63], a[252:255], v[196:199]\n"
    "ds_read_b64_tr_b16 a[40:41], v10 offset:0\n"
    "v_mfma_f32_16x16x32_bf16 v[196:199], a[56:59], a[236:239], v[196:199]\n"
    "ds_read_b64_tr_b16 a[42:43], v10 offset:16384\n"
    "v_mfma_f32_16x16x32_bf16 v[136:139], a[0:3], v[232:235], v[136:139]\n"
    "ds_read_b64_tr_b16 a[48:49], v27 offset:32768\n"
    "v_mfma_f32_16x16x32_bf16 v[136:139], a[4:7], v[248:251], v[136:139]\n"
    "ds_read_b64_tr_b16 a[50:51], v27 offset:49152\n"
    "v_mfma_f32_16x16x32_bf16 v[136:139], a[8:11], a[144:147], v[136:139]\n"
    "ds_read_b64_tr_b16 a[52:53], v27 offset:36864\n"
    "v_mfma_f32_16x16x32_bf16 v[184:187], a[8:11], a[148:151], v[184:187]\n"
    "ds_read_b64_tr_b16 a[54:55], v27 offset:53248\n"
    "v_mfma_f32_16x16x32_bf16 v[184:187], a[4:7], v[252:255], v[184:187]\n"
    "ds_read_b64_tr_b16 a[56:57], v11 offset:0\n"
    "s_barrier\n"
    "v_mfma_f32_16x16x32_bf16 v[184:187], a[0:3], v[236:239], v[184:187]\n"
    "ds_read_b64_tr_b16 a[58:59], v11 offset:16384\n"
    "s_waitcnt lgkmcnt(4)\n"
    "v_mfma_f32_16x16x32_bf16 v[140:143], a[16:19], v[232:235], v[140:143]\n"
    "v_mfma_f32_16x16x32_bf16 v[140:143], a[20:23], v[248:251], v[140:143]\n"
    "v_mfma_f32_16x16x32_bf16 v[140:143], a[24:27], a[144:147], v[140:143]\n"
    "ds_write_b128 v29, v[136:139] offset:0\n"
    "v_mfma_f32_16x16x32_bf16 v[188:191], a[24:27], a[148:151], v[188:191]\n"
    "v_mfma_f32_16x16x32_bf16 v[188:191], a[20:23], v[252:255], v[188:191]\n"
    "v_mfma_f32_16x16x32_bf16 v[188:191], a[16:19], v[236:239], v[188:191]\n"
    "ds_write_b128 v29, v[184:187] offset:4096\n"
    "v_mfma_f32_16x16x32_bf16 v[144:147], a[32:35], v[232:235], v[144:147]\n"
    "v_mfma_f32_16x16x32_bf16 v[144:147], a[36:39], v[248:251], v[144:147]\n"
    "v_mfma_f32_16x16x32_bf16 v[144:147], a[40:43], a[144:147], v[144:147]\n"
    "ds_write_b128 v29, v[140:143] offset:1024\n"
    "v_mfma_f32_16x16x32_bf16 v[192:195], a[40:43], a[148:151], v[192:195]\n"
    "v_mfma_f32_16x16x32_bf16 v[192:195], a[36:39], v[252:255], v[192:195]\n"
    "v_accvgpr_read_b32 v23, a170\n"
    "v_mfma_f32_16x16x32_bf16 v[192:195], a[32:35], v[236:239], v[192:195]\n"
    "ds_write_b128 v29, v[188:191] offset:5120\n"
    "v_accvgpr_read_b32 v20, a196\n"
    "v_mfma_f32_16x16x32_bf16 v[148:151], a[48:51], v[232:235], v[148:151]\n"
    "v_accvgpr_read_b32 v18, a197\n"
    "s_waitcnt lgkmcnt(4)\n"
    "v_mfma_f32_16x16x32_bf16 v[148:151], a[52:55], v[248:251], v[148:151]\n"
    "v_mfma_f32_16x16x32_bf16 v[148:151], a[56:59], a[144:147], v[148:151]\n"
    "ds_write_b128 v29, v[144:147] offset:2048\n"
    "v_mfma_f32_16x16x32_bf16 v[196:199], a[56:59], a[148:151], v[196:199]\n"
    "v_mfma_f32_16x16x32_bf16 v[196:199], a[52:55], v[252:255], v[196:199]\n"
    "v_mfma_f32_16x16x32_bf16 v[196:199], a[48:51], v[236:239], v[196:199]\n"
    "ds_write_b128 v29, v[192:195] offset:6144\n"
    "v_mfma_f32_16x16x32_bf16 v[40:43], a[0:3], v[224:227], v[40:43]\n"
    "v_mfma_f32_16x16x32_bf16 v[40:43], a[4:7], v[240:243], v[40:43]\n"
    "buffer_load_dwordx4 v[32:35], v16, s[44:47], s81 offen sc1\n"
    "v_mfma_f32_16x16x32_bf16 v[40:43], a[8:11], a[136:139], v[40:43]\n"
    "ds_write_b128 v29, v[148:151] offset:3072\n"
    "v_mfma_f32_16x16x32_bf16 v[88:91], a[8:11], a[140:143], v[88:91]\n"
    "buffer_load_dwordx4 v[56:59], v16, s[44:47], s81 offen offset:1024 sc1\n"
    "v_mfma_f32_16x16x32_bf16 v[88:91], a[4:7], v[244:247], v[88:91]\n"
    "v_mfma_f32_16x16x32_bf16 v[88:91], a[0:3], v[228:231], v[88:91]\n"
    "ds_write_b128 v29, v[196:199] offset:7168\n"
    "buffer_load_dwordx4 v[80:83], v16, s[44:47], s81 offen offset:2048 sc1\n"
    "v_mfma_f32_16x16x32_bf16 v[44:47], a[16:19], v[224:227], v[44:47]\n"
    "v_mfma_f32_16x16x32_bf16 v[44:47], a[20:23], v[240:243], v[44:47]\n"
    "s_waitcnt lgkmcnt(0)\n"
    "s_barrier\n"
    "buffer_load_dwordx4 v[104:107], v16, s[44:47], s81 offen offset:3072 sc1\n"
    "v_mfma_f32_16x16x32_bf16 v[44:47], a[24:27], a[136:139], v[44:47]\n"
    "ds_read_b128 v[36:39], v31 offset:0\n"
    "v_mfma_f32_16x16x32_bf16 v[92:95], a[24:27], a[140:143], v[92:95]\n"
    "ds_read_b128 v[128:131], v31 offset:4096\n"
    "v_mfma_f32_16x16x32_bf16 v[92:95], a[20:23], v[244:247], v[92:95]\n"
    "ds_read_b128 v[60:63], v31 offset:1024\n"
    "v_mfma_f32_16x16x32_bf16 v[92:95], a[16:19], v[228:231], v[92:95]\n"
    "ds_read_b128 v[132:135], v31 offset:5120\n"
    "v_mfma_f32_16x16x32_bf16 v[48:51], a[32:35], v[224:227], v[48:51]\n"
    "ds_read_b128 v[84:87], v31 offset:2048\n"
    "v_mfma_f32_16x16x32_bf16 v[48:51], a[36:39], v[240:243], v[48:51]\n"
    "ds_read_b128 v[152:155], v31 offset:6144\n"
    "v_mfma_f32_16x16x32_bf16 v[48:51], a[40:43], a[136:139], v[48:51]\n"
    "ds_read_b128 v[108:111], v31 offset:3072\n"
    "s_waitcnt lgkmcnt(4)\n"
    "v_add_f32 v40, v40, v36\n"
    "v_add_f32 v41, v41, v37\n"
    "v_mfma_f32_16x16x32_bf16 v[96:99], a[40:43], a[140:143], v[96:99]\n"
    "ds_read_b128 v[156:159], v31 offset:7168\n"
    "v_add_f32 v42, v42, v38\n"
    "v_add_f32 v43, v43, v39\n"
    "v_mfma_f32_16x16x32_bf16 v[96:99], a[36:39], v[244:247], v[96:99]\n"
    "v_add_f32 v88, v88, v128\n"
    "v_add_f32 v89, v89, v129\n"
    "v_mfma_f32_16x16x32_bf16 v[96:99], a[32:35], v[228:231], v[96:99]\n"
    "v_add_f32 v90, v90, v130\n"
    "v_add_f32 v91, v91, v131\n"
    "v_mfma_f32_16x16x32_bf16 v[52:55], a[48:51], v[224:227], v[52:55]\n"
    "v_add_f32 v44, v44, v60\n"
    "v_add_f32 v45, v45, v61\n"
    "v_mfma_f32_16x16x32_bf16 v[52:55], a[52:55], v[240:243], v[52:55]\n"
    "v_add_f32 v46, v46, v62\n"
    "v_add_f32 v47, v47, v63\n"
    "v_mfma_f32_16x16x32_bf16 v[52:55], a[56:59], a[136:139], v[52:55]\n"
    "s_waitcnt lgkmcnt(4)\n"
    "v_add_f32 v92, v92, v132\n"
    "v_add_f32 v93, v93, v133\n"
    "v_mfma_f32_16x16x32_bf16 v[100:103], a[56:59], a[140:143], v[100:103]\n"
    "v_add_f32 v94, v94, v134\n"
    "v_add_f32 v95, v95, v135\n"
    "v_mfma_f32_16x16x32_bf16 v[100:103], a[52:55], v[244:247], v[100:103]\n"
    "s_waitcnt lgkmcnt(3)\n"
    "v_add_f32 v48, v48, v84\n"
    "v_add_f32 v49, v49, v85\n"
    "v_mfma_f32_16x16x32_bf16 v[100:103], a[48:51], v[228:231], v[100:103]\n"
    "v_add_f32 v50, v50, v86\n"
    "v_add_f32 v51, v51, v87\n"
    "s_waitcnt lgkmcnt(2)\n"
    "v_pk_add_f32 v[96:97], v[96:97], v[152:153]\n"
    "v_pk_add_f32 v[98:99], v[98:99], v[154:155]\n"
    "s_waitcnt lgkmcnt(1)\n"
    "v_pk_add_f32 v[52:53], v[52:53], v[108:109]\n"
    "v_pk_add_f32 v[54:55], v[54:55], v[110:111]\n"
    "s_waitcnt lgkmcnt(0)\n"
    "v_pk_add_f32 v[100:101], v[100:101], v[156:157]\n"
    "v_pk_add_f32 v[102:103], v[102:103], v[158:159]\n"
    "s_branch .Lk2_zend\n"
    ".Lk2_zs:\n"
    ".Lk2_m8172:\n"
    "s_cbranch_execz .Lk2_hret\n"
    "v_mfma_f32_16x16x32_bf16 v[64:67], a[0:3], a[64:67], v[64:67]\n"
    "ds_read_b64_tr_b16 a[16:17], v10 offset:33792\n"
    "v_mfma_f32_16x16x32_bf16 v[64:67], a[4:7], a[80:83], v[64:67]\n"
    "buffer_load_dwordx4 a[224:227], v14, s[44:47], s84 offen sc1\n"
    "v_mfma_f32_16x16x32_bf16 v[112:115], a[4:7], a[84:87], v[112:115]\n"
    "v_xor_b32 v24, 0x10000, v8\n"
    "v_xor_b32 v25, 0x10000, v9\n"
    "ds_read_b64_tr_b16 a[18:19], v10 offset:50176\n"
    "v_mfma_f32_16x16x32_bf16 v[112:115], a[0:3], a[68:71], v[112:115]\n"
    "v_readlane_b32 s85, v15, s89\n"
    "s_add_u32 s89, s89, 1\n"
    "ds_read_b64_tr_b16 a[20:21], v10 offset:37888\n"
    "v_mfma_f32_16x16x32_bf16 v[160:163], a[0:3], a[72:75], v[160:163]\n"
    "buffer_load_dwordx4 a[228:231], v14, s[44:47], s84 offen offset:1024 sc1\n"
    "v_mfma_f32_16x16x32_bf16 v[160:163], a[4:7], a[88:91], v[160:163]\n"
    "v_xor_b32 v26, 0x10000, v10\n"
    "v_xor_b32 v27, 0x10000, v11\n"
    "ds_read_b64_tr_b16 a[22:23], v10 offset:54272\n"
    "v_mfma_f32_16x16x32_bf16 v[208:211], a[4:7], a[92:95], v[208:211]\n"
    "v_readlane_b32 s86, v28, s90\n"
    "s_add_u32 s90, s90, 1\n"
    "ds_read_b64_tr_b16 a[24:25], v11 offset:33792\n"
    "v_mfma_f32_16x16x32_bf16 v[208:211], a[0:3], a[76:79], v[208:211]\n"
    "buffer_load_dwordx4 a[232:235], v7, s[44:47], s84 offen sc1\n"
    "s_waitcnt lgkmcnt(4)\n"
    "v_mfma_f32_16x16x32_bf16 v[68:71], a[8:11], a[64:67], v[68:71]\n"
    "v_add_u32 v29, s52, v16\n"
    "v_add_u32 v29, 0x20000, v29\n"
    "ds_read_b64_tr_b16 a[26:27], v11 offset:50176\n"
    "v_mfma_f32_16x16x32_bf16 v[68:71], a[12:15], a[80:83], v[68:71]\n"
    "ds_read_b64_tr_b16 a[28:29], v11 offset:37888\n"
    "v_mfma_f32_16x16x32_bf16 v[116:119], a[12:15], a[84:87], v[116:119]\n"
    "buffer_load_dwordx4 a[236:239], v7, s[44:47], s84 offen offset:1024 sc1\n"
    "v_mfma_f32_16x16x32_bf16 v[116:119], a[8:11], a[68:71], v[116:119]\n"
    "v_xor_b32 v31, 0x2000, v29\n"
    "ds_read_b64_tr_b16 a[30:31], v11 offset:54272\n"
    "v_mfma_f32_16x16x32_bf16 v[164:167], a[8:11], a[72:75], v[164:167]\n"
    "v_mfma_f32_16x16x32_bf16 v[164:167], a[12:15], a[88:91], v[164:167]\n"
    "buffer_load_dwordx4 a[240:243], v14, s[44:47], s85 offen sc1\n"
    "v_mfma_f32_16x16x32_bf16 v[212:215], a[12:15], a[92:95], v[212:215]\n"
    "ds_read_b64_tr_b16 a[32:33], v8 offset:32768\n"
    "v_mfma_f32_16x16x32_bf16 v[212:215], a[8:11], a[76:79], v[212:215]\n"
    "v_readlane_b32 s84, v15, s89\n"
    "s_add_u32 s89, s89, 1\n"
    "ds_read_b64_tr_b16 a[34:35], v8 offset:49152\n"
    "s_waitcnt lgkmcnt(4)\n"
    "v_mfma_f32_16x16x32_bf16 v[72:75], a[16:19], a[64:67], v[72:75]\n"
    "buffer_load_dwordx4 a[244:247], v14, s[44:47], s85 offen offset:1024 sc1\n"
    "v_mfma_f32_16x16x32_bf16 v[72:75], a[20:23], a[80:83], v[72:75]\n"
    "ds_read_b64_tr_b16 a[36:37], v8 offset:36864\n"
    "v_mfma_f32_16x16x32_bf16 v[120:123], a[20:23], a[84:87], v[120:123]\n"
    "s_xor_b32 s94, s95, 0x10000\n"
    "ds_read_b64_tr_b16 a[38:39], v8 offset:53248\n"
    "v_mfma_f32_16x16x32_bf16 v[120:123], a[16:19], a[68:71], v[120:123]\n"
    "buffer_load_dwordx4 a[248:251], v7, s[44:47], s85 offen sc1\n"
    "v_mfma_f32_16x16x32_bf16 v[168:171], a[16:19], a[72:75], v[168:171]\n"
    "v_mfma_f32_16x16x32_bf16 v[168:171], a[20:23], a[88:91], v[168:171]\n"
    "v_mfma_f32_16x16x32_bf16 v[216:219], a[20:23], a[92:95], v[216:219]\n"
    "buffer_load_dwordx4 a[252:255], v7, s[44:47], s85 offen offset:1024 sc1\n"
    "v_mfma_f32_16x16x32_bf16 v[216:219], a[16:19], a[76:79], v[216:219]\n"
    "v_mfma_f32_16x16x32_bf16 v[76:79], a[24:27], a[64:67], v[76:79]\n"
    "s_add_u32 m0, s95, 32768\n"
    "ds_read_b64_tr_b16 a[40:41], v9 offset:32768\n"
    "s_waitcnt lgkmcnt(4)\n"
    "v_mfma_f32_16x16x32_bf16 v[76:79], a[28:31], a[80:83], v[76:79]\n"
    "buffer_load_dwordx4 v13, s[48:51], s86 offen offset:1024 lds\n"
    "v_mfma_f32_16x16x32_bf16 v[124:127], a[28:31], a[84:87], v[124:127]\n"
    "ds_read_b64_tr_b16 a[42:43], v9 offset:49152\n"
    "v_mfma_f32_16x16x32_bf16 v[124:127], a[24:27], a[68:71], v[124:127]\n"
    "ds_read_b64_tr_b16 a[44:45], v9 offset:36864\n"
    "v_mfma_f32_16x16x32_bf16 v[172:175], a[24:27], a[72:75], v[172:175]\n"
    "buffer_load_dwordx4 v6, s[48:51], s86 offen offset:3072 lds\n"
    "v_mfma_f32_16x16x32_bf16 v[172:175], a[28:31], a[88:91], v[172:175]\n"
    "ds_read_b64_tr_b16 a[46:47], v9 offset:53248\n"
    "v_mfma_f32_16x16x32_bf16 v[220:223], a[28:31], a[92:95], v[220:223]\n"
    "v_mfma_f32_16x16x32_bf16 v[220:223], a[24:27], a[76:79], v[220:223]\n"
    "buffer_load_dwordx4 v[224:227], v14, s[44:47], s84 offen sc1\n"
    "s_waitcnt lgkmcnt(4)\n"
    "v_mfma_f32_16x16x32_bf16 v[40:43], a[32:35], a[64:67], v[40:43]\n"
    "v_mfma_f32_16x16x32_bf16 v[40:43], a[36:39], a[80:83], v[40:43]\n"
    "s_waitcnt vmcnt(11)\n"
    "s_barrier\n"
    "v_mfma_f32_16x16x32_bf16 v[40:43], a[208:211], a[96:99], v[40:43]\n"
    "buffer_load_dwordx4 v[228:231], v14, s[44:47], s84 offen offset:1024 sc1\n"
    "v_mfma_f32_16x16x32_bf16 v[88:91], a[36:39], a[84:87], v[88:91]\n"
    "ds_read_b64_tr_b16 a[48:49], v24 offset:1024\n"
    "v_mfma_f32_16x16x32_bf16 v[88:91], a[32:35], a[68:71], v[88:91]\n"
    "ds_read_b64_tr_b16 a[50:51], v24 offset:17408\n"
    "v_mfma_f32_16x16x32_bf16 v[88:91], a[208:211], a[112:115], v[88:91]\n"
    "buffer_load_dwordx4 v[232:235], v7, s[44:47], s84 offen sc1\n"
    "v_mfma_f32_16x16x32_bf16 v[136:139], a[32:35], a[72:75], v[136:139]\n"
    "ds_read_b64_tr_b16 a[52:53], v24 offset:5120\n"
    "s_waitcnt vmcnt(3)\n"
    "s_barrier\n"
    "v_mfma_f32_16x16x32_bf16 v[136:139], a[36:39], a[88:91], v[136:139]\n"
    "ds_read_b64_tr_b16 a[54:55], v24 offset:21504\n"
    "v_mfma_f32_16x16x32_bf16 v[184:187], a[36:39], a[92:95], v[184:187]\n"
    "buffer_load_dwordx4 v[236:239], v7, s[44:47], s84 offen offset:1024 sc1\n"
    "v_mfma_f32_16x16x32_bf16 v[184:187], a[32:35], a[76:79], v[184:187]\n"
    "ds_read_b64_tr_b16 a[56:57], v24 offset:33792\n"
    "s_waitcnt lgkmcnt(4)\n"
    "v_mfma_f32_16x16x32_bf16 v[44:47], a[40:43], a[64:67], v[44:47]\n"
    "ds_read_b64_tr_b16 a[58:59], v24 offset:50176\n"
    "v_mfma_f32_16x16x32_bf16 v[44:47], a[44:47], a[80:83], v[44:47]\n"
    "s_mov_b64 exec, 1\n"
    "buffer_load_dword v21, off, s[44:47], s83 sc1\n"
    "s_mov_b64 exec, -1\n"
    "v_mfma_f32_16x16x32_bf16 v[44:47], a[208:211], a[100:103], v[44:47]\n"
    "ds_read_b64_tr_b16 a[0:1], v25 offset:1024\n"
    "v_mfma_f32_16x16x32_bf16 v[92:95], a[44:47], a[84:87], v[92:95]\n"
    "s_add_u32 m0, s95, 32768\n"
    "ds_read_b64_tr_b16 a[2:3], v25 offset:17408\n"
    "v_mfma_f32_16x16x32_bf16 v[92:95], a[40:43], a[68:71], v[92:95]\n"
    "buffer_load_dwordx4 v12, s[48:51], s86 offen lds\n"
    "v_mfma_f32_16x16x32_bf16 v[92:95], a[208:211], a[116:119], v[92:95]\n"
    "ds_read_b64_tr_b16 a[4:5], v25 offset:5120\n"
    "v_mfma_f32_16x16x32_bf16 v[140:143], a[40:43], a[72:75], v[140:143]\n"
    "ds_read_b64_tr_b16 a[6:7], v25 offset:21504\n"
    "v_mfma_f32_16x16x32_bf16 v[140:143], a[44:47], a[88:91], v[140:143]\n"
    "buffer_load_dwordx4 v5, s[48:51], s86 offen offset:2048 lds\n"
    "v_mfma_f32_16x16x32_bf16 v[188:191], a[44:47], a[92:95], v[188:191]\n"
    "ds_read_b64_tr_b16 a[8:9], v25 offset:33792\n"
    "v_mfma_f32_16x16x32_bf16 v[188:191], a[40:43], a[76:79], v[188:191]\n"
    "ds_read_b64_tr_b16 a[10:11], v25 offset:50176\n"
    "s_waitcnt vmcnt(9)\n"
    "s_waitcnt lgkmcnt(4)\n"
    "v_mfma_f32_16x16x32_bf16 v[160:163], a[48:51], a[232:235], v[160:163]\n"
    "ds_read_b64_tr_b16 a[16:17], v26 offset:1024\n"
    "v_mfma_f32_16x16x32_bf16 v[160:163], a[52:55], a[248:251], v[160:163]\n"
    "ds_read_b64_tr_b16 a[18:19], v26 offset:17408\n"
    "s_waitcnt vmcnt(3)\n"
    "v_mfma_f32_16x16x32_bf16 v[160:163], a[56:59], v[232:235], v[160:163]\n"
    "ds_read_b64_tr_b16 a[20:21], v26 offset:5120\n"
    "v_mfma_f32_16x16x32_bf16 v[208:211], a[56:59], v[236:239], v[208:211]\n"
    "ds_read_b64_tr_b16 a[22:23], v26 offset:21504\n"
    "v_mfma_f32_16x16x32_bf16 v[208:211], a[52:55], a[252:255], v[208:211]\n"
    "ds_read_b64_tr_b16 a[24:25], v26 offset:33792\n"
    "v_mfma_f32_16x16x32_bf16 v[208:211], a[48:51], a[236:239], v[208:211]\n"
    "ds_read_b64_tr_b16 a[26:27], v26 offset:50176\n"
    "v_mfma_f32_16x16x32_bf16 v[164:167], a[0:3], a[232:235], v[164:167]\n"
    "ds_read_b64_tr_b16 a[32:33], v27 offset:1024\n"
    "s_waitcnt lgkmcnt(4)\n"
    "v_mfma_f32_16x16x32_bf16 v[164:167], a[4:7], a[248:251], v[164:167]\n"
    "ds_read_b64_tr_b16 a[34:35], v27 offset:17408\n"
    "v_mfma_f32_16x16x32_bf16 v[164:167], a[8:11], v[232:235], v[164:167]\n"
    "ds_read_b64_tr_b16 a[36:37], v27 offset:5120\n"
    "ds_write_b128 v29, v[160:163] offset:0\n"
    "v_mfma_f32_16x16x32_bf16 v[212:215], a[8:11], v[236:239], v[212:215]\n"
    "ds_read_b64_tr_b16 a[38:39], v27 offset:21504\n"
    "v_mfma_f32_16x16x32_bf16 v[212:215], a[4:7], a[252:255], v[212:215]\n"
    "ds_read_b64_tr_b16 a[40:41], v27 offset:33792\n"
    "v_mfma_f32_16x16x32_bf16 v[212:215], a[0:3], a[236:239], v[212:215]\n"
    "ds_read_b64_tr_b16 a[42:43], v27 offset:50176\n"
    "ds_write_b128 v29, v[208:211] offset:4096\n"
    "v_mfma_f32_16x16x32_bf16 v[168:171], a[16:19], a[232:235], v[168:171]\n"
    "s_waitcnt lgkmcnt(4)\n"
    "v_mfma_f32_16x16x32_bf16 v[168:171], a[20:23], a[248:251], v[168:171]\n"
    "v_mfma_f32_16x16x32_bf16 v[168:171], a[24:27], v[232:235], v[168:171]\n"
    "ds_write_b128 v29, v[164:167] offset:1024\n"
    "v_mfma_f32_16x16x32_bf16 v[216:219], a[24:27], v[236:239], v[216:219]\n"
    "v_mfma_f32_16x16x32_bf16 v[216:219], a[20:23], a[252:255], v[216:219]\n"
    "v_mfma_f32_16x16x32_bf16 v[216:219], a[16:19], a[236:239], v[216:219]\n"
    "ds_write_b128 v29, v[212:215] offset:5120\n"
    "v_mfma_f32_16x16x32_bf16 v[172:175], a[32:35], a[232:235], v[172:175]\n"
    "s_waitcnt lgkmcnt(4)\n"
    "v_mfma_f32_16x16x32_bf16 v[172:175], a[36:39], a[248:251], v[172:175]\n"
    "s_waitcnt lgkmcnt(3)\n"
    "v_mfma_f32_16x16x32_bf16 v[172:175], a[40:43], v[232:235], v[172:175]\n"
    "ds_write_b128 v29, v[168:171] offset:2048\n"
    "v_mfma_f32_16x16x32_bf16 v[220:223], a[40:43], v[236:239], v[220:223]\n"
    "v_mfma_f32_16x16x32_bf16 v[220:223], a[36:39], a[252:255], v[220:223]\n"
    "v_mfma_f32_16x16x32_bf16 v[220:223], a[32:35], a[236:239], v[220:223]\n"
    "ds_write_b128 v29, v[216:219] offset:6144\n"
    "v_mfma_f32_16x16x32_bf16 v[64:67], a[48:51], a[224:227], v[64:67]\n"
    "v_mfma_f32_16x16x32_bf16 v[64:67], a[52:55], a[240:243], v[64:67]\n"
    "v_mfma_f32_16x16x32_bf16 v[64:67], a[56:59], v[224:227], v[64:67]\n"
    "ds_write_b128 v29, v[172:175] offset:3072\n"
    "v_mfma_f32_16x16x32_bf16 v[112:115], a[56:59], v[228:231], v[112:115]\n"
    "v_mfma_f32_16x16x32_bf16 v[112:115], a[52:55], a[244:247], v[112:115]\n"
    "v_mfma_f32_16x16x32_bf16 v[112:115], a[48:51], a[228:231], v[112:115]\n"
    "ds_write_b128 v29, v[220:223] offset:7168\n"
    "v_mfma_f32_16x16x32_bf16 v[68:71], a[0:3], a[224:227], v[68:71]\n"
    "ds_read_b64_tr_b16 a[48:49], v10 offset:32768\n"
    "v_mfma_f32_16x16x32_bf16 v[68:71], a[4:7], a[240:243], v[68:71]\n"
    "ds_read_b64_tr_b16 a[50:51], v10 offset:49152\n"
    "s_waitcnt lgkmcnt(2)\n"
    "s_barrier\n"
    "v_mfma_f32_16x16x32_bf16 v[68:71], a[8:11], v[224:227], v[68:71]\n"
    "ds_read_b64_tr_b16 a[52:53], v10 offset:36864\n"
    "ds_read_b128 v[36:39], v31 offset:0\n"
    "s_waitcnt vmcnt(2)\n"
    "v_cmp_ne_u32 vcc, v22, v21\n"
    "v_mfma_f32_16x16x32_bf16 v[116:119], a[8:11], v[228:231], v[116:119]\n"
    "ds_read_b64_tr_b16 a[54:55], v10 offset:53248\n"
    "ds_read_b128 v[128:131], v31 offset:4096\n"
    "v_mfma_f32_16x16x32_bf16 v[116:119], a[4:7], a[244:247], v[116:119]\n"
    "ds_read_b128 v[60:63], v31 offset:1024\n"
    "s_bitcmp0_b32 vcc_lo, 0\n"
    "s_cbranch_scc0 .Lk2_rslowb\n"
    ".Lk2_rokb:\n"
    "v_mfma_f32_16x16x32_bf16 v[116:119], a[0:3], a[228:231], v[116:119]\n"
    "ds_read_b128 v[132:135], v31 offset:5120\n"
    "v_mfma_f32_16x16x32_bf16 v[72:75], a[16:19], a[224:227], v[72:75]\n"
    "ds_read_b128 v[84:87], v31 offset:2048\n"
    "v_mfma_f32_16x16x32_bf16 v[72:75], a[20:23], a[240:243], v[72:75]\n"
    "ds_read_b128 v[152:155], v31 offset:6144\n"
    "v_mfma_f32_16x16x32_bf16 v[72:75], a[24:27], v[224:227], v[72:75]\n"
    "ds_read_b128 v[108:111], v31 offset:3072\n"
    "s_waitcnt lgkmcnt(4)\n"
    "v_add_f32 v64, v64, v36\n"
    "v_add_f32 v65, v65, v37\n"
    "v_mfma_f32_16x16x32_bf16 v[120:123], a[24:27], v[228:231], v[120:123]\n"
    "ds_read_b128 v[156:159], v31 offset:7168\n"
    "v_add_f32 v66, v66, v38\n"
    "v_add_f32 v67, v67, v39\n"
    "v_mfma_f32_16x16x32_bf16 v[120:123], a[20:23], a[244:247], v[120:123]\n"
    "ds_read_b64_tr_b16 a[56:57], v11 offset:32768\n"
    "v_add_f32 v112, v112, v128\n"
    "v_add_f32 v113, v113, v129\n"
    "v_mfma_f32_16x16x32_bf16 v[120:123], a[16:19], a[228:231], v[120:123]\n"
    "ds_read_b64_tr_b16 a[58:59], v11 offset:49152\n"
    "v_add_f32 v114, v114, v130\n"
    "v_add_f32 v115, v115, v131\n"
    "v_mfma_f32_16x16x32_bf16 v[76:79], a[32:35], a[224:227], v[76:79]\n"
    "ds_read_b64_tr_b16 a[60:61], v11 offset:36864\n"
    "v_add_f32 v68, v68, v60\n"
    "v_add_f32 v69, v69, v61\n"
    "v_mfma_f32_16x16x32_bf16 v[76:79], a[36:39], a[240:243], v[76:79]\n"
    "ds_read_b64_tr_b16 a[62:63], v11 offset:53248\n"
    "v_add_f32 v70, v70, v62\n"
    "v_add_f32 v71, v71, v63\n"
    "v_mfma_f32_16x16x32_bf16 v[76:79], a[40:43], v[224:227], v[76:79]\n"
    "s_waitcnt lgkmcnt(4)\n"
    "v_add_f32 v116, v116, v132\n"
    "v_add_f32 v117, v117, v133\n"
    "v_cvt_pk_bf16_f32 v64, v64, v65\n"
    "v_cvt_pk_bf16_f32 v65, v66, v67\n"
    "v_mfma_f32_16x16x32_bf16 v[124:127], a[40:43], v[228:231], v[124:127]\n"
    "v_add_f32 v118, v118, v134\n"
    "v_add_f32 v119, v119, v135\n"
    "v_cvt_pk_bf16_f32 v66, v68, v69\n"
    "v_cvt_pk_bf16_f32 v67, v70, v71\n"
    "v_mfma_f32_16x16x32_bf16 v[124:127], a[36:39], a[244:247], v[124:127]\n"
    "v_add_f32 v72, v72, v84\n"
    "v_add_f32 v73, v73, v85\n"
    "v_min_u32 v64, -2, v64\n"
    "v_min_u32 v65, -2, v65\n"
    "v_mfma_f32_16x16x32_bf16 v[124:127], a[32:35], a[228:231], v[124:127]\n"
    "v_add_f32 v74, v74, v86\n"
    "v_add_f32 v75, v75, v87\n"
    "v_min_u32 v66, -2, v66\n"
    "v_min_u32 v67, -2, v67\n"
    "v_mfma_f32_16x16x32_bf16 v[48:51], a[48:51], a[64:67], v[48:51]\n"
    "v_add_f32 v120, v120, v152\n"
    "v_add_f32 v121, v121, v153\n"
    "buffer_store_dwordx4 v[64:67], v16, s[44:47], s80 offen sc1\n"
    "v_mfma_f32_16x16x32_bf16 v[48:51], a[52:55], a[80:83], v[48:51]\n"
    "v_add_f32 v122, v122, v154\n"
    "v_add_f32 v123, v123, v155\n"
    "v_cvt_pk_bf16_f32 v112, v112, v113\n"
    "v_cvt_pk_bf16_f32 v113, v114, v115\n"
    "v_mfma_f32_16x16x32_bf16 v[48:51], a[208:211], a[104:107], v[48:51]\n"
    "ds_read_b64_tr_b16 a[0:1], v24 offset:0\n"
    "v_add_f32 v76, v76, v108\n"
    "v_add_f32 v77, v77, v109\n"
    "v_cvt_pk_bf16_f32 v114, v116, v117\n"
    "v_cvt_pk_bf16_f32 v115, v118, v119\n"
    "v_mfma_f32_16x16x32_bf16 v[96:99], a[52:55], a[84:87], v[96:99]\n"
    "ds_read_b64_tr_b16 a[2:3], v24 offset:16384\n"
    "v_add_f32 v78, v78, v110\n"
    "v_add_f32 v79, v79, v111\n"
    "v_min_u32 v112, -2, v112\n"
    "v_min_u32 v113, -2, v113\n"
    "v_mfma_f32_16x16x32_bf16 v[96:99], a[48:51], a[68:71], v[96:99]\n"
    "ds_read_b64_tr_b16 a[4:5], v24 offset:4096\n"
    "s_waitcnt vmcnt(1)\n"
    "s_barrier\n"
    "v_add_f32 v124, v124, v156\n"
    "v_add_f32 v125, v125, v157\n"
    "v_min_u32 v114, -2, v114\n"
    "v_min_u32 v115, -2, v115\n"
    "v_mfma_f32_16x16x32_bf16 v[96:99], a[208:211], a[120:123], v[96:99]\n"
    "ds_read_b64_tr_b16 a[6:7], v24 offset:20480\n"
    "v_add_f32 v126, v126, v158\n"
    "v_add_f32 v127, v127, v159\n"
    "buffer_store_dwordx4 v[112:115], v16, s[44:47], s80 offen offset:2048 sc1\n"
    "v_mfma_f32_16x16x32_bf16 v[144:147], a[48:51], a[72:75], v[144:147]\n"
    "ds_read_b64_tr_b16 a[8:9], v24 offset:32768\n"
    "v_cvt_pk_bf16_f32 v72, v72, v73\n"
    "v_cvt_pk_bf16_f32 v73, v74, v75\n"
    "v_mfma_f32_16x16x32_bf16 v[144:147], a[52:55], a[88:91], v[144:147]\n"
    "ds_read_b64_tr_b16 a[10:11], v24 offset:49152\n"
    "v_cvt_pk_bf16_f32 v74, v76, v77\n"
    "v_cvt_pk_bf16_f32 v75, v78, v79\n"
    "v_mfma_f32_16x16x32_bf16 v[192:195], a[52:55], a[92:95], v[192:195]\n"
    "ds_read_b64_tr_b16 a[16:17], v25 offset:0\n"
    "v_min_u32 v72, -2, v72\n"
    ".Lk2_m8470:\n"
    "s_cbranch_execz .Lk2_hret\n"
    "v_min_u32 v73, -2, v73\n"
    "v_mfma_f32_16x16x32_bf16 v[192:195], a[48:51], a[76:79], v[192:195]\n"
    "ds_read_b64_tr_b16 a[18:19], v25 offset:16384\n"
    "v_min_u32 v74, -2, v74\n"
    "v_min_u32 v75, -2, v75\n"
    "s_waitcnt lgkmcnt(4)\n"
    "v_mfma_f32_16x16x32_bf16 v[52:55], a[56:59], a[64:67], v[52:55]\n"
    "ds_read_b64_tr_b16 a[20:21], v25 offset:4096\n"
    "buffer_store_dwordx4 v[72:75], v16, s[44:47], s80 offen offset:1024 sc1\n"
    "v_mfma_f32_16x16x32_bf16 v[52:55], a[60:63], a[80:83], v[52:55]\n"
    "ds_read_b64_tr_b16 a[22:23], v25 offset:20480\n"
    "v_cvt_pk_bf16_f32 v120, v120, v121\n"
    "v_cvt_pk_bf16_f32 v121, v122, v123\n"
    "v_mfma_f32_16x16x32_bf16 v[52:55], a[208:211], a[108:111], v[52:55]\n"
    "ds_read_b64_tr_b16 a[24:25], v25 offset:32768\n"
    "v_cvt_pk_bf16_f32 v122, v124, v125\n"
    "v_cvt_pk_bf16_f32 v123, v126, v127\n"
    "v_mfma_f32_16x16x32_bf16 v[100:103], a[60:63], a[84:87], v[100:103]\n"
    "ds_read_b64_tr_b16 a[26:27], v25 offset:49152\n"
    "v_min_u32 v120, -2, v120\n"
    "v_min_u32 v121, -2, v121\n"
    "v_mfma_f32_16x16x32_bf16 v[100:103], a[56:59], a[68:71], v[100:103]\n"
    "ds_read_b64_tr_b16 a[32:33], v26 offset:0\n"
    "v_min_u32 v122, -2, v122\n"
    "v_min_u32 v123, -2, v123\n"
    "v_mfma_f32_16x16x32_bf16 v[100:103], a[208:211], a[124:127], v[100:103]\n"
    "ds_read_b64_tr_b16 a[34:35], v26 offset:16384\n"
    "buffer_store_dwordx4 v[120:123], v16, s[44:47], s80 offen offset:3072 sc1\n"
    "v_mfma_f32_16x16x32_bf16 v[148:151], a[56:59], a[72:75], v[148:151]\n"
    "ds_read_b64_tr_b16 a[36:37], v26 offset:4096\n"
    "v_mfma_f32_16x16x32_bf16 v[148:151], a[60:63], a[88:91], v[148:151]\n"
    "ds_read_b64_tr_b16 a[38:39], v26 offset:20480\n"
    "v_mfma_f32_16x16x32_bf16 v[196:199], a[60:63], a[92:95], v[196:199]\n"
    "ds_read_b64_tr_b16 a[40:41], v26 offset:32768\n"
    "v_mfma_f32_16x16x32_bf16 v[196:199], a[56:59], a[76:79], v[196:199]\n"
    "ds_read_b64_tr_b16 a[42:43], v26 offset:49152\n"
    "v_mfma_f32_16x16x32_bf16 v[136:139], a[0:3], a[232:235], v[136:139]\n"
    "ds_read_b64_tr_b16 a[48:49], v27 offset:0\n"
    "v_mfma_f32_16x16x32_bf16 v[136:139], a[4:7], a[248:251], v[136:139]\n"
    "ds_read_b64_tr_b16 a[50:51], v27 offset:16384\n"
    "s_waitcnt lgkmcnt(4)\n"
    "v_mfma_f32_16x16x32_bf16 v[136:139], a[8:11], v[232:235], v[136:139]\n"
    "ds_read_b64_tr_b16 a[52:53], v27 offset:4096\n"
    "v_mfma_f32_16x16x32_bf16 v[184:187], a[8:11], v[236:239], v[184:187]\n"
    "ds_read_b64_tr_b16 a[54:55], v27 offset:20480\n"
    "v_mfma_f32_16x16x32_bf16 v[184:187], a[4:7], a[252:255], v[184:187]\n"
    "ds_read_b64_tr_b16 a[56:57], v27 offset:32768\n"
    "s_barrier\n"
    "v_mfma_f32_16x16x32_bf16 v[184:187], a[0:3], a[236:239], v[184:187]\n"
    "ds_read_b64_tr_b16 a[58:59], v27 offset:49152\n"
    "v_mfma_f32_16x16x32_bf16 v[140:143], a[16:19], a[232:235], v[140:143]\n"
    "v_mfma_f32_16x16x32_bf16 v[140:143], a[20:23], a[248:251], v[140:143]\n"
    "v_mfma_f32_16x16x32_bf16 v[140:143], a[24:27], v[232:235], v[140:143]\n"
    "ds_write_b128 v29, v[136:139] offset:0\n"
    "v_mfma_f32_16x16x32_bf16 v[188:191], a[24:27], v[236:239], v[188:191]\n"
    "v_mfma_f32_16x16x32_bf16 v[188:191], a[20:23], a[252:255], v[188:191]\n"
    "v_mfma_f32_16x16x32_bf16 v[188:191], a[16:19], a[236:239], v[188:191]\n"
    "ds_write_b128 v29, v[184:187] offset:4096\n"
    "v_mfma_f32_16x16x32_bf16 v[144:147], a[32:35], a[232:235], v[144:147]\n"
    "v_mfma_f32_16x16x32_bf16 v[144:147], a[36:39], a[248:251], v[144:147]\n"
    "s_waitcnt lgkmcnt(4)\n"
    "v_mfma_f32_16x16x32_bf16 v[144:147], a[40:43], v[232:235], v[144:147]\n"
    "ds_write_b128 v29, v[140:143] offset:1024\n"
    "v_mfma_f32_16x16x32_bf16 v[192:195], a[40:43], v[236:239], v[192:195]\n"
    "v_mfma_f32_16x16x32_bf16 v[192:195], a[36:39], a[252:255], v[192:195]\n"
    "v_accvgpr_read_b32 v23, a170\n"
    "v_mfma_f32_16x16x32_bf16 v[192:195], a[32:35], a[236:239], v[192:195]\n"
    "ds_write_b128 v29, v[188:191] offset:5120\n"
    "v_accvgpr_read_b32 v20, a196\n"
    "v_mfma_f32_16x16x32_bf16 v[148:151], a[48:51], a[232:235], v[148:151]\n"
    "v_accvgpr_read_b32 v18, a197\n"
    "v_mfma_f32_16x16x32_bf16 v[148:151], a[52:55], a[248:251], v[148:151]\n"
    "s_waitcnt lgkmcnt(4)\n"
    "v_mfma_f32_16x16x32_bf16 v[148:151], a[56:59], v[232:235], v[148:151]\n"
    "ds_write_b128 v29, v[144:147] offset:2048\n"
    "v_mfma_f32_16x16x32_bf16 v[196:199], a[56:59], v[236:239], v[196:199]\n"
    "v_mfma_f32_16x16x32_bf16 v[196:199], a[52:55], a[252:255], v[196:199]\n"
    "v_mfma_f32_16x16x32_bf16 v[196:199], a[48:51], a[236:239], v[196:199]\n"
    "ds_write_b128 v29, v[192:195] offset:6144\n"
    "v_mfma_f32_16x16x32_bf16 v[40:43], a[0:3], a[224:227], v[40:43]\n"
    "v_mfma_f32_16x16x32_bf16 v[40:43], a[4:7], a[240:243], v[40:43]\n"
    "buffer_load_dwordx4 v[32:35], v16, s[44:47], s81 offen sc1\n"
    "v_mfma_f32_16x16x32_bf16 v[40:43], a[8:11], v[224:227], v[40:43]\n"
    "ds_write_b128 v29, v[148:151] offset:3072\n"
    "v_mfma_f32_16x16x32_bf16 v[88:91], a[8:11], v[228:231], v[88:91]\n"
    "buffer_load_dwordx4 v[56:59], v16, s[44:47], s81 offen offset:1024 sc1\n"
    "v_mfma_f32_16x16x32_bf16 v[88:91], a[4:7], a[244:247], v[88:91]\n"
    "v_mfma_f32_16x16x32_bf16 v[88:91], a[0:3], a[228:231], v[88:91]\n"
    "ds_write_b128 v29, v[196:199] offset:7168\n"
    "buffer_load_dwordx4 v[80:83], v16, s[44:47], s81 offen offset:2048 sc1\n"
    "v_mfma_f32_16x16x32_bf16 v[44:47], a[16:19], a[224:227], v[44:47]\n"
    "v_mfma_f32_16x16x32_bf16 v[44:47], a[20:23], a[240:243], v[44:47]\n"
    "s_waitcnt lgkmcnt(0)\n"
    "s_barrier\n"
    "buffer_load_dwordx4 v[104:107], v16, s[44:47], s81 offen offset:3072 sc1\n"
    "v_mfma_f32_16x16x32_bf16 v[44:47], a[24:27], v[224:227], v[44:47]\n"
    "ds_read_b128 v[36:39], v31 offset:0\n"
    "v_mfma_f32_16x16x32_bf16 v[92:95], a[24:27], v[228:231], v[92:95]\n"
    "ds_read_b128 v[128:131], v31 offset:4096\n"
    "v_mfma_f32_16x16x32_bf16 v[92:95], a[20:23], a[244:247], v[92:95]\n"
    "ds_read_b128 v[60:63], v31 offset:1024\n"
    "v_mfma_f32_16x16x32_bf16 v[92:95], a[16:19], a[228:231], v[92:95]\n"
    "ds_read_b128 v[132:135], v31 offset:5120\n"
    "v_mfma_f32_16x16x32_bf16 v[48:51], a[32:35], a[224:227], v[48:51]\n"
    "ds_read_b128 v[84:87], v31 offset:2048\n"
    "v_mfma_f32_16x16x32_bf16 v[48:51], a[36:39], a[240:243], v[48:51]\n"
    "ds_read_b128 v[152:155], v31 offset:6144\n"
    "v_mfma_f32_16x16x32_bf16 v[48:51], a[40:43], v[224:227], v[48:51]\n"
    "ds_read_b128 v[108:111], v31 offset:3072\n"
    "s_waitcnt lgkmcnt(4)\n"
    "v_add_f32 v40, v40, v36\n"
    "v_add_f32 v41, v41, v37\n"
    "v_mfma_f32_16x16x32_bf16 v[96:99], a[40:43], v[228:231], v[96:99]\n"
    "ds_read_b128 v[156:159], v31 offset:7168\n"
    "v_add_f32 v42, v42, v38\n"
    "v_add_f32 v43, v43, v39\n"
    "v_mfma_f32_16x16x32_bf16 v[96:99], a[36:39], a[244:247], v[96:99]\n"
    "v_add_f32 v88, v88, v128\n"
    "v_add_f32 v89, v89, v129\n"
    "v_mfma_f32_16x16x32_bf16 v[96:99], a[32:35], a[228:231], v[96:99]\n"
    "v_add_f32 v90, v90, v130\n"
    "v_add_f32 v91, v91, v131\n"
    "v_mfma_f32_16x16x32_bf16 v[52:55], a[48:51], a[224:227], v[52:55]\n"
    "v_add_f32 v44, v44, v60\n"
    "v_add_f32 v45, v45, v61\n"
    "v_mfma_f32_16x16x32_bf16 v[52:55], a[52:55], a[240:243], v[52:55]\n"
    "v_add_f32 v46, v46, v62\n"
    "v_add_f32 v47, v47, v63\n"
    "v_mfma_f32_16x16x32_bf16 v[52:55], a[56:59], v[224:227], v[52:55]\n"
    "s_waitcnt lgkmcnt(4)\n"
    "v_add_f32 v92, v92, v132\n"
    "v_add_f32 v93, v93, v133\n"
    "v_mfma_f32_16x16x32_bf16 v[100:103], a[56:59], v[228:231], v[100:103]\n"
    "v_add_f32 v94, v94, v134\n"
    "v_add_f32 v95, v95, v135\n"
    "v_mfma_f32_16x16x32_bf16 v[100:103], a[52:55], a[244:247], v[100:103]\n"
    "s_waitcnt lgkmcnt(3)\n"
    "v_add_f32 v48, v48, v84\n"
    "v_add_f32 v49, v49, v85\n"
    "v_mfma_f32_16x16x32_bf16 v[100:103], a[48:51], a[228:231], v[100:103]\n"
    "v_add_f32 v50, v50, v86\n"
    "v_add_f32 v51, v51, v87\n"
    "s_waitcnt lgkmcnt(2)\n"
    "v_pk_add_f32 v[96:97], v[96:97], v[152:153]\n"
    "v_pk_add_f32 v[98:99], v[98:99], v[154:155]\n"
    "s_waitcnt lgkmcnt(1)\n"
    "v_pk_add_f32 v[52:53], v[52:53], v[108:109]\n"
    "v_pk_add_f32 v[54:55], v[54:55], v[110:111]\n"
    "s_waitcnt lgkmcnt(0)\n"
    "v_pk_add_f32 v[100:101], v[100:101], v[156:157]\n"
    "v_pk_add_f32 v[102:103], v[102:103], v[158:159]\n"
    ".Lk2_zend:\n"
    ".Lk2_m8623:\n"
    "s_cbranch_execz .Lk2_hret\n"
    ".Lk2_tail:\n"
    "s_waitcnt vmcnt(0)\n"
    "v_max3_u32 v0, v32, v33, v34\n"
    "v_max3_u32 v0, v0, v35, v56\n"
    "v_max3_u32 v0, v0, v57, v58\n"
    "v_max3_u32 v0, v0, v59, v80\n"
    "v_max3_u32 v0, v0, v81, v82\n"
    "v_max3_u32 v0, v0, v83, v104\n"
    "v_max3_u32 v0, v0, v105, v106\n"
    "v_max_u32 v0, v0, v107\n"
    "v_cmp_eq_u32 vcc, -1, v0\n"
    "s_nop 0\n"
    "s_cmp_eq_u64 vcc, 0\n"
    "s_cbranch_scc0 .Lk2_xslow\n"
    ".Lk2_xok:\n"
    "v_mfma_f32_16x16x32_bf16 v[40:43], a[200:203], v[32:35], v[40:43]\n"
    "v_mfma_f32_16x16x32_bf16 v[44:47], a[204:207], v[32:35], v[44:47]\n"
    "v_mfma_f32_16x16x32_bf16 v[48:51], a[200:203], v[56:59], v[48:51]\n"
    "v_mfma_f32_16x16x32_bf16 v[52:55], a[204:207], v[56:59], v[52:55]\n"
    "v_mfma_f32_16x16x32_bf16 v[88:91], a[200:203], v[80:83], v[88:91]\n"
    "s_nop 3\n"
    "v_cvt_pk_bf16_f32 v40, v40, v41\n"
    "v_cvt_pk_bf16_f32 v41, v42, v43\n"
    "v_mfma_f32_16x16x32_bf16 v[92:95], a[204:207], v[80:83], v[92:95]\n"
    "v_cvt_pk_bf16_f32 v42, v44, v45\n"
    "v_cvt_pk_bf16_f32 v43, v46, v47\n"
    "ds_write2_b64 v20, v[40:41], v[42:43] offset0:0 offset1:4\n"
    "v_mfma_f32_16x16x32_bf16 v[96:99], a[200:203], v[104:107], v[96:99]\n"
    "v_cvt_pk_bf16_f32 v48, v48, v49\n"
    "v_cvt_pk_bf16_f32 v49, v50, v51\n"
    "v_mfma_f32_16x16x32_bf16 v[100:103], a[204:207], v[104:107], v[100:103]\n"
    "v_cvt_pk_bf16_f32 v50, v52, v53\n"
    "v_cvt_pk_bf16_f32 v51, v54, v55\n"
    "ds_write2_b64 v20, v[48:49], v[50:51] offset0:8 offset1:12\n"
    "v_add_u32 v20, 2304, v20\n"
    "v_cvt_pk_bf16_f32 v88, v88, v89\n"
    "v_cvt_pk_bf16_f32 v89, v90, v91\n"
    "v_cvt_pk_bf16_f32 v90, v92, v93\n"
    "v_cvt_pk_bf16_f32 v91, v94, v95\n"
    "ds_write2_b64 v20, v[88:89], v[90:91] offset0:0 offset1:4\n"
    "s_waitcnt lgkmcnt(1)\n"
    "ds_read_b128 v[224:227], v18 offset:0\n"
    "ds_read_b128 v[228:231], v18 offset:1152\n"
    "v_cvt_pk_bf16_f32 v96, v96, v97\n"
    "v_cvt_pk_bf16_f32 v97, v98, v99\n"
    "v_cvt_pk_bf16_f32 v98, v100, v101\n"
    "v_cvt_pk_bf16_f32 v99, v102, v103\n"
    "ds_write2_b64 v20, v[96:97], v[98:99] offset0:8 offset1:12\n"
    "s_waitcnt lgkmcnt(0)\n"
    "ds_read_b128 v[232:235], v18 offset:2304\n"
    "ds_read_b128 v[236:239], v18 offset:3456\n"
    "global_store_dwordx4 v23, v[224:227], s[70:71] sc1\n"
    "v_add_u32 v23, 0x4000, v23\n"
    "global_store_dwordx4 v23, v[228:231], s[70:71] sc1\n"
    "v_add_u32 v23, 0xc000, v23\n"
    "s_waitcnt lgkmcnt(1)\n"
    "global_store_dwordx4 v23, v[232:235], s[70:71] sc1\n"
    "v_add_u32 v23, 0x4000, v23\n"
    "s_waitcnt lgkmcnt(0)\n"
    "global_store_dwordx4 v23, v[236:239], s[70:71] sc1\n"
    "s_cmp_lg_u32 s35, 0\n"
    "s_cbranch_scc1 .Lk2_end\n"
    "s_add_u32 s93, s92, 32\n"
    "v_mov_b32 v0, s93\n"
    "s_mov_b32 s93, 0x2000000\n"
    "s_mov_b64 exec, 1\n"
    "buffer_store_dword v0, off, s[44:47], s93 sc1\n"
    "s_mov_b64 exec, -1\n"
    ".Lk2_end:\n"
    "s_endpgm\n"
    ".Lk2_wpub:\n"
    ".Lk2_m8695:\n"
    "s_cbranch_execz .Lk2_m7874\n"
    "s_waitcnt lgkmcnt(6)\n"
    "v_mfma_f32_16x16x32_bf16 v[32:35], a[0:3], a[64:67], v[32:35]\n"
    "s_add_u32 m0, s52, 36864\n"
    "ds_read_b64_tr_b16 a[40:41], v15 offset:2048\n"
    "v_mfma_f32_16x16x32_bf16 v[32:35], a[4:7], a[68:71], v[32:35]\n"
    "buffer_load_dwordx4 v16, s[36:39], s84 offen lds\n"
    "v_mfma_f32_16x16x32_bf16 v[56:59], a[4:7], a[76:79], v[56:59]\n"
    "ds_read_b64_tr_b16 a[42:43], v15 offset:2560\n"
    "v_accvgpr_read_b32 v28, a178\n"
    "v_mfma_f32_16x16x32_bf16 v[56:59], a[0:3], a[72:75], v[56:59]\n"
    "ds_read_b64_tr_b16 a[44:45], v15 offset:14336\n"
    "s_mov_b64 s[28:29], s[48:49]\n"
    "v_mfma_f32_16x16x32_bf16 v[60:63], a[8:11], a[72:75], v[60:63]\n"
    "ds_read_b64_tr_b16 a[46:47], v15 offset:14848\n"
    "s_mov_b64 s[48:49], s[58:59]\n"
    "v_mfma_f32_16x16x32_bf16 v[60:63], a[12:15], a[76:79], v[60:63]\n"
    "buffer_load_dwordx4 v17, s[36:39], s84 offen offset:1024 lds\n"
    "v_mfma_f32_16x16x32_bf16 v[36:39], a[12:15], a[68:71], v[36:39]\n"
    "ds_read_b64_tr_b16 a[48:49], v12 offset:0\n"
    "s_mov_b32 s34, 0\n"
    "v_mfma_f32_16x16x32_bf16 v[36:39], a[8:11], a[64:67], v[36:39]\n"
    "ds_read_b64_tr_b16 a[50:51], v12 offset:256\n"
    "v_accvgpr_read_b32 v7, a168\n"
    "v_mfma_f32_16x16x32_bf16 v[40:43], a[16:19], a[64:67], v[40:43]\n"
    "ds_read_b64_tr_b16 a[52:53], v12 offset:512\n"
    "v_accvgpr_read_b32 v6, a222\n"
    "v_mfma_f32_16x16x32_bf16 v[40:43], a[20:23], a[68:71], v[40:43]\n"
    "s_getpc_b64 s[0:1]\n"
    ".Lk2_wpc:\n"
    "s_sub_u32 s0, s0, .Lk2_wpc-.Lk2_start\n"
    "s_subb_u32 s1, s1, 0\n"
    "v_mfma_f32_16x16x32_bf16 v[64:67], a[20:23], a[76:79], v[64:67]\n"
    "buffer_load_dwordx4 v18, s[36:39], s84 offen offset:2048 lds\n"
    "v_mfma_f32_16x16x32_bf16 v[64:67], a[16:19], a[72:75], v[64:67]\n"
    "ds_read_b64_tr_b16 a[54:55], v12 offset:768\n"
    "global_load_dword a220, v6, s[0:1]\n"
    "v_mfma_f32_16x16x32_bf16 v[68:71], a[24:27], a[72:75], v[68:71]\n"
    "ds_read_b64_tr_b16 a[56:57], v13 offset:0\n"
    "s_lshr_b32 s94, s52, 1\n"
    "s_waitcnt lgkmcnt(6)\n"
    "v_mfma_f32_16x16x32_bf16 v[68:71], a[28:31], a[76:79], v[68:71]\n"
    "ds_read_b64_tr_b16 a[58:59], v13 offset:256\n"
    "v_mfma_f32_16x16x32_bf16 v[44:47], a[28:31], a[68:71], v[44:47]\n"
    "ds_read_b64_tr_b16 a[60:61], v13 offset:512\n"
    "s_mov_b32 s33, m0\n"
    "s_add_u32 m0, s94, 137216\n"
    "s_nop 0\n"
    "global_load_lds_dwordx4 v7, s[68:69]\n"
    "s_mov_b32 m0, s33\n"
    "v_mfma_f32_16x16x32_bf16 v[44:47], a[24:27], a[64:67], v[44:47]\n"
    "buffer_load_dwordx4 v19, s[36:39], s84 offen offset:3072 lds\n"
    "v_mfma_f32_16x16x32_bf16 v[48:51], a[32:35], a[64:67], v[48:51]\n"
    "ds_read_b64_tr_b16 a[62:63], v13 offset:768\n"
    "v_add_u32 v7, 0x4000, v7\n"
    "v_mfma_f32_16x16x32_bf16 v[48:51], a[36:39], a[68:71], v[48:51]\n"
    "ds_read_b128 a[80:83], v8 offset:8192\n"
    "v_xor_b32 v7, 64, v7\n"
    "v_mfma_f32_16x16x32_bf16 v[72:75], a[36:39], a[76:79], v[72:75]\n"
    "s_add_u32 s84, s84, 128\n"
    "s_add_u32 m0, s52, 40960\n"
    "s_mov_b32 s33, m0\n"
    "s_add_u32 m0, s94, 138240\n"
    "s_nop 0\n"
    "global_load_lds_dwordx4 v7, s[68:69]\n"
    "s_mov_b32 m0, s33\n"
    "v_mfma_f32_16x16x32_bf16 v[72:75], a[32:35], a[72:75], v[72:75]\n"
    "buffer_load_dwordx4 v16, s[36:39], s85 offen lds\n"
    "v_mfma_f32_16x16x32_bf16 v[76:79], a[40:43], a[72:75], v[76:79]\n"
    "ds_read_b128 a[84:87], v10 offset:8192\n"
    "v_add_u32 v7, 0xc000, v7\n"
    "s_waitcnt lgkmcnt(6)\n"
    "v_mfma_f32_16x16x32_bf16 v[76:79], a[44:47], a[76:79], v[76:79]\n"
    "ds_read_b128 a[88:91], v8 offset:12288\n"
    "v_xor_b32 v7, 64, v7\n"
    "v_mfma_f32_16x16x32_bf16 v[52:55], a[44:47], a[68:71], v[52:55]\n"
    "ds_read_b128 a[92:95], v10 offset:12288\n"
    "v_mfma_f32_16x16x32_bf16 v[52:55], a[40:43], a[64:67], v[52:55]\n"
    "ds_read_b128 a[96:99], v9 offset:0\n"
    "v_mfma_f32_16x16x32_bf16 v[224:227], a[48:51], a[64:67], v[224:227]\n"
    "buffer_load_dwordx4 v17, s[36:39], s85 offen offset:1024 lds\n"
    "v_mfma_f32_16x16x32_bf16 v[224:227], a[52:55], a[68:71], v[224:227]\n"
    "ds_read_b128 a[100:103], v11 offset:0\n"
    "v_mfma_f32_16x16x32_bf16 v[232:235], a[52:55], a[76:79], v[232:235]\n"
    "s_mov_b32 s33, m0\n"
    "s_add_u32 m0, s94, 139264\n"
    "s_nop 0\n"
    "global_load_lds_dwordx4 v7, s[68:69]\n"
    "s_mov_b32 m0, s33\n"
    "v_mfma_f32_16x16x32_bf16 v[232:235], a[48:51], a[72:75], v[232:235]\n"
    "ds_read_b128 a[104:107], v9 offset:4096\n"
    "v_add_u32 v7, 0x4000, v7\n"
    "s_waitcnt lgkmcnt(6)\n"
    "v_mfma_f32_16x16x32_bf16 v[236:239], a[56:59], a[72:75], v[236:239]\n"
    "v_xor_b32 v7, 64, v7\n"
    "v_mfma_f32_16x16x32_bf16 v[236:239], a[60:63], a[76:79], v[236:239]\n"
    "buffer_load_dwordx4 v18, s[36:39], s85 offen offset:2048 lds\n"
    "v_mfma_f32_16x16x32_bf16 v[228:231], a[60:63], a[68:71], v[228:231]\n"
    "ds_read_b128 a[108:111], v11 offset:4096\n"
    "v_mfma_f32_16x16x32_bf16 v[228:231], a[56:59], a[64:67], v[228:231]\n"
    "s_mov_b32 s33, m0\n"
    "s_add_u32 m0, s94, 140288\n"
    "s_nop 0\n"
    "global_load_lds_dwordx4 v7, s[68:69]\n"
    "s_mov_b32 m0, s33\n"
    "v_mfma_f32_16x16x32_bf16 v[244:247], a[56:59], a[80:83], v[244:247]\n"
    "ds_read_b128 a[112:115], v9 offset:8192\n"
    "v_readfirstlane_b32 s92, v27\n"
    "s_waitcnt lgkmcnt(6)\n"
    "v_mfma_f32_16x16x32_bf16 v[244:247], a[60:63], a[84:87], v[244:247]\n"
    "buffer_load_dwordx4 v19, s[36:39], s85 offen offset:3072 lds\n"
    "s_waitcnt lgkmcnt(5)\n"
    "v_mfma_f32_16x16x32_bf16 v[252:255], a[60:63], a[92:95], v[252:255]\n"
    "ds_read_b128 a[116:119], v11 offset:8192\n"
    "v_accvgpr_read_b32 v0, a128\n"
    "v_mfma_f32_16x16x32_bf16 v[252:255], a[56:59], a[88:91], v[252:255]\n"
    "v_accvgpr_read_b32 v1, a129\n"
    "v_accvgpr_read_b32 v2, a130\n"
    "v_mfma_f32_16x16x32_bf16 v[248:251], a[48:51], a[88:91], v[248:251]\n"
    "v_accvgpr_read_b32 v3, a131\n"
    "v_mfma_f32_16x16x32_bf16 v[248:251], a[52:55], a[92:95], v[248:251]\n"
    "s_add_u32 s85, s85, 128\n"
    "s_add_u32 m0, s54, 36864\n"
    "v_accvgpr_read_b32 v4, a192\n"
    "v_mfma_f32_16x16x32_bf16 v[240:243], a[52:55], a[84:87], v[240:243]\n"
    "buffer_load_dwordx4 v23, s[40:43], s87 offen lds\n"
    "v_mfma_f32_16x16x32_bf16 v[240:243], a[48:51], a[80:83], v[240:243]\n"
    "s_add_u32 s87, s87, s55\n"
    "s_add_u32 s87, s87, s55\n"
    "v_add_f32_dpp v0, v0, v0 row_ror:8 row_mask:0xf bank_mask:0xf\n"
    "v_mfma_f32_16x16x32_bf16 v[100:103], a[40:43], a[80:83], v[100:103]\n"
    "ds_read_b128 a[120:123], v9 offset:12288\n"
    "v_add_f32_dpp v1, v1, v1 row_ror:8 row_mask:0xf bank_mask:0xf\n"
    "v_mfma_f32_16x16x32_bf16 v[100:103], a[44:47], a[84:87], v[100:103]\n"
    "ds_read_b128 a[124:127], v11 offset:12288\n"
    "v_mfma_f32_16x16x32_bf16 v[124:127], a[44:47], a[92:95], v[124:127]\n"
    "v_add_f32_dpp v2, v2, v2 row_ror:8 row_mask:0xf bank_mask:0xf\n"
    "v_mfma_f32_16x16x32_bf16 v[124:127], a[40:43], a[88:91], v[124:127]\n"
    "v_add_f32_dpp v3, v3, v3 row_ror:8 row_mask:0xf bank_mask:0xf\n"
    "v_mfma_f32_16x16x32_bf16 v[120:123], a[32:35], a[88:91], v[120:123]\n"
    "v_mfma_f32_16x16x32_bf16 v[120:123], a[36:39], a[92:95], v[120:123]\n"
    "s_mov_b32 vcc_lo, 0xff00ff00\n"
    "v_mfma_f32_16x16x32_bf16 v[96:99], a[36:39], a[84:87], v[96:99]\n"
    "s_mov_b32 vcc_hi, 0xff00ff00\n"
    "v_mfma_f32_16x16x32_bf16 v[96:99], a[32:35], a[80:83], v[96:99]\n"
    "s_waitcnt lgkmcnt(0)\n"
    "s_barrier\n"
    "v_mfma_f32_16x16x32_bf16 v[92:95], a[24:27], a[80:83], v[92:95]\n"
    "s_add_u32 m0, s53, 0\n"
    "v_cndmask_b32 v0, v0, v1, vcc\n"
    "v_mfma_f32_16x16x32_bf16 v[92:95], a[28:31], a[84:87], v[92:95]\n"
    "buffer_load_dwordx4 v20, s[40:43], s86 offen lds\n"
    "v_mfma_f32_16x16x32_bf16 v[116:119], a[28:31], a[92:95], v[116:119]\n"
    "v_cndmask_b32 v2, v2, v3, vcc\n"
    "v_mfma_f32_16x16x32_bf16 v[116:119], a[24:27], a[88:91], v[116:119]\n"
    "s_mov_b32 vcc_lo, 0\n"
    "v_mfma_f32_16x16x32_bf16 v[112:115], a[16:19], a[88:91], v[112:115]\n"
    "v_mfma_f32_16x16x32_bf16 v[112:115], a[20:23], a[92:95], v[112:115]\n"
    "buffer_load_dwordx4 v21, s[40:43], s86 offen offset:1024 lds\n"
    "v_mfma_f32_16x16x32_bf16 v[88:91], a[20:23], a[84:87], v[88:91]\n"
    "s_mov_b32 vcc_hi, -1\n"
    "v_cndmask_b32 v0, v0, v2, vcc\n"
    "v_mfma_f32_16x16x32_bf16 v[88:91], a[16:19], a[80:83], v[88:91]\n"
    "v_mfma_f32_16x16x32_bf16 v[84:87], a[8:11], a[80:83], v[84:87]\n"
    "s_add_u32 s93, s92, 1\n"
    "v_mfma_f32_16x16x32_bf16 v[84:87], a[12:15], a[84:87], v[84:87]\n"
    "buffer_load_dwordx4 v22, s[40:43], s86 offen offset:2048 lds\n"
    "v_mfma_f32_16x16x32_bf16 v[108:111], a[12:15], a[92:95], v[108:111]\n"
    "v_mov_b32 v1, s93\n"
    "v_mfma_f32_16x16x32_bf16 v[108:111], a[8:11], a[88:91], v[108:111]\n"
    "buffer_store_dwordx2 v[0:1], v4, s[44:47], s76 offen sc1\n"
    "v_mfma_f32_16x16x32_bf16 v[104:107], a[0:3], a[88:91], v[104:107]\n"
    "s_add_u32 s86, s86, s55\n"
    "s_add_u32 m0, s53, 12288\n"
    "v_mfma_f32_16x16x32_bf16 v[104:107], a[4:7], a[92:95], v[104:107]\n"
    "buffer_load_dwordx4 v20, s[40:43], s86 offen lds\n"
    "v_mfma_f32_16x16x32_bf16 v[80:83], a[4:7], a[84:87], v[80:83]\n"
    "v_mfma_f32_16x16x32_bf16 v[80:83], a[0:3], a[80:83], v[80:83]\n"
    "s_branch .Lk2_wback\n"
    ".Lk2_wslow:\n"
    "s_add_u32 s98, s98, 1\n"
    "s_cmp_gt_u32 s98, 1000000\n"
    "s_cbranch_scc1 .Lk2_wok\n"
    "s_sleep 2\n"
    "s_add_u32 s93, s77, 4096\n"
    "buffer_load_dwordx2 v[0:1], v26, s[44:47], s77 offen sc1\n"
    "buffer_load_dwordx2 v[2:3], v26, s[44:47], s77 offen offset:2048 sc1\n"
    "s_add_u32 s94, s77, 8192\n"
    "buffer_load_dwordx2 v[4:5], v26, s[44:47], s93 offen sc1\n"
    "buffer_load_dwordx2 v[6:7], v26, s[44:47], s93 offen offset:2048 sc1\n"
    "s_add_u32 s93, s77, 12288\n"
    "buffer_load_dwordx2 v[16:17], v26, s[44:47], s94 offen sc1\n"
    "buffer_load_dwordx2 v[18:19], v26, s[44:47], s94 offen offset:2048 sc1\n"
    "buffer_load_dwordx2 v[28:29], v26, s[44:47], s93 offen sc1\n"
    "buffer_load_dwordx2 v[30:31], v26, s[44:47], s93 offen offset:2048 sc1\n"
    "buffer_load_dwordx4 v[20:23], v25, s[44:47], s79 offen sc1\n"
    "s_waitcnt vmcnt(0)\n"
    "s_add_u32 s93, s92, 1\n"
    "v_xor_b32 v21, s93, v21\n"
    "v_xor_b32 v23, s93, v23\n"
    "v_xor_b32 v1, s93, v1\n"
    "v_xor_b32 v3, s93, v3\n"
    "v_xor_b32 v5, s93, v5\n"
    "v_xor_b32 v7, s93, v7\n"
    "v_xor_b32 v17, s93, v17\n"
    "v_xor_b32 v19, s93, v19\n"
    "v_xor_b32 v29, s93, v29\n"
    "v_xor_b32 v31, s93, v31\n"
    "v_or3_b32 v21, v21, v23, v1\n"
    "v_or3_b32 v3, v3, v5, v7\n"
    "v_or3_b32 v17, v17, v19, v29\n"
    "v_or3_b32 v21, v21, v3, v17\n"
    "v_or_b32 v21, v21, v31\n"
    "v_cmp_ne_u32 vcc, 0, v21\n"
    "s_nop 0\n"
    "s_cmp_eq_u64 vcc, 0\n"
    "s_cbranch_scc0 .Lk2_wslow\n"
    "s_branch .Lk2_wok\n"
    ".Lk2_aslow:\n"
    "s_mov_b32 exec_hi, 0\n"
    "buffer_load_dword v143, v142, s[44:47], s82 offen sc1\n"
    "s_mov_b32 exec_hi, -1\n"
    "s_add_u32 s98, s98, 1\n"
    "s_waitcnt vmcnt(0)\n"
    "s_add_u32 s33, s92, 1\n"
    "v_subrev_u32 v164, s33, v143\n"
    "v_cmp_lt_u32 vcc, 2, v164\n"
    "s_nop 0\n"
    "s_cmp_eq_u32 vcc_lo, -1\n"
    "s_cselect_b32 s27, -1, 0\n"
    "v_cmp_gt_u32 vcc, 16, v164\n"
    "s_cmp_gt_u32 s98, 1000000\n"
    "s_cbranch_scc1 .Lk2_ago\n"
    "s_cmp_eq_u32 vcc_lo, -1\n"
    "s_cbranch_scc1 .Lk2_ago\n"
    "s_sleep 4\n"
    "s_branch .Lk2_aslow\n"
    ".Lk2_ago:\n"
    "v_readlane_b32 s91, v140, 0\n"
    "s_nop 0\n"
    "s_add_u32 s84, s91, s67\n"
    "v_readlane_b32 s91, v140, 1\n"
    "s_xor_b32 s85, s84, 2048\n"
    "buffer_load_dwordx4 a[224:227], v141, s[44:47], s84 offen sc1\n"
    "buffer_load_dwordx4 a[228:231], v141, s[44:47], s84 offen offset:1024 sc1\n"
    "buffer_load_dwordx4 a[232:235], v141, s[44:47], s85 offen sc1\n"
    "buffer_load_dwordx4 a[236:239], v141, s[44:47], s85 offen offset:1024 sc1\n"
    "s_add_u32 s84, s91, s67\n"
    "s_xor_b32 s85, s84, 2048\n"
    "buffer_load_dwordx4 a[240:243], v141, s[44:47], s84 offen sc1\n"
    "buffer_load_dwordx4 a[244:247], v141, s[44:47], s84 offen offset:1024 sc1\n"
    "buffer_load_dwordx4 a[248:251], v141, s[44:47], s85 offen sc1\n"
    "buffer_load_dwordx4 a[252:255], v141, s[44:47], s85 offen offset:1024 sc1\n"
    "s_waitcnt vmcnt(0)\n"
    "s_branch .Lk2_aok\n"
    ".Lk2_qpub:\n"
    ".Lk2_m8951:\n"
    "s_cbranch_execz .Lk2_m7320\n"
    "s_waitcnt lgkmcnt(6)\n"
    "v_mfma_f32_16x16x32_bf16 v[32:35], a[0:3], a[64:67], v[32:35]\n"
    "s_add_u32 m0, s52, 36864\n"
    "ds_read_b64_tr_b16 a[40:41], v13 offset:2048\n"
    "v_mfma_f32_16x16x32_bf16 v[32:35], a[4:7], a[68:71], v[32:35]\n"
    "buffer_load_dwordx4 v16, s[36:39], s84 offen lds\n"
    "v_mfma_f32_16x16x32_bf16 v[56:59], a[4:7], a[76:79], v[56:59]\n"
    "ds_read_b64_tr_b16 a[42:43], v13 offset:2560\n"
    "v_accvgpr_read_b32 v28, a178\n"
    "v_mfma_f32_16x16x32_bf16 v[56:59], a[0:3], a[72:75], v[56:59]\n"
    "ds_read_b64_tr_b16 a[44:45], v13 offset:14336\n"
    "s_mov_b64 s[28:29], s[48:49]\n"
    "v_mfma_f32_16x16x32_bf16 v[60:63], a[8:11], a[72:75], v[60:63]\n"
    "buffer_load_dwordx4 v17, s[36:39], s84 offen offset:1024 lds\n"
    "v_mfma_f32_16x16x32_bf16 v[60:63], a[12:15], a[76:79], v[60:63]\n"
    "ds_read_b64_tr_b16 a[46:47], v13 offset:14848\n"
    "s_mov_b64 s[48:49], s[58:59]\n"
    "v_mfma_f32_16x16x32_bf16 v[36:39], a[12:15], a[68:71], v[36:39]\n"
    "s_mov_b32 s34, 0\n"
    "v_accvgpr_read_b32 v7, a168\n"
    "v_accvgpr_read_b32 v6, a222\n"
    "v_mfma_f32_16x16x32_bf16 v[36:39], a[8:11], a[64:67], v[36:39]\n"
    "ds_read_b128 a[80:83], v8 offset:8192\n"
    "v_mfma_f32_16x16x32_bf16 v[40:43], a[16:19], a[64:67], v[40:43]\n"
    "buffer_load_dwordx4 v18, s[36:39], s84 offen offset:2048 lds\n"
    "v_mfma_f32_16x16x32_bf16 v[40:43], a[20:23], a[68:71], v[40:43]\n"
    "ds_read_b128 a[84:87], v10 offset:8192\n"
    "v_mfma_f32_16x16x32_bf16 v[64:67], a[20:23], a[76:79], v[64:67]\n"
    "s_getpc_b64 s[0:1]\n"
    ".Lk2_qpc:\n"
    "s_sub_u32 s0, s0, .Lk2_qpc-.Lk2_start\n"
    "s_subb_u32 s1, s1, 0\n"
    "v_mfma_f32_16x16x32_bf16 v[64:67], a[16:19], a[72:75], v[64:67]\n"
    "buffer_load_dwordx4 v19, s[36:39], s84 offen offset:3072 lds\n"
    "v_mfma_f32_16x16x32_bf16 v[68:71], a[24:27], a[72:75], v[68:71]\n"
    "ds_read_b128 a[88:91], v8 offset:12288\n"
    "global_load_dword a220, v6, s[0:1]\n"
    "s_waitcnt lgkmcnt(6)\n"
    "v_mfma_f32_16x16x32_bf16 v[68:71], a[28:31], a[76:79], v[68:71]\n"
    "ds_read_b128 a[92:95], v10 offset:12288\n"
    "s_lshr_b32 s94, s52, 1\n"
    "v_mfma_f32_16x16x32_bf16 v[44:47], a[28:31], a[68:71], v[44:47]\n"
    "s_add_u32 s84, s84, 128\n"
    "s_add_u32 m0, s52, 40960\n"
    "s_mov_b32 s33, m0\n"
    "s_add_u32 m0, s94, 137216\n"
    "s_nop 0\n"
    "global_load_lds_dwordx4 v7, s[68:69]\n"
    "s_mov_b32 m0, s33\n"
    "v_mfma_f32_16x16x32_bf16 v[44:47], a[24:27], a[64:67], v[44:47]\n"
    "buffer_load_dwordx4 v16, s[36:39], s85 offen lds\n"
    "v_mfma_f32_16x16x32_bf16 v[48:51], a[32:35], a[64:67], v[48:51]\n"
    "ds_read_b128 a[96:99], v9 offset:0\n"
    "v_add_u32 v7, 0x4000, v7\n"
    "v_mfma_f32_16x16x32_bf16 v[48:51], a[36:39], a[68:71], v[48:51]\n"
    "ds_read_b128 a[100:103], v11 offset:0\n"
    "v_xor_b32 v7, 64, v7\n"
    "v_mfma_f32_16x16x32_bf16 v[72:75], a[36:39], a[76:79], v[72:75]\n"
    "ds_read_b128 a[104:107], v9 offset:4096\n"
    "v_mfma_f32_16x16x32_bf16 v[72:75], a[32:35], a[72:75], v[72:75]\n"
    "buffer_load_dwordx4 v17, s[36:39], s85 offen offset:1024 lds\n"
    "s_waitcnt lgkmcnt(6)\n"
    "v_mfma_f32_16x16x32_bf16 v[76:79], a[40:43], a[72:75], v[76:79]\n"
    "ds_read_b128 a[108:111], v11 offset:4096\n"
    "v_mfma_f32_16x16x32_bf16 v[76:79], a[44:47], a[76:79], v[76:79]\n"
    "s_mov_b32 s33, m0\n"
    "s_add_u32 m0, s94, 138240\n"
    "s_nop 0\n"
    "global_load_lds_dwordx4 v7, s[68:69]\n"
    "s_mov_b32 m0, s33\n"
    "v_mfma_f32_16x16x32_bf16 v[52:55], a[44:47], a[68:71], v[52:55]\n"
    "buffer_load_dwordx4 v18, s[36:39], s85 offen offset:2048 lds\n"
    "v_mfma_f32_16x16x32_bf16 v[52:55], a[40:43], a[64:67], v[52:55]\n"
    "ds_read_b128 a[112:115], v9 offset:8192\n"
    "v_add_u32 v7, 0xc000, v7\n"
    "v_mfma_f32_16x16x32_bf16 v[100:103], a[40:43], a[80:83], v[100:103]\n"
    "v_xor_b32 v7, 64, v7\n"
    "s_waitcnt lgkmcnt(6)\n"
    "v_mfma_f32_16x16x32_bf16 v[100:103], a[44:47], a[84:87], v[100:103]\n"
    "ds_read_b128 a[116:119], v11 offset:8192\n"
    "s_waitcnt lgkmcnt(6)\n"
    "v_mfma_f32_16x16x32_bf16 v[124:127], a[44:47], a[92:95], v[124:127]\n"
    "buffer_load_dwordx4 v19, s[36:39], s85 offen offset:3072 lds\n"
    "v_mfma_f32_16x16x32_bf16 v[124:127], a[40:43], a[88:91], v[124:127]\n"
    "s_add_u32 s85, s85, 128\n"
    "ds_read_b128 a[120:123], v9 offset:12288\n"
    "v_mfma_f32_16x16x32_bf16 v[120:123], a[32:35], a[88:91], v[120:123]\n"
    "s_mov_b32 s33, m0\n"
    "s_add_u32 m0, s94, 139264\n"
    "s_nop 0\n"
    "global_load_lds_dwordx4 v7, s[68:69]\n"
    "s_mov_b32 m0, s33\n"
    "v_mfma_f32_16x16x32_bf16 v[120:123], a[36:39], a[92:95], v[120:123]\n"
    "ds_read_b128 a[124:127], v11 offset:12288\n"
    "v_add_u32 v7, 0x4000, v7\n"
    "v_mfma_f32_16x16x32_bf16 v[96:99], a[36:39], a[84:87], v[96:99]\n"
    "v_xor_b32 v7, 64, v7\n"
    "v_mfma_f32_16x16x32_bf16 v[96:99], a[32:35], a[80:83], v[96:99]\n"
    "s_mov_b32 s33, m0\n"
    "s_add_u32 m0, s94, 140288\n"
    "s_nop 0\n"
    "global_load_lds_dwordx4 v7, s[68:69]\n"
    "s_mov_b32 m0, s33\n"
    "v_mfma_f32_16x16x32_bf16 v[92:95], a[24:27], a[80:83], v[92:95]\n"
    "v_readfirstlane_b32 s92, v27\n"
    "v_accvgpr_read_b32 v0, a128\n"
    "v_mfma_f32_16x16x32_bf16 v[92:95], a[28:31], a[84:87], v[92:95]\n"
    "v_accvgpr_read_b32 v1, a129\n"
    "v_accvgpr_read_b32 v2, a130\n"
    "v_mfma_f32_16x16x32_bf16 v[116:119], a[28:31], a[92:95], v[116:119]\n"
    "v_accvgpr_read_b32 v3, a131\n"
    "v_accvgpr_read_b32 v4, a192\n"
    "v_mfma_f32_16x16x32_bf16 v[116:119], a[24:27], a[88:91], v[116:119]\n"
    "v_add_f32_dpp v0, v0, v0 row_ror:8 row_mask:0xf bank_mask:0xf\n"
    "v_add_f32_dpp v1, v1, v1 row_ror:8 row_mask:0xf bank_mask:0xf\n"
    "v_mfma_f32_16x16x32_bf16 v[112:115], a[16:19], a[88:91], v[112:115]\n"
    "v_add_f32_dpp v2, v2, v2 row_ror:8 row_mask:0xf bank_mask:0xf\n"
    "v_add_f32_dpp v3, v3, v3 row_ror:8 row_mask:0xf bank_mask:0xf\n"
    "s_mov_b32 vcc_lo, 0xff00ff00\n"
    "v_mfma_f32_16x16x32_bf16 v[112:115], a[20:23], a[92:95], v[112:115]\n"
    "s_waitcnt lgkmcnt(0)\n"
    "s_barrier\n"
    "s_mov_b32 vcc_hi, 0xff00ff00\n"
    "v_mfma_f32_16x16x32_bf16 v[88:91], a[20:23], a[84:87], v[88:91]\n"
    "s_add_u32 m0, s53, 0\n"
    "v_cndmask_b32 v0, v0, v1, vcc\n"
    "v_cndmask_b32 v2, v2, v3, vcc\n"
    "v_mfma_f32_16x16x32_bf16 v[88:91], a[16:19], a[80:83], v[88:91]\n"
    "buffer_load_dwordx4 v20, s[40:43], s86 offen lds\n"
    "v_mfma_f32_16x16x32_bf16 v[84:87], a[8:11], a[80:83], v[84:87]\n"
    ".Lk2_m9082:\n"
    "s_cbranch_execz .Lk2_m6825\n"
    "buffer_load_dwordx4 v21, s[40:43], s86 offen offset:1024 lds\n"
    "v_mfma_f32_16x16x32_bf16 v[84:87], a[12:15], a[84:87], v[84:87]\n"
    "s_mov_b32 vcc_lo, 0\n"
    "s_mov_b32 vcc_hi, -1\n"
    "v_cndmask_b32 v0, v0, v2, vcc\n"
    "v_mfma_f32_16x16x32_bf16 v[108:111], a[12:15], a[92:95], v[108:111]\n"
    "buffer_load_dwordx4 v22, s[40:43], s86 offen offset:2048 lds\n"
    "v_mfma_f32_16x16x32_bf16 v[108:111], a[8:11], a[88:91], v[108:111]\n"
    "s_add_u32 s86, s86, s55\n"
    "s_add_u32 m0, s53, 12288\n"
    "s_add_u32 s93, s92, 1\n"
    "v_mfma_f32_16x16x32_bf16 v[104:107], a[0:3], a[88:91], v[104:107]\n"
    "buffer_load_dwordx4 v20, s[40:43], s86 offen lds\n"
    "v_mfma_f32_16x16x32_bf16 v[104:107], a[4:7], a[92:95], v[104:107]\n"
    "v_mov_b32 v1, s93\n"
    "v_mfma_f32_16x16x32_bf16 v[80:83], a[4:7], a[84:87], v[80:83]\n"
    "buffer_load_dwordx4 v21, s[40:43], s86 offen offset:1024 lds\n"
    "v_mfma_f32_16x16x32_bf16 v[80:83], a[0:3], a[80:83], v[80:83]\n"
    "buffer_store_dwordx2 v[0:1], v4, s[44:47], s76 offen sc1\n"
    "v_add_f32 v224, v224, v225\n"
    "v_add_f32 v226, v226, v227\n"
    "v_accvgpr_read_b32 v5, a193\n"
    "v_add_f32_dpp v224, v224, v224 row_ror:8 row_mask:0xf bank_mask:0xf\n"
    "v_add_f32_dpp v226, v226, v226 row_ror:8 row_mask:0xf bank_mask:0xf\n"
    "s_nop 0\n"
    "v_add_f32_dpp v224, v224, v224 row_ror:4 row_mask:0xf bank_mask:0xf\n"
    "v_add_f32_dpp v226, v226, v226 row_ror:4 row_mask:0xf bank_mask:0xf\n"
    "s_nop 0\n"
    "v_add_f32_dpp v224, v224, v224 row_ror:2 row_mask:0xf bank_mask:0xf\n"
    "v_add_f32_dpp v226, v226, v226 row_ror:2 row_mask:0xf bank_mask:0xf\n"
    "s_nop 0\n"
    "v_add_f32_dpp v224, v224, v224 row_ror:1 row_mask:0xf bank_mask:0xf\n"
    "v_add_f32_dpp v226, v226, v226 row_ror:1 row_mask:0xf bank_mask:0xf\n"
    "v_mul_f32 v224, 0x3a800000, v224\n"
    "v_mul_f32 v226, 0x3a800000, v226\n"
    "v_mov_b32 v225, s93\n"
    "v_fma_f32 v226, -v224, v224, v226\n"
    "v_mov_b32 v227, s93\n"
    "v_max_f32 v226, 0, v226\n"
    "buffer_store_dwordx4 v[224:227], v5, s[44:47], s78 offen sc1\n"
    "s_branch .Lk2_qback\n"
    ".Lk2_qslow:\n"
    "s_add_u32 s98, s98, 1\n"
    "s_cmp_gt_u32 s98, 1000000\n"
    "s_cbranch_scc1 .Lk2_qok\n"
    "s_sleep 2\n"
    "s_add_u32 s93, s77, 4096\n"
    "buffer_load_dwordx2 v[0:1], v26, s[44:47], s77 offen sc1\n"
    "buffer_load_dwordx2 v[2:3], v26, s[44:47], s77 offen offset:2048 sc1\n"
    "s_add_u32 s94, s77, 8192\n"
    "buffer_load_dwordx2 v[4:5], v26, s[44:47], s93 offen sc1\n"
    "buffer_load_dwordx2 v[6:7], v26, s[44:47], s93 offen offset:2048 sc1\n"
    "s_add_u32 s93, s77, 12288\n"
    "buffer_load_dwordx2 v[16:17], v26, s[44:47], s94 offen sc1\n"
    "buffer_load_dwordx2 v[18:19], v26, s[44:47], s94 offen offset:2048 sc1\n"
    "buffer_load_dwordx2 v[28:29], v26, s[44:47], s93 offen sc1\n"
    "buffer_load_dwordx2 v[30:31], v26, s[44:47], s93 offen offset:2048 sc1\n"
    "buffer_load_dwordx4 v[20:23], v25, s[44:47], s79 offen sc1\n"
    "s_waitcnt vmcnt(0)\n"
    "s_add_u32 s93, s92, 1\n"
    "v_xor_b32 v21, s93, v21\n"
    "v_xor_b32 v23, s93, v23\n"
    "v_xor_b32 v1, s93, v1\n"
    "v_xor_b32 v3, s93, v3\n"
    "v_xor_b32 v5, s93, v5\n"
    "v_xor_b32 v7, s93, v7\n"
    "v_xor_b32 v17, s93, v17\n"
    "v_xor_b32 v19, s93, v19\n"
    "v_xor_b32 v29, s93, v29\n"
    "v_xor_b32 v31, s93, v31\n"
    "v_or3_b32 v21, v21, v23, v1\n"
    "v_or3_b32 v3, v3, v5, v7\n"
    "v_or3_b32 v17, v17, v19, v29\n"
    "v_or3_b32 v21, v21, v3, v17\n"
    "v_or_b32 v21, v21, v31\n"
    "v_cmp_ne_u32 vcc, 0, v21\n"
    "s_nop 0\n"
    "s_cmp_eq_u64 vcc, 0\n"
    "s_cbranch_scc0 .Lk2_qslow\n"
    "s_branch .Lk2_qok\n"
    ".Lk2_q0:\n"
    "v_mov_b32 v52, s33\n"
    "v_mov_b32 v53, s33\n"
    "v_mov_b32 v54, s33\n"
    "v_mov_b32 v55, s33\n"
    "v_mov_b32 v76, s33\n"
    "v_mov_b32 v77, s33\n"
    "v_mov_b32 v78, s33\n"
    "v_mov_b32 v79, s33\n"
    "v_mov_b32 v100, s33\n"
    "v_mov_b32 v101, s33\n"
    "v_mov_b32 v102, s33\n"
    "v_mov_b32 v103, s33\n"
    "v_mov_b32 v124, s33\n"
    "v_mov_b32 v125, s33\n"
    "v_mov_b32 v126, s33\n"
    "v_mov_b32 v127, s33\n"
    "v_mov_b32 v148, s33\n"
    "v_mov_b32 v149, s33\n"
    "v_mov_b32 v150, s33\n"
    "v_mov_b32 v151, s33\n"
    "v_mov_b32 v172, s33\n"
    "v_mov_b32 v173, s33\n"
    "v_mov_b32 v174, s33\n"
    "v_mov_b32 v175, s33\n"
    "v_mov_b32 v196, s33\n"
    "v_mov_b32 v197, s33\n"
    "v_mov_b32 v198, s33\n"
    "v_mov_b32 v199, s33\n"
    "v_mov_b32 v220, s33\n"
    "v_mov_b32 v221, s33\n"
    "v_mov_b32 v222, s33\n"
    "v_mov_b32 v223, s33\n"
    "s_branch .Lk2_q1\n"
    ".Lk2_fl:\n"
    "s_bitcmp1_b32 s97, 0\n"
    "s_cbranch_scc1 .Lk2_xr\n"
    "s_or_b32 s97, s97, 1\n"
    "s_waitcnt vmcnt(0) lgkmcnt(0)\n"
    "v_add_u32 v244, 0xffff0000, v244\n"
    "v_add_u32 v245, 0xffff0000, v245\n"
    "v_add_u32 v246, 0xffff0000, v246\n"
    "ds_read_b128 a[0:3], v244 offset:0\n"
    "ds_read_b128 a[4:7], v244 offset:1024\n"
    "ds_read_b128 a[8:11], v244 offset:2048\n"
    "ds_read_b128 a[12:15], v244 offset:3072\n"
    "ds_read_b128 a[16:19], v244 offset:4096\n"
    "ds_read_b128 a[20:23], v244 offset:5120\n"
    "ds_read_b128 a[24:27], v244 offset:6144\n"
    "ds_read_b128 a[28:31], v244 offset:7168\n"
    "ds_read_b128 a[32:35], v244 offset:8192\n"
    "ds_read_b128 a[36:39], v244 offset:9216\n"
    "ds_read_b128 a[40:43], v244 offset:10240\n"
    "ds_read_b128 a[44:47], v244 offset:11264\n"
    "ds_read_b128 a[48:51], v244 offset:12288\n"
    "ds_read_b128 a[52:55], v244 offset:13312\n"
    "ds_read_b128 a[56:59], v244 offset:14336\n"
    "ds_read_b128 a[60:63], v244 offset:15360\n"
    "v_accvgpr_read_b32 v224, a184\n"
    "v_accvgpr_read_b32 v225, a185\n"
    "v_accvgpr_read_b32 v226, a186\n"
    "v_accvgpr_read_b32 v227, a187\n"
    "v_accvgpr_read_b32 v228, a188\n"
    "v_accvgpr_read_b32 v229, a189\n"
    "v_accvgpr_read_b32 v230, a190\n"
    "v_accvgpr_read_b32 v231, a191\n"
    "s_waitcnt lgkmcnt(0)\n"
    "v_mfma_f32_16x16x32_bf16 v[0:3], a[0:3], v[32:35], v[224:227]\n"
    "v_mfma_f32_16x16x32_bf16 v[4:7], a[4:7], v[32:35], v[228:231]\n"
    "v_mfma_f32_16x16x32_bf16 v[8:11], a[0:3], v[56:59], 0\n"
    "v_mfma_f32_16x16x32_bf16 v[12:15], a[4:7], v[56:59], 0\n"
    "v_mfma_f32_16x16x32_bf16 v[16:19], a[8:11], v[56:59], v[224:227]\n"
    "s_nop 3\n"
    "v_max_f32 v38, v0, v1\n"
    "v_max3_f32 v38, v38, v2, v3\n"
    "v_mfma_f32_16x16x32_bf16 v[20:23], a[12:15], v[56:59], v[228:231]\n"
    "v_max3_f32 v38, v38, v4, v5\n"
    "v_max3_f32 v38, v38, v6, v7\n"
    "v_mfma_f32_16x16x32_bf16 v[0:3], a[0:3], v[80:83], 0\n"
    "v_max_f32 v62, v8, v9\n"
    "v_max3_f32 v62, v62, v10, v11\n"
    "v_mfma_f32_16x16x32_bf16 v[4:7], a[4:7], v[80:83], 0\n"
    "v_max3_f32 v62, v62, v12, v13\n"
    "v_max3_f32 v62, v62, v14, v15\n"
    "v_mfma_f32_16x16x32_bf16 v[8:11], a[8:11], v[80:83], 0\n"
    "v_max3_f32 v62, v62, v16, v17\n"
    "v_max3_f32 v62, v62, v18, v19\n"
    "v_mfma_f32_16x16x32_bf16 v[12:15], a[12:15], v[80:83], 0\n"
    "v_max3_f32 v62, v62, v20, v21\n"
    "v_max3_f32 v62, v62, v22, v23\n"
    "v_mfma_f32_16x16x32_bf16 v[16:19], a[16:19], v[80:83], v[224:227]\n"
    "v_max_f32 v86, v0, v1\n"
    "v_max3_f32 v86, v86, v2, v3\n"
    "v_mfma_f32_16x16x32_bf16 v[20:23], a[20:23], v[80:83], v[228:231]\n"
    "v_max3_f32 v86, v86, v4, v5\n"
    "v_max3_f32 v86, v86, v6, v7\n"
    "v_mfma_f32_16x16x32_bf16 v[0:3], a[0:3], v[104:107], 0\n"
    "v_max3_f32 v86, v86, v8, v9\n"
    "v_max3_f32 v86, v86, v10, v11\n"
    "v_mfma_f32_16x16x32_bf16 v[4:7], a[4:7], v[104:107], 0\n"
    "v_max3_f32 v86, v86, v12, v13\n"
    "v_max3_f32 v86, v86, v14, v15\n"
    "v_mfma_f32_16x16x32_bf16 v[8:11], a[8:11], v[104:107], 0\n"
    "v_max3_f32 v86, v86, v16, v17\n"
    "v_max3_f32 v86, v86, v18, v19\n"
    "v_mfma_f32_16x16x32_bf16 v[12:15], a[12:15], v[104:107], 0\n"
    "v_max3_f32 v86, v86, v20, v21\n"
    "v_max3_f32 v86, v86, v22, v23\n"
    "v_mfma_f32_16x16x32_bf16 v[16:19], a[16:19], v[104:107], 0\n"
    "v_max_f32 v110, v0, v1\n"
    "v_max3_f32 v110, v110, v2, v3\n"
    "v_mfma_f32_16x16x32_bf16 v[20:23], a[20:23], v[104:107], 0\n"
    "v_max3_f32 v110, v110, v4, v5\n"
    "v_max3_f32 v110, v110, v6, v7\n"
    "v_mfma_f32_16x16x32_bf16 v[0:3], a[24:27], v[104:107], v[224:227]\n"
    "v_max3_f32 v110, v110, v8, v9\n"
    "v_max3_f32 v110, v110, v10, v11\n"
    "v_mfma_f32_16x16x32_bf16 v[4:7], a[28:31], v[104:107], v[228:231]\n"
    "v_max3_f32 v110, v110, v12, v13\n"
    "v_max3_f32 v110, v110, v14, v15\n"
    "v_mfma_f32_16x16x32_bf16 v[8:11], a[0:3], v[128:131], 0\n"
    "v_max3_f32 v110, v110, v16, v17\n"
    "v_max3_f32 v110, v110, v18, v19\n"
    "v_mfma_f32_16x16x32_bf16 v[12:15], a[4:7], v[128:131], 0\n"
    "v_max3_f32 v110, v110, v20, v21\n"
    "v_max3_f32 v110, v110, v22, v23\n"
    "v_mfma_f32_16x16x32_bf16 v[16:19], a[8:11], v[128:131], 0\n"
    "v_max3_f32 v110, v110, v0, v1\n"
    "v_max3_f32 v110, v110, v2, v3\n"
    "v_mfma_f32_16x16x32_bf16 v[20:23], a[12:15], v[128:131], 0\n"
    "v_max3_f32 v110, v110, v4, v5\n"
    "v_max3_f32 v110, v110, v6, v7\n"
    "v_mfma_f32_16x16x32_bf16 v[0:3], a[16:19], v[128:131], 0\n"
    "v_max_f32 v134, v8, v9\n"
    "v_max3_f32 v134, v134, v10, v11\n"
    "v_mfma_f32_16x16x32_bf16 v[4:7], a[20:23], v[128:131], 0\n"
    "v_max3_f32 v134, v134, v12, v13\n"
    "v_max3_f32 v134, v134, v14, v15\n"
    "v_mfma_f32_16x16x32_bf16 v[8:11], a[24:27], v[128:131], 0\n"
    "v_max3_f32 v134, v134, v16, v17\n"
    "v_max3_f32 v134, v134, v18, v19\n"
    "v_mfma_f32_16x16x32_bf16 v[12:15], a[28:31], v[128:131], 0\n"
    "v_max3_f32 v134, v134, v20, v21\n"
    "v_max3_f32 v134, v134, v22, v23\n"
    "v_mfma_f32_16x16x32_bf16 v[16:19], a[32:35], v[128:131], v[224:227]\n"
    "v_max3_f32 v134, v134, v0, v1\n"
    "v_max3_f32 v134, v134, v2, v3\n"
    "v_mfma_f32_16x16x32_bf16 v[20:23], a[36:39], v[128:131], v[228:231]\n"
    "v_max3_f32 v134, v134, v4, v5\n"
    "v_max3_f32 v134, v134, v6, v7\n"
    "v_mfma_f32_16x16x32_bf16 v[0:3], a[0:3], v[152:155], 0\n"
    "v_max3_f32 v134, v134, v8, v9\n"
    "v_max3_f32 v134, v134, v10, v11\n"
    "v_mfma_f32_16x16x32_bf16 v[4:7], a[4:7], v[152:155], 0\n"
    "v_max3_f32 v134, v134, v12, v13\n"
    "v_max3_f32 v134, v134, v14, v15\n"
    "v_mfma_f32_16x16x32_bf16 v[8:11], a[8:11], v[152:155], 0\n"
    "v_max3_f32 v134, v134, v16, v17\n"
    "v_max3_f32 v134, v134, v18, v19\n"
    "v_mfma_f32_16x16x32_bf16 v[12:15], a[12:15], v[152:155], 0\n"
    "v_max3_f32 v134, v134, v20, v21\n"
    "v_max3_f32 v134, v134, v22, v23\n"
    "v_mfma_f32_16x16x32_bf16 v[16:19], a[16:19], v[152:155], 0\n"
    "v_max_f32 v158, v0, v1\n"
    "v_max3_f32 v158, v158, v2, v3\n"
    "v_mfma_f32_16x16x32_bf16 v[20:23], a[20:23], v[152:155], 0\n"
    "v_max3_f32 v158, v158, v4, v5\n"
    "v_max3_f32 v158, v158, v6, v7\n"
    "v_mfma_f32_16x16x32_bf16 v[0:3], a[24:27], v[152:155], 0\n"
    "v_max3_f32 v158, v158, v8, v9\n"
    "v_max3_f32 v158, v158, v10, v11\n"
    "v_mfma_f32_16x16x32_bf16 v[4:7], a[28:31], v[152:155], 0\n"
    "v_max3_f32 v158, v158, v12, v13\n"
    "v_max3_f32 v158, v158, v14, v15\n"
    "v_mfma_f32_16x16x32_bf16 v[8:11], a[32:35], v[152:155], 0\n"
    "v_max3_f32 v158, v158, v16, v17\n"
    "v_max3_f32 v158, v158, v18, v19\n"
    "v_mfma_f32_16x16x32_bf16 v[12:15], a[36:39], v[152:155], 0\n"
    "v_max3_f32 v158, v158, v20, v21\n"
    "v_max3_f32 v158, v158, v22, v23\n"
    "v_mfma_f32_16x16x32_bf16 v[16:19], a[40:43], v[152:155], v[224:227]\n"
    "v_max3_f32 v158, v158, v0, v1\n"
    "v_max3_f32 v158, v158, v2, v3\n"
    "v_mfma_f32_16x16x32_bf16 v[20:23], a[44:47], v[152:155], v[228:231]\n"
    "v_max3_f32 v158, v158, v4, v5\n"
    "v_max3_f32 v158, v158, v6, v7\n"
    "v_mfma_f32_16x16x32_bf16 v[0:3], a[0:3], v[176:179], 0\n"
    "v_max3_f32 v158, v158, v8, v9\n"
    "v_max3_f32 v158, v158, v10, v11\n"
    "v_mfma_f32_16x16x32_bf16 v[4:7], a[4:7], v[176:179], 0\n"
    "v_max3_f32 v158, v158, v12, v13\n"
    "v_max3_f32 v158, v158, v14, v15\n"
    "v_mfma_f32_16x16x32_bf16 v[8:11], a[8:11], v[176:179], 0\n"
    "v_max3_f32 v158, v158, v16, v17\n"
    "v_max3_f32 v158, v158, v18, v19\n"
    "v_mfma_f32_16x16x32_bf16 v[12:15], a[12:15], v[176:179], 0\n"
    "v_max3_f32 v158, v158, v20, v21\n"
    "v_max3_f32 v158, v158, v22, v23\n"
    "v_mfma_f32_16x16x32_bf16 v[16:19], a[16:19], v[176:179], 0\n"
    "v_max_f32 v182, v0, v1\n"
    "v_max3_f32 v182, v182, v2, v3\n"
    "v_mfma_f32_16x16x32_bf16 v[20:23], a[20:23], v[176:179], 0\n"
    "v_max3_f32 v182, v182, v4, v5\n"
    "v_max3_f32 v182, v182, v6, v7\n"
    "v_mfma_f32_16x16x32_bf16 v[0:3], a[24:27], v[176:179], 0\n"
    "v_max3_f32 v182, v182, v8, v9\n"
    "v_max3_f32 v182, v182, v10, v11\n"
    "v_mfma_f32_16x16x32_bf16 v[4:7], a[28:31], v[176:179], 0\n"
    "v_max3_f32 v182, v182, v12, v13\n"
    "v_max3_f32 v182, v182, v14, v15\n"
    "v_mfma_f32_16x16x32_bf16 v[8:11], a[32:35], v[176:179], 0\n"
    "v_max3_f32 v182, v182, v16, v17\n"
    "v_max3_f32 v182, v182, v18, v19\n"
    "v_mfma_f32_16x16x32_bf16 v[12:15], a[36:39], v[176:179], 0\n"
    "v_max3_f32 v182, v182, v20, v21\n"
    "v_max3_f32 v182, v182, v22, v23\n"
    "v_mfma_f32_16x16x32_bf16 v[16:19], a[40:43], v[176:179], 0\n"
    "v_max3_f32 v182, v182, v0, v1\n"
    "v_max3_f32 v182, v182, v2, v3\n"
    "v_mfma_f32_16x16x32_bf16 v[20:23], a[44:47], v[176:179], 0\n"
    "v_max3_f32 v182, v182, v4, v5\n"
    "v_max3_f32 v182, v182, v6, v7\n"
    "v_mfma_f32_16x16x32_bf16 v[0:3], a[48:51], v[176:179], v[224:227]\n"
    "v_max3_f32 v182, v182, v8, v9\n"
    "v_max3_f32 v182, v182, v10, v11\n"
    "v_mfma_f32_16x16x32_bf16 v[4:7], a[52:55], v[176:179], v[228:231]\n"
    "v_max3_f32 v182, v182, v12, v13\n"
    "v_max3_f32 v182, v182, v14, v15\n"
    "v_mfma_f32_16x16x32_bf16 v[8:11], a[0:3], v[200:203], 0\n"
    "v_max3_f32 v182, v182, v16, v17\n"
    "v_max3_f32 v182, v182, v18, v19\n"
    "v_mfma_f32_16x16x32_bf16 v[12:15], a[4:7], v[200:203], 0\n"
    "v_max3_f32 v182, v182, v20, v21\n"
    "v_max3_f32 v182, v182, v22, v23\n"
    "v_mfma_f32_16x16x32_bf16 v[16:19], a[8:11], v[200:203], 0\n"
    "v_max3_f32 v182, v182, v0, v1\n"
    "v_max3_f32 v182, v182, v2, v3\n"
    "v_mfma_f32_16x16x32_bf16 v[20:23], a[12:15], v[200:203], 0\n"
    "v_max3_f32 v182, v182, v4, v5\n"
    "v_max3_f32 v182, v182, v6, v7\n"
    "v_mfma_f32_16x16x32_bf16 v[0:3], a[16:19], v[200:203], 0\n"
    "v_max_f32 v206, v8, v9\n"
    "v_max3_f32 v206, v206, v10, v11\n"
    "v_mfma_f32_16x16x32_bf16 v[4:7], a[20:23], v[200:203], 0\n"
    "v_max3_f32 v206, v206, v12, v13\n"
    "v_max3_f32 v206, v206, v14, v15\n"
    "v_mfma_f32_16x16x32_bf16 v[8:11], a[24:27], v[200:203], 0\n"
    "v_max3_f32 v206, v206, v16, v17\n"
    "v_max3_f32 v206, v206, v18, v19\n"
    "v_mfma_f32_16x16x32_bf16 v[12:15], a[28:31], v[200:203], 0\n"
    "v_max3_f32 v206, v206, v20, v21\n"
    "v_max3_f32 v206, v206, v22, v23\n"
    "v_mfma_f32_16x16x32_bf16 v[16:19], a[32:35], v[200:203], 0\n"
    "v_max3_f32 v206, v206, v0, v1\n"
    "v_max3_f32 v206, v206, v2, v3\n"
    "v_mfma_f32_16x16x32_bf16 v[20:23], a[36:39], v[200:203], 0\n"
    "v_max3_f32 v206, v206, v4, v5\n"
    "v_max3_f32 v206, v206, v6, v7\n"
    "v_mfma_f32_16x16x32_bf16 v[0:3], a[40:43], v[200:203], 0\n"
    "v_max3_f32 v206, v206, v8, v9\n"
    "v_max3_f32 v206, v206, v10, v11\n"
    "v_mfma_f32_16x16x32_bf16 v[4:7], a[44:47], v[200:203], 0\n"
    "v_max3_f32 v206, v206, v12, v13\n"
    "v_max3_f32 v206, v206, v14, v15\n"
    "v_mfma_f32_16x16x32_bf16 v[8:11], a[48:51], v[200:203], 0\n"
    "v_max3_f32 v206, v206, v16, v17\n"
    "v_max3_f32 v206, v206, v18, v19\n"
    "v_mfma_f32_16x16x32_bf16 v[12:15], a[52:55], v[200:203], 0\n"
    "v_max3_f32 v206, v206, v20, v21\n"
    "v_max3_f32 v206, v206, v22, v23\n"
    "v_mfma_f32_16x16x32_bf16 v[16:19], a[56:59], v[200:203], v[224:227]\n"
    "v_max3_f32 v206, v206, v0, v1\n"
    "v_max3_f32 v206, v206, v2, v3\n"
    "v_mfma_f32_16x16x32_bf16 v[20:23], a[60:63], v[200:203], v[228:231]\n"
    "v_max3_f32 v206, v206, v4, v5\n"
    "v_max3_f32 v206, v206, v6, v7\n"
    "v_max3_f32 v206, v206, v8, v9\n"
    "v_max3_f32 v206, v206, v10, v11\n"
    "v_max3_f32 v206, v206, v12, v13\n"
    "v_max3_f32 v206, v206, v14, v15\n"
    "v_max3_f32 v206, v206, v16, v17\n"
    "v_max3_f32 v206, v206, v18, v19\n"
    "v_max3_f32 v206, v206, v20, v21\n"
    "v_max3_f32 v206, v206, v22, v23\n"
    "v_mov_b32 v39, v38\n"
    "v_mov_b32 v63, v62\n"
    "v_mov_b32 v87, v86\n"
    "v_mov_b32 v111, v110\n"
    "v_mov_b32 v135, v134\n"
    "v_mov_b32 v159, v158\n"
    "v_mov_b32 v183, v182\n"
    "v_mov_b32 v207, v206\n"
    "v_permlane32_swap_b32 v38, v39\n"
    "v_permlane32_swap_b32 v62, v63\n"
    "v_permlane32_swap_b32 v86, v87\n"
    "v_permlane32_swap_b32 v110, v111\n"
    "v_permlane32_swap_b32 v134, v135\n"
    "v_permlane32_swap_b32 v158, v159\n"
    "v_permlane32_swap_b32 v182, v183\n"
    "v_permlane32_swap_b32 v206, v207\n"
    "v_max_f32 v38, v38, v39\n"
    "v_max_f32 v62, v62, v63\n"
    "v_max_f32 v86, v86, v87\n"
    "v_max_f32 v110, v110, v111\n"
    "v_max_f32 v134, v134, v135\n"
    "v_max_f32 v158, v158, v159\n"
    "v_max_f32 v182, v182, v183\n"
    "v_max_f32 v206, v206, v207\n"
    "v_mov_b32 v39, v38\n"
    "v_mov_b32 v63, v62\n"
    "v_mov_b32 v87, v86\n"
    "v_mov_b32 v111, v110\n"
    "v_mov_b32 v135, v134\n"
    "v_mov_b32 v159, v158\n"
    "v_mov_b32 v183, v182\n"
    "v_mov_b32 v207, v206\n"
    "v_permlane16_swap_b32 v38, v39\n"
    "v_permlane16_swap_b32 v62, v63\n"
    "v_permlane16_swap_b32 v86, v87\n"
    "v_permlane16_swap_b32 v110, v111\n"
    "v_permlane16_swap_b32 v134, v135\n"
    "v_permlane16_swap_b32 v158, v159\n"
    "v_permlane16_swap_b32 v182, v183\n"
    "v_permlane16_swap_b32 v206, v207\n"
    "v_max_f32 v38, v38, v39\n"
    "v_max_f32 v62, v62, v63\n"
    "v_max_f32 v86, v86, v87\n"
    "v_max_f32 v110, v110, v111\n"
    "v_max_f32 v134, v134, v135\n"
    "v_max_f32 v158, v158, v159\n"
    "v_max_f32 v182, v182, v183\n"
    "v_max_f32 v206, v206, v207\n"
    "v_pk_mul_f32 v[52:53], v[38:39], -1.0 op_sel_hi:[0,0]\n"
    "v_pk_mul_f32 v[54:55], v[38:39], -1.0 op_sel_hi:[0,0]\n"
    "v_pk_mul_f32 v[76:77], v[62:63], -1.0 op_sel_hi:[0,0]\n"
    "v_pk_mul_f32 v[78:79], v[62:63], -1.0 op_sel_hi:[0,0]\n"
    "v_pk_mul_f32 v[100:101], v[86:87], -1.0 op_sel_hi:[0,0]\n"
    "v_pk_mul_f32 v[102:103], v[86:87], -1.0 op_sel_hi:[0,0]\n"
    "v_pk_mul_f32 v[124:125], v[110:111], -1.0 op_sel_hi:[0,0]\n"
    "v_pk_mul_f32 v[126:127], v[110:111], -1.0 op_sel_hi:[0,0]\n"
    "v_pk_mul_f32 v[148:149], v[134:135], -1.0 op_sel_hi:[0,0]\n"
    "v_pk_mul_f32 v[150:151], v[134:135], -1.0 op_sel_hi:[0,0]\n"
    "v_pk_mul_f32 v[172:173], v[158:159], -1.0 op_sel_hi:[0,0]\n"
    "v_pk_mul_f32 v[174:175], v[158:159], -1.0 op_sel_hi:[0,0]\n"
    "v_pk_mul_f32 v[196:197], v[182:183], -1.0 op_sel_hi:[0,0]\n"
    "v_pk_mul_f32 v[198:199], v[182:183], -1.0 op_sel_hi:[0,0]\n"
    "v_pk_mul_f32 v[220:221], v[206:207], -1.0 op_sel_hi:[0,0]\n"
    "v_pk_mul_f32 v[222:223], v[206:207], -1.0 op_sel_hi:[0,0]\n"
    "s_branch .Lk2_xl\n"
    ".Lk2_fr:\n"
    "s_bitcmp1_b32 s97, 1\n"
    "s_cbranch_scc1 .Lk2_frok\n"
    "s_cmp_eq_u32 s57, 0\n"
    "s_cbranch_scc1 .Lk2_frok\n"
    "s_or_b32 s97, s97, 2\n"
    "s_waitcnt vmcnt(0) lgkmcnt(0)\n"
    "v_xor_b32 v36, 0x80000000, v52\n"
    "v_xor_b32 v60, 0x80000000, v76\n"
    "v_xor_b32 v84, 0x80000000, v100\n"
    "v_xor_b32 v108, 0x80000000, v124\n"
    "v_xor_b32 v132, 0x80000000, v148\n"
    "v_xor_b32 v156, 0x80000000, v172\n"
    "v_xor_b32 v180, 0x80000000, v196\n"
    "v_xor_b32 v204, 0x80000000, v220\n"
    "v_mfma_f32_16x16x32_bf16 v[0:3], a[0:3], v[200:203], v[224:227]\n"
    "v_mfma_f32_16x16x32_bf16 v[4:7], a[4:7], v[200:203], v[228:231]\n"
    "v_mfma_f32_16x16x32_bf16 v[8:11], a[8:11], v[176:179], v[224:227]\n"
    "v_mfma_f32_16x16x32_bf16 v[12:15], a[12:15], v[176:179], v[228:231]\n"
    "v_mfma_f32_16x16x32_bf16 v[16:19], a[0:3], v[176:179], 0\n"
    "s_nop 3\n"
    "v_max3_f32 v206, v204, v0, v1\n"
    "v_max3_f32 v206, v206, v2, v3\n"
    "v_mfma_f32_16x16x32_bf16 v[20:23], a[4:7], v[176:179], 0\n"
    "v_max3_f32 v206, v206, v4, v5\n"
    "v_max3_f32 v206, v206, v6, v7\n"
    "v_mfma_f32_16x16x32_bf16 v[0:3], a[16:19], v[152:155], v[224:227]\n"
    "v_max3_f32 v182, v180, v8, v9\n"
    "v_max3_f32 v182, v182, v10, v11\n"
    "v_mfma_f32_16x16x32_bf16 v[4:7], a[20:23], v[152:155], v[228:231]\n"
    "v_max3_f32 v182, v182, v12, v13\n"
    "v_max3_f32 v182, v182, v14, v15\n"
    "v_mfma_f32_16x16x32_bf16 v[8:11], a[8:11], v[152:155], 0\n"
    "v_max3_f32 v182, v182, v16, v17\n"
    "v_max3_f32 v182, v182, v18, v19\n"
    "v_mfma_f32_16x16x32_bf16 v[12:15], a[12:15], v[152:155], 0\n"
    "v_max3_f32 v182, v182, v20, v21\n"
    "v_max3_f32 v182, v182, v22, v23\n"
    "v_mfma_f32_16x16x32_bf16 v[16:19], a[0:3], v[152:155], 0\n"
    "v_max3_f32 v158, v156, v0, v1\n"
    "v_max3_f32 v158, v158, v2, v3\n"
    "v_mfma_f32_16x16x32_bf16 v[20:23], a[4:7], v[152:155], 0\n"
    "v_max3_f32 v158, v158, v4, v5\n"
    "v_max3_f32 v158, v158, v6, v7\n"
    "v_mfma_f32_16x16x32_bf16 v[0:3], a[24:27], v[128:131], v[224:227]\n"
    "v_max3_f32 v158, v158, v8, v9\n"
    "v_max3_f32 v158, v158, v10, v11\n"
    "v_mfma_f32_16x16x32_bf16 v[4:7], a[28:31], v[128:131], v[228:231]\n"
    "v_max3_f32 v158, v158, v12, v13\n"
    "v_max3_f32 v158, v158, v14, v15\n"
    "v_mfma_f32_16x16x32_bf16 v[8:11], a[16:19], v[128:131], 0\n"
    "v_max3_f32 v158, v158, v16, v17\n"
    "v_max3_f32 v158, v158, v18, v19\n"
    "v_mfma_f32_16x16x32_bf16 v[12:15], a[20:23], v[128:131], 0\n"
    "v_max3_f32 v158, v158, v20, v21\n"
    "v_max3_f32 v158, v158, v22, v23\n"
    "v_mfma_f32_16x16x32_bf16 v[16:19], a[8:11], v[128:131], 0\n"
    "v_max3_f32 v134, v132, v0, v1\n"
    "v_max3_f32 v134, v134, v2, v3\n"
    "v_mfma_f32_16x16x32_bf16 v[20:23], a[12:15], v[128:131], 0\n"
    "v_max3_f32 v134, v134, v4, v5\n"
    "v_max3_f32 v134, v134, v6, v7\n"
    "v_mfma_f32_16x16x32_bf16 v[0:3], a[0:3], v[128:131], 0\n"
    "v_max3_f32 v134, v134, v8, v9\n"
    "v_max3_f32 v134, v134, v10, v11\n"
    "v_mfma_f32_16x16x32_bf16 v[4:7], a[4:7], v[128:131], 0\n"
    "v_max3_f32 v134, v134, v12, v13\n"
    "v_max3_f32 v134, v134, v14, v15\n"
    "v_mfma_f32_16x16x32_bf16 v[8:11], a[32:35], v[104:107], v[224:227]\n"
    "v_max3_f32 v134, v134, v16, v17\n"
    "v_max3_f32 v134, v134, v18, v19\n"
    "v_mfma_f32_16x16x32_bf16 v[12:15], a[36:39], v[104:107], v[228:231]\n"
    "v_max3_f32 v134, v134, v20, v21\n"
    "v_max3_f32 v134, v134, v22, v23\n"
    "v_mfma_f32_16x16x32_bf16 v[16:19], a[24:27], v[104:107], 0\n"
    "v_max3_f32 v134, v134, v0, v1\n"
    "v_max3_f32 v134, v134, v2, v3\n"
    "v_mfma_f32_16x16x32_bf16 v[20:23], a[28:31], v[104:107], 0\n"
    "v_max3_f32 v134, v134, v4, v5\n"
    "v_max3_f32 v134, v134, v6, v7\n"
    "v_mfma_f32_16x16x32_bf16 v[0:3], a[16:19], v[104:107], 0\n"
    "v_max3_f32 v110, v108, v8, v9\n"
    "v_max3_f32 v110, v110, v10, v11\n"
    "v_mfma_f32_16x16x32_bf16 v[4:7], a[20:23], v[104:107], 0\n"
    "v_max3_f32 v110, v110, v12, v13\n"
    "v_max3_f32 v110, v110, v14, v15\n"
    "v_mfma_f32_16x16x32_bf16 v[8:11], a[8:11], v[104:107], 0\n"
    "v_max3_f32 v110, v110, v16, v17\n"
    "v_max3_f32 v110, v110, v18, v19\n"
    "v_mfma_f32_16x16x32_bf16 v[12:15], a[12:15], v[104:107], 0\n"
    "v_max3_f32 v110, v110, v20, v21\n"
    "v_max3_f32 v110, v110, v22, v23\n"
    "v_mfma_f32_16x16x32_bf16 v[16:19], a[0:3], v[104:107], 0\n"
    "v_max3_f32 v110, v110, v0, v1\n"
    "v_max3_f32 v110, v110, v2, v3\n"
    "v_mfma_f32_16x16x32_bf16 v[20:23], a[4:7], v[104:107], 0\n"
    "v_max3_f32 v110, v110, v4, v5\n"
    "v_max3_f32 v110, v110, v6, v7\n"
    "v_mfma_f32_16x16x32_bf16 v[0:3], a[40:43], v[80:83], v[224:227]\n"
    "v_max3_f32 v110, v110, v8, v9\n"
    "v_max3_f32 v110, v110, v10, v11\n"
    "v_mfma_f32_16x16x32_bf16 v[4:7], a[44:47], v[80:83], v[228:231]\n"
    "v_max3_f32 v110, v110, v12, v13\n"
    "v_max3_f32 v110, v110, v14, v15\n"
    "v_mfma_f32_16x16x32_bf16 v[8:11], a[32:35], v[80:83], 0\n"
    "v_max3_f32 v110, v110, v16, v17\n"
    "v_max3_f32 v110, v110, v18, v19\n"
    "v_mfma_f32_16x16x32_bf16 v[12:15], a[36:39], v[80:83], 0\n"
    "v_max3_f32 v110, v110, v20, v21\n"
    "v_max3_f32 v110, v110, v22, v23\n"
    "v_mfma_f32_16x16x32_bf16 v[16:19], a[24:27], v[80:83], 0\n"
    "v_max3_f32 v86, v84, v0, v1\n"
    "v_max3_f32 v86, v86, v2, v3\n"
    "v_mfma_f32_16x16x32_bf16 v[20:23], a[28:31], v[80:83], 0\n"
    "v_max3_f32 v86, v86, v4, v5\n"
    "v_max3_f32 v86, v86, v6, v7\n"
    "v_mfma_f32_16x16x32_bf16 v[0:3], a[16:19], v[80:83], 0\n"
    "v_max3_f32 v86, v86, v8, v9\n"
    "v_max3_f32 v86, v86, v10, v11\n"
    "v_mfma_f32_16x16x32_bf16 v[4:7], a[20:23], v[80:83], 0\n"
    "v_max3_f32 v86, v86, v12, v13\n"
    "v_max3_f32 v86, v86, v14, v15\n"
    "v_mfma_f32_16x16x32_bf16 v[8:11], a[8:11], v[80:83], 0\n"
    "v_max3_f32 v86, v86, v16, v17\n"
    "v_max3_f32 v86, v86, v18, v19\n"
    "v_mfma_f32_16x16x32_bf16 v[12:15], a[12:15], v[80:83], 0\n"
    "v_max3_f32 v86, v86, v20, v21\n"
    "v_max3_f32 v86, v86, v22, v23\n"
    "v_mfma_f32_16x16x32_bf16 v[16:19], a[0:3], v[80:83], 0\n"
    "v_max3_f32 v86, v86, v0, v1\n"
    "v_max3_f32 v86, v86, v2, v3\n"
    "v_mfma_f32_16x16x32_bf16 v[20:23], a[4:7], v[80:83], 0\n"
    "v_max3_f32 v86, v86, v4, v5\n"
    "v_max3_f32 v86, v86, v6, v7\n"
    "v_mfma_f32_16x16x32_bf16 v[0:3], a[48:51], v[56:59], v[224:227]\n"
    "v_max3_f32 v86, v86, v8, v9\n"
    "v_max3_f32 v86, v86, v10, v11\n"
    "v_mfma_f32_16x16x32_bf16 v[4:7], a[52:55], v[56:59], v[228:231]\n"
    "v_max3_f32 v86, v86, v12, v13\n"
    "v_max3_f32 v86, v86, v14, v15\n"
    "v_mfma_f32_16x16x32_bf16 v[8:11], a[40:43], v[56:59], 0\n"
    "v_max3_f32 v86, v86, v16, v17\n"
    "v_max3_f32 v86, v86, v18, v19\n"
    "v_mfma_f32_16x16x32_bf16 v[12:15], a[44:47], v[56:59], 0\n"
    "v_max3_f32 v86, v86, v20, v21\n"
    "v_max3_f32 v86, v86, v22, v23\n"
    "v_mfma_f32_16x16x32_bf16 v[16:19], a[32:35], v[56:59], 0\n"
    "v_max3_f32 v62, v60, v0, v1\n"
    "v_max3_f32 v62, v62, v2, v3\n"
    "v_mfma_f32_16x16x32_bf16 v[20:23], a[36:39], v[56:59], 0\n"
    "v_max3_f32 v62, v62, v4, v5\n"
    "v_max3_f32 v62, v62, v6, v7\n"
    "v_mfma_f32_16x16x32_bf16 v[0:3], a[24:27], v[56:59], 0\n"
    "v_max3_f32 v62, v62, v8, v9\n"
    "v_max3_f32 v62, v62, v10, v11\n"
    "v_mfma_f32_16x16x32_bf16 v[4:7], a[28:31], v[56:59], 0\n"
    "v_max3_f32 v62, v62, v12, v13\n"
    "v_max3_f32 v62, v62, v14, v15\n"
    "v_mfma_f32_16x16x32_bf16 v[8:11], a[16:19], v[56:59], 0\n"
    "v_max3_f32 v62, v62, v16, v17\n"
    "v_max3_f32 v62, v62, v18, v19\n"
    "v_mfma_f32_16x16x32_bf16 v[12:15], a[20:23], v[56:59], 0\n"
    "v_max3_f32 v62, v62, v20, v21\n"
    "v_max3_f32 v62, v62, v22, v23\n"
    "v_mfma_f32_16x16x32_bf16 v[16:19], a[8:11], v[56:59], 0\n"
    "v_max3_f32 v62, v62, v0, v1\n"
    "v_max3_f32 v62, v62, v2, v3\n"
    "v_mfma_f32_16x16x32_bf16 v[20:23], a[12:15], v[56:59], 0\n"
    "v_max3_f32 v62, v62, v4, v5\n"
    "v_max3_f32 v62, v62, v6, v7\n"
    "v_mfma_f32_16x16x32_bf16 v[0:3], a[0:3], v[56:59], 0\n"
    "v_max3_f32 v62, v62, v8, v9\n"
    "v_max3_f32 v62, v62, v10, v11\n"
    "v_mfma_f32_16x16x32_bf16 v[4:7], a[4:7], v[56:59], 0\n"
    "v_max3_f32 v62, v62, v12, v13\n"
    "v_max3_f32 v62, v62, v14, v15\n"
    "v_mfma_f32_16x16x32_bf16 v[8:11], a[56:59], v[32:35], v[224:227]\n"
    "v_max3_f32 v62, v62, v16, v17\n"
    "v_max3_f32 v62, v62, v18, v19\n"
    "v_mfma_f32_16x16x32_bf16 v[12:15], a[60:63], v[32:35], v[228:231]\n"
    "v_max3_f32 v62, v62, v20, v21\n"
    "v_max3_f32 v62, v62, v22, v23\n"
    "v_mfma_f32_16x16x32_bf16 v[16:19], a[48:51], v[32:35], 0\n"
    "v_max3_f32 v62, v62, v0, v1\n"
    "v_max3_f32 v62, v62, v2, v3\n"
    "v_mfma_f32_16x16x32_bf16 v[20:23], a[52:55], v[32:35], 0\n"
    "v_max3_f32 v62, v62, v4, v5\n"
    "v_max3_f32 v62, v62, v6, v7\n"
    "v_mfma_f32_16x16x32_bf16 v[0:3], a[40:43], v[32:35], 0\n"
    "v_max3_f32 v38, v36, v8, v9\n"
    "v_max3_f32 v38, v38, v10, v11\n"
    "v_mfma_f32_16x16x32_bf16 v[4:7], a[44:47], v[32:35], 0\n"
    "v_max3_f32 v38, v38, v12, v13\n"
    "v_max3_f32 v38, v38, v14, v15\n"
    "v_mfma_f32_16x16x32_bf16 v[8:11], a[32:35], v[32:35], 0\n"
    "v_max3_f32 v38, v38, v16, v17\n"
    "v_max3_f32 v38, v38, v18, v19\n"
    "v_mfma_f32_16x16x32_bf16 v[12:15], a[36:39], v[32:35], 0\n"
    "v_max3_f32 v38, v38, v20, v21\n"
    "v_max3_f32 v38, v38, v22, v23\n"
    "v_mfma_f32_16x16x32_bf16 v[16:19], a[24:27], v[32:35], 0\n"
    "v_max3_f32 v38, v38, v0, v1\n"
    "v_max3_f32 v38, v38, v2, v3\n"
    "v_mfma_f32_16x16x32_bf16 v[20:23], a[28:31], v[32:35], 0\n"
    "v_max3_f32 v38, v38, v4, v5\n"
    "v_max3_f32 v38, v38, v6, v7\n"
    "v_mfma_f32_16x16x32_bf16 v[0:3], a[16:19], v[32:35], 0\n"
    "v_max3_f32 v38, v38, v8, v9\n"
    "v_max3_f32 v38, v38, v10, v11\n"
    "v_mfma_f32_16x16x32_bf16 v[4:7], a[20:23], v[32:35], 0\n"
    "v_max3_f32 v38, v38, v12, v13\n"
    "v_max3_f32 v38, v38, v14, v15\n"
    "v_mfma_f32_16x16x32_bf16 v[8:11], a[8:11], v[32:35], 0\n"
    "v_max3_f32 v38, v38, v16, v17\n"
    "v_max3_f32 v38, v38, v18, v19\n"
    "v_mfma_f32_16x16x32_bf16 v[12:15], a[12:15], v[32:35], 0\n"
    "v_max3_f32 v38, v38, v20, v21\n"
    "v_max3_f32 v38, v38, v22, v23\n"
    "v_mfma_f32_16x16x32_bf16 v[16:19], a[0:3], v[32:35], 0\n"
    "v_max3_f32 v38, v38, v0, v1\n"
    "v_max3_f32 v38, v38, v2, v3\n"
    "v_mfma_f32_16x16x32_bf16 v[20:23], a[4:7], v[32:35], 0\n"
    "v_max3_f32 v38, v38, v4, v5\n"
    "v_max3_f32 v38, v38, v6, v7\n"
    "v_max3_f32 v38, v38, v8, v9\n"
    "v_max3_f32 v38, v38, v10, v11\n"
    "v_max3_f32 v38, v38, v12, v13\n"
    "v_max3_f32 v38, v38, v14, v15\n"
    "v_max3_f32 v38, v38, v16, v17\n"
    "v_max3_f32 v38, v38, v18, v19\n"
    "v_max3_f32 v38, v38, v20, v21\n"
    "v_max3_f32 v38, v38, v22, v23\n"
    "v_mov_b32 v39, v38\n"
    "v_mov_b32 v63, v62\n"
    "v_mov_b32 v87, v86\n"
    "v_mov_b32 v111, v110\n"
    "v_mov_b32 v135, v134\n"
    "v_mov_b32 v159, v158\n"
    "v_mov_b32 v183, v182\n"
    "v_mov_b32 v207, v206\n"
    "v_permlane32_swap_b32 v38, v39\n"
    "v_permlane32_swap_b32 v62, v63\n"
    "v_permlane32_swap_b32 v86, v87\n"
    "v_permlane32_swap_b32 v110, v111\n"
    "v_permlane32_swap_b32 v134, v135\n"
    "v_permlane32_swap_b32 v158, v159\n"
    "v_permlane32_swap_b32 v182, v183\n"
    "v_permlane32_swap_b32 v206, v207\n"
    "v_max_f32 v38, v38, v39\n"
    "v_max_f32 v62, v62, v63\n"
    "v_max_f32 v86, v86, v87\n"
    "v_max_f32 v110, v110, v111\n"
    "v_max_f32 v134, v134, v135\n"
    "v_max_f32 v158, v158, v159\n"
    "v_max_f32 v182, v182, v183\n"
    "v_max_f32 v206, v206, v207\n"
    "v_mov_b32 v39, v38\n"
    "v_mov_b32 v63, v62\n"
    "v_mov_b32 v87, v86\n"
    "v_mov_b32 v111, v110\n"
    "v_mov_b32 v135, v134\n"
    "v_mov_b32 v159, v158\n"
    "v_mov_b32 v183, v182\n"
    "v_mov_b32 v207, v206\n"
    "v_permlane16_swap_b32 v38, v39\n"
    "v_permlane16_swap_b32 v62, v63\n"
    "v_permlane16_swap_b32 v86, v87\n"
    "v_permlane16_swap_b32 v110, v111\n"
    "v_permlane16_swap_b32 v134, v135\n"
    "v_permlane16_swap_b32 v158, v159\n"
    "v_permlane16_swap_b32 v182, v183\n"
    "v_permlane16_swap_b32 v206, v207\n"
    "v_max_f32 v38, v38, v39\n"
    "v_max_f32 v62, v62, v63\n"
    "v_max_f32 v86, v86, v87\n"
    "v_max_f32 v110, v110, v111\n"
    "v_max_f32 v134, v134, v135\n"
    "v_max_f32 v158, v158, v159\n"
    "v_max_f32 v182, v182, v183\n"
    "v_max_f32 v206, v206, v207\n"
    "v_sub_f32 v36, v36, v38\n"
    "v_sub_f32 v60, v60, v62\n"
    "v_sub_f32 v84, v84, v86\n"
    "v_sub_f32 v108, v108, v110\n"
    "v_sub_f32 v132, v132, v134\n"
    "v_sub_f32 v156, v156, v158\n"
    "v_sub_f32 v180, v180, v182\n"
    "v_sub_f32 v204, v204, v206\n"
    "v_exp_f32 v36, v36\n"
    "v_exp_f32 v60, v60\n"
    "v_exp_f32 v84, v84\n"
    "v_exp_f32 v108, v108\n"
    "v_exp_f32 v132, v132\n"
    "v_exp_f32 v156, v156\n"
    "v_exp_f32 v180, v180\n"
    "v_exp_f32 v204, v204\n"
    "v_pk_mul_f32 v[40:41], v[40:41], v[36:37] op_sel_hi:[1,0]\n"
    "v_pk_mul_f32 v[42:43], v[42:43], v[36:37] op_sel_hi:[1,0]\n"
    "v_pk_mul_f32 v[44:45], v[44:45], v[36:37] op_sel_hi:[1,0]\n"
    "v_pk_mul_f32 v[46:47], v[46:47], v[36:37] op_sel_hi:[1,0]\n"
    "v_pk_mul_f32 v[48:49], v[48:49], v[36:37] op_sel_hi:[1,0]\n"
    "v_pk_mul_f32 v[50:51], v[50:51], v[36:37] op_sel_hi:[1,0]\n"
    "v_pk_mul_f32 v[64:65], v[64:65], v[60:61] op_sel_hi:[1,0]\n"
    "v_pk_mul_f32 v[66:67], v[66:67], v[60:61] op_sel_hi:[1,0]\n"
    "v_pk_mul_f32 v[68:69], v[68:69], v[60:61] op_sel_hi:[1,0]\n"
    "v_pk_mul_f32 v[70:71], v[70:71], v[60:61] op_sel_hi:[1,0]\n"
    "v_pk_mul_f32 v[72:73], v[72:73], v[60:61] op_sel_hi:[1,0]\n"
    "v_pk_mul_f32 v[74:75], v[74:75], v[60:61] op_sel_hi:[1,0]\n"
    "v_pk_mul_f32 v[88:89], v[88:89], v[84:85] op_sel_hi:[1,0]\n"
    "v_pk_mul_f32 v[90:91], v[90:91], v[84:85] op_sel_hi:[1,0]\n"
    "v_pk_mul_f32 v[92:93], v[92:93], v[84:85] op_sel_hi:[1,0]\n"
    "v_pk_mul_f32 v[94:95], v[94:95], v[84:85] op_sel_hi:[1,0]\n"
    "v_pk_mul_f32 v[96:97], v[96:97], v[84:85] op_sel_hi:[1,0]\n"
    "v_pk_mul_f32 v[98:99], v[98:99], v[84:85] op_sel_hi:[1,0]\n"
    "v_pk_mul_f32 v[112:113], v[112:113], v[108:109] op_sel_hi:[1,0]\n"
    "v_pk_mul_f32 v[114:115], v[114:115], v[108:109] op_sel_hi:[1,0]\n"
    "v_pk_mul_f32 v[116:117], v[116:117], v[108:109] op_sel_hi:[1,0]\n"
    "v_pk_mul_f32 v[118:119], v[118:119], v[108:109] op_sel_hi:[1,0]\n"
    "v_pk_mul_f32 v[120:121], v[120:121], v[108:109] op_sel_hi:[1,0]\n"
    "v_pk_mul_f32 v[122:123], v[122:123], v[108:109] op_sel_hi:[1,0]\n"
    "v_pk_mul_f32 v[136:137], v[136:137], v[132:133] op_sel_hi:[1,0]\n"
    "v_pk_mul_f32 v[138:139], v[138:139], v[132:133] op_sel_hi:[1,0]\n"
    "v_pk_mul_f32 v[140:141], v[140:141], v[132:133] op_sel_hi:[1,0]\n"
    "v_pk_mul_f32 v[142:143], v[142:143], v[132:133] op_sel_hi:[1,0]\n"
    "v_pk_mul_f32 v[144:145], v[144:145], v[132:133] op_sel_hi:[1,0]\n"
    "v_pk_mul_f32 v[146:147], v[146:147], v[132:133] op_sel_hi:[1,0]\n"
    "v_pk_mul_f32 v[160:161], v[160:161], v[156:157] op_sel_hi:[1,0]\n"
    "v_pk_mul_f32 v[162:163], v[162:163], v[156:157] op_sel_hi:[1,0]\n"
    "v_pk_mul_f32 v[164:165], v[164:165], v[156:157] op_sel_hi:[1,0]\n"
    "v_pk_mul_f32 v[166:167], v[166:167], v[156:157] op_sel_hi:[1,0]\n"
    "v_pk_mul_f32 v[168:169], v[168:169], v[156:157] op_sel_hi:[1,0]\n"
    "v_pk_mul_f32 v[170:171], v[170:171], v[156:157] op_sel_hi:[1,0]\n"
    "v_pk_mul_f32 v[184:185], v[184:185], v[180:181] op_sel_hi:[1,0]\n"
    "v_pk_mul_f32 v[186:187], v[186:187], v[180:181] op_sel_hi:[1,0]\n"
    "v_pk_mul_f32 v[188:189], v[188:189], v[180:181] op_sel_hi:[1,0]\n"
    "v_pk_mul_f32 v[190:191], v[190:191], v[180:181] op_sel_hi:[1,0]\n"
    "v_pk_mul_f32 v[192:193], v[192:193], v[180:181] op_sel_hi:[1,0]\n"
    "v_pk_mul_f32 v[194:195], v[194:195], v[180:181] op_sel_hi:[1,0]\n"
    "v_pk_mul_f32 v[208:209], v[208:209], v[204:205] op_sel_hi:[1,0]\n"
    "v_pk_mul_f32 v[210:211], v[210:211], v[204:205] op_sel_hi:[1,0]\n"
    "v_pk_mul_f32 v[212:213], v[212:213], v[204:205] op_sel_hi:[1,0]\n"
    "v_pk_mul_f32 v[214:215], v[214:215], v[204:205] op_sel_hi:[1,0]\n"
    "v_pk_mul_f32 v[216:217], v[216:217], v[204:205] op_sel_hi:[1,0]\n"
    "v_pk_mul_f32 v[218:219], v[218:219], v[204:205] op_sel_hi:[1,0]\n"
    "v_pk_mul_f32 v[52:53], v[38:39], -1.0 op_sel_hi:[0,0]\n"
    "v_pk_mul_f32 v[54:55], v[38:39], -1.0 op_sel_hi:[0,0]\n"
    "v_pk_mul_f32 v[76:77], v[62:63], -1.0 op_sel_hi:[0,0]\n"
    "v_pk_mul_f32 v[78:79], v[62:63], -1.0 op_sel_hi:[0,0]\n"
    "v_pk_mul_f32 v[100:101], v[86:87], -1.0 op_sel_hi:[0,0]\n"
    "v_pk_mul_f32 v[102:103], v[86:87], -1.0 op_sel_hi:[0,0]\n"
    "v_pk_mul_f32 v[124:125], v[110:111], -1.0 op_sel_hi:[0,0]\n"
    "v_pk_mul_f32 v[126:127], v[110:111], -1.0 op_sel_hi:[0,0]\n"
    "v_pk_mul_f32 v[148:149], v[134:135], -1.0 op_sel_hi:[0,0]\n"
    "v_pk_mul_f32 v[150:151], v[134:135], -1.0 op_sel_hi:[0,0]\n"
    "v_pk_mul_f32 v[172:173], v[158:159], -1.0 op_sel_hi:[0,0]\n"
    "v_pk_mul_f32 v[174:175], v[158:159], -1.0 op_sel_hi:[0,0]\n"
    "v_pk_mul_f32 v[196:197], v[182:183], -1.0 op_sel_hi:[0,0]\n"
    "v_pk_mul_f32 v[198:199], v[182:183], -1.0 op_sel_hi:[0,0]\n"
    "v_pk_mul_f32 v[220:221], v[206:207], -1.0 op_sel_hi:[0,0]\n"
    "v_pk_mul_f32 v[222:223], v[206:207], -1.0 op_sel_hi:[0,0]\n"
    "v_accvgpr_read_b32 v254, a198\n"
    "s_and_b32 s91, s35, 4\n"
    "v_readlane_b32 s84, v254, 0\n"
    "v_readlane_b32 s85, v254, 1\n"
    "v_readlane_b32 s87, v254, 2\n"
    "v_readlane_b32 s89, v254, 3\n"
    "s_lshl_b32 s91, s91, 6\n"
    "s_add_u32 s91, s91, 896\n"
    "v_mov_b32 v255, 0\n"
    "v_pk_add_f32 v[232:233], v[224:225], v[220:221]\n"
    "v_pk_add_f32 v[234:235], v[226:227], v[220:221]\n"
    "v_pk_add_f32 v[236:237], v[228:229], v[220:221]\n"
    "v_pk_add_f32 v[238:239], v[230:231], v[220:221]\n"
    "v_mfma_f32_16x16x32_bf16 v[0:3], a[0:3], v[200:203], v[232:235]\n"
    "s_nop 0\n"
    "v_mfma_f32_16x16x32_bf16 v[4:7], a[4:7], v[200:203], v[236:239]\n"
    "s_nop 0\n"
    "v_pk_add_f32 v[232:233], v[224:225], v[196:197]\n"
    "v_pk_add_f32 v[234:235], v[226:227], v[196:197]\n"
    "v_pk_add_f32 v[236:237], v[228:229], v[196:197]\n"
    "v_pk_add_f32 v[238:239], v[230:231], v[196:197]\n"
    "v_mfma_f32_16x16x32_bf16 v[8:11], a[8:11], v[176:179], v[232:235]\n"
    "v_exp_f32 v0, v0\n"
    "v_exp_f32 v1, v1\n"
    "v_mfma_f32_16x16x32_bf16 v[12:15], a[12:15], v[176:179], v[236:239]\n"
    "v_exp_f32 v2, v2\n"
    "v_cvt_pk_bf16_f32 v24, v0, v1\n"
    "v_exp_f32 v3, v3\n"
    "v_exp_f32 v4, v4\n"
    "v_exp_f32 v5, v5\n"
    "v_cvt_pk_bf16_f32 v25, v2, v3\n"
    "v_exp_f32 v6, v6\n"
    "v_exp_f32 v7, v7\n"
    "v_cvt_pk_bf16_f32 v26, v4, v5\n"
    "v_cvt_pk_bf16_f32 v27, v6, v7\n"
    "s_branch .Lk2_xr\n"
    ".Lk2_kslow:\n"
    "s_cmp_eq_u32 s57, 0\n"
    "s_cbranch_scc1 .Lk2_kok\n"
    "s_add_u32 s98, s98, 1\n"
    "s_cmp_gt_u32 s98, 1000000\n"
    "s_cbranch_scc1 .Lk2_kok\n"
    "s_sleep 2\n"
    "s_mov_b64 exec, 15\n"
    "buffer_load_dword v251, v252, s[44:47], s65 offen sc1\n"
    "s_mov_b64 exec, -1\n"
    "s_waitcnt vmcnt(0)\n"
    "v_cmp_ne_u32 vcc, s93, v251\n"
    "s_nop 0\n"
    "s_cmp_eq_u64 vcc, 0\n"
    "s_cbranch_scc0 .Lk2_kslow\n"
    "s_branch .Lk2_kok\n"
    ".Lk2_bslow:\n"
    "s_mov_b32 exec_hi, 0\n"
    "buffer_load_dword v251, v252, s[44:47], s82 offen sc1\n"
    "s_mov_b32 exec_hi, -1\n"
    "s_add_u32 s98, s98, 1\n"
    "s_waitcnt vmcnt(0)\n"
    "s_add_u32 s33, s92, 1\n"
    "v_subrev_u32 v254, s33, v251\n"
    "v_cmp_lt_u32 vcc, 2, v254\n"
    "s_nop 0\n"
    "s_cmp_eq_u32 vcc_lo, -1\n"
    "s_cselect_b32 s27, -1, 0\n"
    "v_cmp_gt_u32 vcc, 16, v254\n"
    "s_cmp_gt_u32 s98, 1000000\n"
    "s_cbranch_scc1 .Lk2_bgo\n"
    "s_cmp_eq_u32 vcc_lo, -1\n"
    "s_cbranch_scc1 .Lk2_bgo\n"
    "s_sleep 4\n"
    "s_branch .Lk2_bslow\n"
    ".Lk2_bgo:\n"
    "v_readlane_b32 s91, v247, 0\n"
    "s_nop 0\n"
    "s_add_u32 s84, s91, s67\n"
    "v_readlane_b32 s91, v247, 1\n"
    "s_xor_b32 s85, s84, 2048\n"
    "buffer_load_dwordx4 a[224:227], v253, s[44:47], s84 offen sc1\n"
    "buffer_load_dwordx4 a[228:231], v253, s[44:47], s84 offen offset:1024 sc1\n"
    "buffer_load_dwordx4 a[232:235], v253, s[44:47], s85 offen sc1\n"
    "buffer_load_dwordx4 a[236:239], v253, s[44:47], s85 offen offset:1024 sc1\n"
    "s_add_u32 s84, s91, s67\n"
    "s_xor_b32 s85, s84, 2048\n"
    "buffer_load_dwordx4 a[240:243], v253, s[44:47], s84 offen sc1\n"
    "buffer_load_dwordx4 a[244:247], v253, s[44:47], s84 offen offset:1024 sc1\n"
    "buffer_load_dwordx4 a[248:251], v253, s[44:47], s85 offen sc1\n"
    "buffer_load_dwordx4 a[252:255], v253, s[44:47], s85 offen offset:1024 sc1\n"
    "s_waitcnt vmcnt(0)\n"
    "s_branch .Lk2_bok\n"
    ".Lk2_pha:\n"
    ".Lk2_m9962:\n"
    "s_cbranch_execz .Lk2_hret\n"
    "buffer_load_dword v19, v17, s[44:47], s82 offen sc1\n"
    "s_branch .Lk2_pra\n"
    ".Lk2_lha:\n"
    "s_cmp_eq_u32 s88, 4\n"
    "s_cselect_b64 s[48:49], s[58:59], s[48:49]\n"
    "s_cselect_b32 s50, 2098176, s50\n"
    "s_cselect_b64 vcc, -1, 0\n"
    "v_cndmask_b32 v17, v17, v18, vcc\n"
    "s_cmp_eq_u32 s27, 0\n"
    "s_cbranch_scc1 .Lk2_lra\n"
    "buffer_load_dword v19, v17, s[44:47], s82 offen sc1\n"
    "s_branch .Lk2_lra\n"
    ".Lk2_phb:\n"
    "s_add_u32 s33, s92, 4\n"
    "v_subrev_u32 v20, s33, v19\n"
    "v_cmp_gt_u32 vcc, 13, v20\n"
    "s_nop 0\n"
    "s_cmp_eq_u64 vcc, -1\n"
    "s_cbranch_scc1 .Lk2_pok\n"
    "s_add_u32 s98, s98, 1\n"
    "s_cmp_gt_u32 s98, 1000000\n"
    "s_cbranch_scc1 .Lk2_pok\n"
    "s_sleep 8\n"
    "buffer_load_dword v19, v17, s[44:47], s82 offen sc1\n"
    "s_waitcnt vmcnt(0)\n"
    "s_branch .Lk2_phb\n"
    ".Lk2_lhb:\n"
    "v_cmp_eq_u32 vcc, v22, v19\n"
    "s_nop 0\n"
    "s_cmp_eq_u64 vcc, -1\n"
    "s_cbranch_scc1 .Lk2_lok\n"
    "s_add_u32 s98, s98, 1\n"
    "s_cmp_gt_u32 s98, 1000000\n"
    "s_cbranch_scc1 .Lk2_lok\n"
    "s_sleep 8\n"
    "buffer_load_dword v19, v17, s[44:47], s82 offen sc1\n"
    "s_waitcnt vmcnt(0)\n"
    "s_branch .Lk2_lhb\n"
    ".Lk2_rslowa:\n"
    "s_add_u32 s98, s98, 1\n"
    "s_cmp_gt_u32 s98, 1000000\n"
    "s_cbranch_scc1 .Lk2_roka\n"
    "s_sleep 2\n"
    "s_mov_b64 exec, 1\n"
    "buffer_load_dword v21, off, s[44:47], s83 sc1\n"
    "s_mov_b64 exec, -1\n"
    "s_waitcnt vmcnt(0)\n"
    "v_cmp_ne_u32 vcc, v22, v21\n"
    "s_nop 0\n"
    "s_bitcmp0_b32 vcc_lo, 0\n"
    "s_cbranch_scc0 .Lk2_rslowa\n"
    "s_branch .Lk2_roka\n"
    ".Lk2_rslowb:\n"
    "s_add_u32 s98, s98, 1\n"
    "s_cmp_gt_u32 s98, 1000000\n"
    "s_cbranch_scc1 .Lk2_rokb\n"
    "s_sleep 2\n"
    "s_mov_b64 exec, 1\n"
    "buffer_load_dword v21, off, s[44:47], s83 sc1\n"
    "s_mov_b64 exec, -1\n"
    "s_waitcnt vmcnt(0)\n"
    "v_cmp_ne_u32 vcc, v22, v21\n"
    "s_nop 0\n"
    "s_bitcmp0_b32 vcc_lo, 0\n"
    "s_cbranch_scc0 .Lk2_rslowb\n"
    "s_branch .Lk2_rokb\n"
    ".Lk2_xslow:\n"
    "s_add_u32 s98, s98, 1\n"
    "s_cmp_gt_u32 s98, 1000000\n"
    "s_cbranch_scc1 .Lk2_xok\n"
    "buffer_load_dwordx4 v[32:35], v16, s[44:47], s81 offen sc1\n"
    "buffer_load_dwordx4 v[56:59], v16, s[44:47], s81 offen offset:1024 sc1\n"
    "buffer_load_dwordx4 v[80:83], v16, s[44:47], s81 offen offset:2048 sc1\n"
    "buffer_load_dwordx4 v[104:107], v16, s[44:47], s81 offen offset:3072 sc1\n"
    "s_branch .Lk2_tail\n"
    ".Lk2_fin:\n"
    : "+{s[0:1]}"(ka), "+{s6}"(b), "+{v0}"(t) :: "memory","scc","vcc","s2","s3","s4","s5","s7","s8","s9","s10","s11","s12","s13","s14","s15","s16","s17","s18","s19","s20","s21","s22","s23","s24","s25","s26","s27","s28","s29","s30","s31","s33","s34","s35","s36","s37","s38","s39","s40","s41","s42","s43","s44","s45","s46","s47","s48","s49","s50","s51","s52","s53","s54","s55","s56","s57","s58","s59","s60","s61","s62","s63","s64","s65","s66","s67","s68","s69","s70","s71","s72","s73","s74","s75","s76","s77","s78","s79","s80","s81","s82","s83","s84","s85","s86","s87","s88","s89","s90","s91","s92","s93","s94","s95","s96","s97","s98","s99","v1","v2","v3","v4","v5","v6","v7","v8","v9","v10","v11","v12","v13","v14","v15","v16","v17","v18","v19","v20","v21","v22","v23","v24","v25","v26","v27","v28","v29","v30","v31","v32","v33","v34","v35","v36","v37","v38","v39","v40","v41","v42","v43","v44","v45","v46","v47","v48","v49","v50","v51","v52","v53","v54","v55","v56","v57","v58","v59","v60","v61","v62","v63","v64","v65","v66","v67","v68","v69","v70","v71","v72","v73","v74","v75","v76","v77","v78","v79","v80","v81","v82","v83","v84","v85","v86","v87","v88","v89","v90","v91","v92","v93","v94","v95","v96","v97","v98","v99","v100","v101","v102","v103","v104","v105","v106","v107","v108","v109","v110","v111","v112","v113","v114","v115","v116","v117","v118","v119","v120","v121","v122","v123","v124","v125","v126","v127","v128","v129","v130","v131","v132","v133","v134","v135","v136","v137","v138","v139","v140","v141","v142","v143","v144","v145","v146","v147","v148","v149","v150","v151","v152","v153","v154","v155","v156","v157","v158","v159","v160","v161","v162","v163","v164","v165","v166","v167","v168","v169","v170","v171","v172","v173","v174","v175","v176","v177","v178","v179","v180","v181","v182","v183","v184","v185","v186","v187","v188","v189","v190","v191","v192","v193","v194","v195","v196","v197","v198","v199","v200","v201","v202","v203","v204","v205","v206","v207","v208","v209","v210","v211","v212","v213","v214","v215","v216","v217","v218","v219","v220","v221","v222","v223","v224","v225","v226","v227","v228","v229","v230","v231","v232","v233","v234","v235","v236","v237","v238","v239","v240","v241","v242","v243","v244","v245","v246","v247","v248","v249","v250","v251","v252","v253","v254","v255","a0","a1","a2","a3","a4","a5","a6","a7","a8","a9","a10","a11","a12","a13","a14","a15","a16","a17","a18","a19","a20","a21","a22","a23","a24","a25","a26","a27","a28","a29","a30","a31","a32","a33","a34","a35","a36","a37","a38","a39","a40","a41","a42","a43","a44","a45","a46","a47","a48","a49","a50","a51","a52","a53","a54","a55","a56","a57","a58","a59","a60","a61","a62","a63","a64","a65","a66","a67","a68","a69","a70","a71","a72","a73","a74","a75","a76","a77","a78","a79","a80","a81","a82","a83","a84","a85","a86","a87","a88","a89","a90","a91","a92","a93","a94","a95","a96","a97","a98","a99","a100","a101","a102","a103","a104","a105","a106","a107","a108","a109","a110","a111","a112","a113","a114","a115","a116","a117","a118","a119","a120","a121","a122","a123","a124","a125","a126","a127","a128","a129","a130","a131","a132","a133","a134","a135","a136","a137","a138","a139","a140","a141","a142","a143","a144","a145","a146","a147","a148","a149","a150","a151","a152","a153","a154","a155","a156","a157","a158","a159","a160","a161","a162","a163","a164","a165","a166","a167","a168","a169","a170","a171","a172","a173","a174","a175","a176","a177","a178","a179","a180","a181","a182","a183","a184","a185","a186","a187","a188","a189","a190","a191","a192","a193","a194","a195","a196","a197","a198","a199","a200","a201","a202","a203","a204","a205","a206","a207","a208","a209","a210","a211","a212","a213","a214","a215","a216","a217","a218","a219","a220","a221","a222","a223","a224","a225","a226","a227","a228","a229","a230","a231","a232","a233","a234","a235","a236","a237","a238","a239","a240","a241","a242","a243","a244","a245","a246","a247","a248","a249","a250","a251","a252","a253","a254","a255");
}
extern "C" void kernel_launch(void* const* d_in, const int* in_sizes, int n_in, void* d_out, int out_size, void* d_ws, size_t ws_size, hipStream_t stream){
  (void)hipFuncSetAttribute((const void*)fused_kernel, hipFuncAttributeMaxDynamicSharedMemorySize, 160*1024);
  fused_kernel<<<256,256,163840,stream>>>((const u16*)d_in[0],(u16*)d_out,(const u16*)d_in[5],(const u16*)d_in[7],(const u16*)d_in[1],(const u16*)d_in[4],(const u16*)d_in[6],(const u16*)d_in[2],(char*)d_ws);
}
```
